```python
import torch

def _softmax(x, axis=-1):
    return torch.softmax(x, dim=axis)
def _gelu(x):
    return torch.nn.functional.gelu(x, approximate="tanh")
def _silu(x):
    return torch.nn.functional.silu(x)
def _sigmoid(x):
    return torch.sigmoid(x)
def _layernorm(x, axis=-1, eps=1e-5):
    m = x.mean(dim=axis, keepdim=True)
    v = x.var(dim=axis, keepdim=True, unbiased=False)
    return (x - m) / torch.sqrt(v + eps)
def _scatter_add(tgt, idx, src, axis=0):
    return tgt.clone().index_add_(axis, idx, src)
def _segment_sum(src, seg, n):
    out = torch.zeros((n,) + src.shape[1:], dtype=src.dtype)
    return out.index_add_(0, seg, src)
def _pad(x, pw):
    flat = tuple(v for pair in reversed(pw) for v in pair)
    return torch.nn.functional.pad(x, flat)

kernel_name = 'interleave_layers__ssm_conv_h3__linear_kernel_hedgehog__prompt'

def setup_inputs():

    _w = (lambda a: (lambda t: t.to(torch.bfloat16) if t.is_floating_point() else t)(torch.as_tensor(a)))
    def _A_setup():
        B, S, D = 1, 2048, 512
        ks = 4
        return {
            "q": _w((lambda *s: torch.randn(*s, dtype=torch.bfloat16) * 0.05)(B, S, D)),
            "k": _w((lambda *s: torch.randn(*s, dtype=torch.bfloat16) * 0.8)(B, S, D)),
            "v": _w((lambda *s: torch.randn(*s, dtype=torch.bfloat16) * 0.8)(B, S, D)),
            "shift_k": _w((lambda *s: torch.randn(*s, dtype=torch.bfloat16) * 0.8)(D, ks) * 0.5),
            "ssm_k": _w((lambda *s: torch.randn(*s, dtype=torch.bfloat16) * 0.8)(D, S) * (1.0 / S ** 0.5)),
            "ssm_D": _w((lambda *s: torch.randn(*s, dtype=torch.bfloat16) * 0.8)(D)),
        }
    _A_in = _A_setup()

    def _B_setup():
        B, S, D, H = 1, 2048, 512, 32
        Dh = D // H
        return {
            "x": _w((lambda *s: torch.randn(*s, dtype=torch.bfloat16) * 0.05)(B, S, D)),
            "wqkv": _w((lambda *s: torch.randn(*s, dtype=torch.bfloat16) * 0.4)(D, 3 * D)),
            "w_phi": _w((lambda *s: torch.randn(*s, dtype=torch.bfloat16) * 0.4)(H, Dh, Dh) * (Dh ** -0.5)),
            "wo": _w((lambda *s: torch.randn(*s, dtype=torch.bfloat16) * 0.2)(D, D)),
        }
    _B_in = _B_setup()

    return {
        "A_q": _A_in["q"],
        "A_k": _A_in["k"],
        "A_v": _A_in["v"],
        "A_shift_k": _A_in["shift_k"],
        "A_ssm_k": _A_in["ssm_k"],
        "A_ssm_D": _A_in["ssm_D"],
        "B_x": _B_in["x"],
        "B_wqkv": _B_in["wqkv"],
        "B_w_phi": _B_in["w_phi"],
        "B_wo": _B_in["wo"],
    }

def reference(A_q, A_k, A_v, A_shift_k, A_ssm_k, A_ssm_D, B_x, B_wqkv, B_w_phi, B_wo):
    def _A_ref(q, k, v, shift_k, ssm_k, ssm_D):
        B, S, D = q.shape
        ks = shift_k.shape[-1]
        k_pad = _pad(k, ((0, 0), (ks - 1, 0), (0, 0)))
        k_shift = torch.zeros_like(k)
        for t in range(ks):
            k_shift = k_shift + k_pad[:, t:t + S, :] * shift_k[:, ks - 1 - t][None, None, :]
        kv = k_shift * v
        L = 2 * S
        kv_pad = _pad(kv, ((0, 0), (0, S), (0, 0)))
        kf_pad = _pad(ssm_k, ((0, 0), (0, S)))
        kv_f = (lambda x, axes=(-2,-1): torch.fft.rfft2(x.float(), dim=axes))(kv_pad[:, None, :, :], axes=(1, 2))
        kf_f = (lambda x, axes=(-2,-1): torch.fft.rfft2(x.float(), dim=axes))(kf_pad[:, None, :], axes=(1, 2))
        kf_f = (lambda x, *d: x.permute(*d))(kf_f, 1, 2, 0)
        y_f = kv_f * kf_f[None, :, :, :]
        y = (lambda x, s, axes=(-2,-1): torch.fft.irfft2(x, s=s, dim=axes).to(torch.bfloat16))(y_f, (1, L), axes=(1, 2))[:, 0, :S, :]
        y = y + kv * ssm_D[None, None, :]
        return q * y

    def _B_ref(x, wqkv, w_phi, wo):
        B, S, D = x.shape
        H = 32; Dh = D // H
        qkv = (lambda x, *d: x.permute(*d))((x @ wqkv).reshape(B, S, 3, H, Dh), 2, 0, 3, 1, 4)
        q, k, v = qkv[0], qkv[1], qkv[2]
        pq = torch.einsum("bhsd,hdf->bhsf", q, w_phi)
        pk = torch.einsum("bhsd,hdf->bhsf", k, w_phi)
        phi_q = torch.concatenate([_softmax(pq, axis=-1), _softmax(-pq, axis=-1)], axis=-1)
        phi_k = torch.concatenate([_softmax(pk, axis=-1), _softmax(-pk, axis=-1)], axis=-1)
        kv = torch.einsum("bhsf,bhsd->bhfd", phi_k, v)
        z  = torch.einsum("bhsf->bhf", phi_k)
        num = torch.einsum("bhqf,bhfd->bhqd", phi_q, kv)
        den = torch.einsum("bhqf,bhf->bhq", phi_q, z)[..., None] + 1e-6
        o = num / den
        return (lambda x, *d: x.permute(*d))(o, 0, 2, 1, 3).reshape(B, S, D) @ wo

    x = A_q
    x = x + _A_ref(_layernorm(x, axis=-1), A_k, A_v, A_shift_k, A_ssm_k, A_ssm_D)
    x = x + _B_ref(_layernorm(x, axis=-1), B_wqkv, B_w_phi, B_wo)
    x = x + _A_ref(_layernorm(x, axis=-1), A_k, A_v, A_shift_k, A_ssm_k, A_ssm_D)
    x = x + _B_ref(_layernorm(x, axis=-1), B_wqkv, B_w_phi, B_wo)
    return x
```

```cpp
#include <hip/hip_runtime.h>
#include <hip/hip_bf16.h>

typedef unsigned short u16;
typedef unsigned u32;
typedef unsigned long long u64;
typedef u32 v4u __attribute__((ext_vector_type(4)));
typedef u32 v2u __attribute__((ext_vector_type(2)));
typedef short v4s __attribute__((ext_vector_type(4)));
typedef float f32x2 __attribute__((ext_vector_type(2)));
typedef float f32x4 __attribute__((ext_vector_type(4)));
typedef float f32x16 __attribute__((ext_vector_type(16)));
typedef __bf16 v8bf __attribute__((ext_vector_type(8)));
typedef __bf16 v2bf __attribute__((ext_vector_type(2)));
#define LAS __attribute__((address_space(3)))
#define GAS __attribute__((address_space(1)))
#define DI __device__ __forceinline__
template <class T, class F> DI T bc(F x) { return __builtin_bit_cast(T, x); }
#define BC(T, x) bc<T>(x)
typedef __amdgpu_buffer_rsrc_t rsrc_t;

constexpr int WS_EPOCH = 0;
constexpr int WS_TMO = 256;
constexpr int FSTR = 128;
constexpr int F_UT = 4096;
constexpr int F_X0 = F_UT + 2048 * FSTR;
constexpr int F_ST = F_X0 + 2048 * FSTR;
constexpr int F_XI0 = F_ST + 256 * FSTR;
constexpr int F_KVP = F_XI0 + 1024 * FSTR;
constexpr int F_PHI = F_KVP + 2048 * FSTR;
constexpr int F_KVR = F_PHI + 2048 * FSTR;
constexpr int F_SP = F_KVR + 2 * 320 * FSTR;
constexpr int F_XI1 = F_SP + 1024 * FSTR;
constexpr int F_END = F_XI1 + 1024 * FSTR;
constexpr int MB = 1 << 20;
static_assert(F_END <= 2 * MB, "flags");
constexpr int B_UT = 2 * MB;
constexpr int B_X0 = 4 * MB;
constexpr int B_XI0 = 6 * MB;
constexpr int B_YI = 8 * MB;
constexpr int B_XI1 = 10 * MB;
constexpr int B_PHI = 12 * MB;
constexpr int B_KVP = 20 * MB;
constexpr int B_ZP = 24 * MB;
constexpr int B_KVR = B_ZP + 256 * 1024;
constexpr int B_ZR = B_KVR + 64 * 1024;
constexpr int B_ST0 = B_ZR + 8 * 1024;
constexpr int B_SP = B_ST0 + 16 * 1024;
constexpr int B_STAMP = 25 * MB;
constexpr int WS_BYTES = 26 * MB;

constexpr int R1A = 0, R1B = 65536, R2 = 98304, LDS_BYTES = 163840;
constexpr int L_KT = 0, L_VT = 9216, L_QT = 17408;
constexpr int L_H = R2, L_U = R2 + 16640, L_P = R2 + 25856;
constexpr int L_ST = R1B, L_EX = R1B + 4096;
constexpr int L_O = R1B;

constexpr u32 SPIN_CAP = 1u << 17;
constexpr int SC1 = 16;
constexpr float LOG2E = 1.4426950408889634f;

struct Params {
  const u16 *q, *k, *v, *shk, *h, *dv, *wqkv, *wphi, *wo;
  u16* out;
  unsigned char* ws;
};

DI float bflo(u32 x) { return BC(float, x << 16); }
DI float bfhi(u32 x) { return BC(float, x & 0xffff0000u); }
DI u32 pk2(float a, float b) { f32x2 p = {a, b}; return BC(u32, __builtin_convertvector(p, v2bf)); }
DI f32x16 mfma32(v4u a, v4u b, f32x16 c) { return __builtin_amdgcn_mfma_f32_32x32x16_bf16(BC(v8bf, a), BC(v8bf, b), c, 0, 0, 0); }
DI f32x4 mfma16(v4u a, v4u b, f32x4 c) { return __builtin_amdgcn_mfma_f32_16x16x32_bf16(BC(v8bf, a), BC(v8bf, b), c, 0, 0, 0); }
DI float dot2(u32 a, u32 b, float c) { return __builtin_amdgcn_fdot2_f32_bf16(BC(v2bf, a), BC(v2bf, b), c, false); }
DI v2u trd(LAS unsigned char* p) { return BC(v2u, __builtin_amdgcn_ds_read_tr16_b64_v4i16((LAS v4s*)p)); }
DI v4u trd2(LAS unsigned char* p, int second) { v2u a = trd(p), b = trd(p + second); return v4u{a.x, a.y, b.x, b.y}; }
DI v4u pack8(const f32x16& x, int s) { return v4u{pk2(x[8 * s], x[8 * s + 1]), pk2(x[8 * s + 2], x[8 * s + 3]), pk2(x[8 * s + 4], x[8 * s + 5]), pk2(x[8 * s + 6], x[8 * s + 7])}; }
DI void swap32(float& a, float& b) { auto r = __builtin_amdgcn_permlane32_swap(BC(u32, a), BC(u32, b), false, false); a = BC(float, r[0]); b = BC(float, r[1]); }
DI void swap16(float& a, float& b) { auto r = __builtin_amdgcn_permlane16_swap(BC(u32, a), BC(u32, b), false, false); a = BC(float, r[0]); b = BC(float, r[1]); }
DI void swap32u(u32& a, u32& b) { auto r = __builtin_amdgcn_permlane32_swap(a, b, false, false); a = r[0]; b = r[1]; }
template <int CTRL> DI float dppf(float x) { return BC(float, __builtin_amdgcn_update_dpp(0, BC(int, x), CTRL, 0xf, 0xf, true)); }
#define BAR() asm volatile("s_waitcnt lgkmcnt(0)\n\ts_barrier" ::: "memory")
#define DRAIN() asm volatile("s_waitcnt vmcnt(0)" ::: "memory")
#define PIN(x) asm volatile("" : "+v"(x))
#define SB() __builtin_amdgcn_sched_barrier(0)

template <bool COH> DI void dma16(const void* base, u32 voff, u32 ldst) {
  u32 keep;
  ldst = __builtin_amdgcn_readfirstlane(ldst);
  if (COH) asm volatile("s_mov_b32 %0, m0\n\ts_mov_b32 m0, %3\n\ts_nop 0\n\tglobal_load_lds_dwordx4 %1, %2 sc1\n\ts_mov_b32 m0, %0" : "=&s"(keep) : "v"(voff), "s"(base), "s"(ldst) : "memory");
  else asm volatile("s_mov_b32 %0, m0\n\ts_mov_b32 m0, %3\n\ts_nop 0\n\tglobal_load_lds_dwordx4 %1, %2\n\ts_mov_b32 m0, %0" : "=&s"(keep) : "v"(voff), "s"(base), "s"(ldst) : "memory");
}
DI v4u ld16(rsrc_t r, int off) { return __builtin_amdgcn_raw_buffer_load_b128(r, off, 0, SC1); }
DI v2u ld8(rsrc_t r, int off) { return __builtin_amdgcn_raw_buffer_load_b64(r, off, 0, SC1); }
DI void st16(rsrc_t r, int off, v4u v) { __builtin_amdgcn_raw_buffer_store_b128(v, r, off, 0, SC1); }
DI void st8(rsrc_t r, int off, v2u v) { __builtin_amdgcn_raw_buffer_store_b64(v, r, off, 0, SC1); }

DI void signal(rsrc_t r, int off, v2u ep, int lane) {
  DRAIN();
  if (lane == 0) st8(r, off, ep);
}
DI void await(rsrc_t r, int off, v2u ep) {
#pragma nounroll
  for (u32 spins = 0;; ++spins) {
    asm volatile("" ::: "memory");
    const v2u f = ld8(r, off);
    if (__all(f.x == ep.x && f.y == ep.y)) break;
    if (spins > SPIN_CAP) { __builtin_amdgcn_raw_buffer_store_b32(1u, r, WS_TMO, 0, SC1); break; }
    __builtin_amdgcn_s_sleep(2);
  }
  asm volatile("" ::: "memory");
}

#define STC(i)
#define ST_PARAM
#define ST_ARG
#define ST_FLUSH()
#define ST(i)

DI void dual_softmax(const f32x16& x, int o, float c, v4u& plus, v4u& minus) {
  float mx = fmaxf(fmaxf(fmaxf(x[o], x[o + 1]), fmaxf(x[o + 2], x[o + 3])), fmaxf(fmaxf(x[o + 4], x[o + 5]), fmaxf(x[o + 6], x[o + 7])));
  float nm = -fminf(fminf(fminf(x[o], x[o + 1]), fminf(x[o + 2], x[o + 3])), fminf(fminf(x[o + 4], x[o + 5]), fminf(x[o + 6], x[o + 7])));
  swap32(mx, nm);
  float t = -fmaxf(mx, nm) * c, t2 = t;
  swap32(t, t2);
  float ep[8], em[8];
#pragma unroll
  for (int i = 0; i < 8; ++i) { ep[i] = __builtin_amdgcn_exp2f(fmaf(x[o + i], c, t)); em[i] = __builtin_amdgcn_exp2f(fmaf(x[o + i], -c, t2)); }
  float sp = ((ep[0] + ep[1]) + (ep[2] + ep[3])) + ((ep[4] + ep[5]) + (ep[6] + ep[7]));
  float sm = ((em[0] + em[1]) + (em[2] + em[3])) + ((em[4] + em[5]) + (em[6] + em[7]));
  swap32(sp, sm);
  float ip = __builtin_amdgcn_rcpf(sp + sm), im = ip;
  swap32(ip, im);
  plus = v4u{pk2(ep[0] * ip, ep[1] * ip), pk2(ep[2] * ip, ep[3] * ip), pk2(ep[4] * ip, ep[5] * ip), pk2(ep[6] * ip, ep[7] * ip)};
  minus = v4u{pk2(em[0] * im, em[1] * im), pk2(em[2] * im, em[3] * im), pk2(em[4] * im, em[5] * im), pk2(em[6] * im, em[7] * im)};
}

struct Ctx {
  LAS unsigned char* L;
  rsrc_t r;
  v2u ep;
  int b, w, lane;
};

template <int LY> DI v4u xfrag(LAS unsigned char* xb, int ks) {
  if (LY == 0) return trd2(xb + ks * 2048, 1024);
  return *(LAS v4u*)(xb + ks * 1024);
}
template <int LY> DI LAS unsigned char* xbase(LAS unsigned char* L, int T, int lane) {
  if (LY == 0) {
    const int G = lane >> 4, q = (lane >> 2) & 3, pp = lane & 3, h = G >> 1;
    return L + R2 + (4 * h + q) * 128 + ((64 * T) ^ ((q >> 1) << 6)) + 32 * (G & 1) + 8 * pp;
  }
  return L + R2 + T * 32768 + lane * 16;
}
template <int LY, int N, bool SKIP> DI void gemm(f32x16& acc, const v4u (&W)[32], LAS unsigned char* xb) {
  constexpr int PD = 4;
  v4u x[PD];
#pragma unroll
  for (int i = 0; i < PD; ++i) x[i] = xfrag<LY>(xb, i);
  SB();
#pragma unroll
  for (int i = 0; i < N; ++i) {
    const v4u cur = x[i % PD];
    acc = mfma32(W[i], cur, acc);
    if (i + PD < N) x[i % PD] = xfrag<LY>(xb, SKIP ? (i + PD) + ((i + PD) & 8) : i + PD);
    SB();
  }
  PIN(acc);
}
DI void row_stats(LAS unsigned char* L, int T, int lane, v4u& mf, float& rs) {
  const f32x4 a = *(LAS f32x4*)(L + L_ST + (T * 32 + (lane & 31)) * 32), c = *(LAS f32x4*)(L + L_ST + (T * 32 + (lane & 31)) * 32 + 16);
  const float m = ((a.x + a.z) + (c.x + c.z)) * (1.f / 512.f);
  const float var = fmaxf(((a.y + a.w) + (c.y + c.w)) * (1.f / 512.f) - m * m, 0.f);
  rs = __builtin_amdgcn_rsqf(var + 1e-5f);
  const u32 hi = pk2(-m, -m) & 0xffffu;
  const u32 hl = pk2(bflo(hi), -m - bflo(hi));
  mf = v4u{hl, hl, 0u, 0u};
}

template <int LY, bool KV>
DI void layer(const Params& p, const Ctx& c, v4u (&W)[32], const v4u csf, const v4u wph ST_PARAM) {
  LAS unsigned char* L = c.L;
  const rsrc_t r = c.r;
  int lane = c.lane;
  PIN(lane);
  const int b = c.b, w = c.w, rb = b & 31, cg = b >> 5, hf = cg & 1, cq = cg >> 1;
  const int G = lane >> 4, q = (lane >> 2) & 3, pp = lane & 3, h = lane >> 5, n = lane & 31;
  constexpr int S0 = LY ? 20 : 10;
  f32x16 zero;
#pragma unroll
  for (int i = 0; i < 16; ++i) zero[i] = 0.f;
  if (KV) {
    __builtin_amdgcn_s_setprio(2);
    f32x16 acc0 = zero, acc1 = zero;
    gemm<LY, 32, false>(acc0, W, xbase<LY>(L, 0, lane));
    BAR();
    gemm<LY, 32, false>(acc1, W, xbase<LY>(L, 1, lane));
    v4u sel;
    {
      const int n16 = n & 15, j = 4 * (n16 >> 3) + (n16 & 3);
      const u32 one = (((n16 >> 2) & 1) == h) ? (0x3f80u << (16 * (j & 1))) : 0u;
      sel = v4u{(j >> 1) == 0 ? one : 0u, (j >> 1) == 1 ? one : 0u, (j >> 1) == 2 ? one : 0u, (j >> 1) == 3 ? one : 0u};
    }
    const v4u z4 = {0u, 0u, 0u, 0u};
    const v4u selp = n < 16 ? sel : z4, selm = n < 16 ? z4 : sel;
    const u32 ones = n < 16 ? 0u : 0x3f803f80u;
    f32x16 kv = zero;
#pragma unroll
    for (int T = 0; T < 2; ++T) {
      v4u mf; float rs;
      row_stats(L, T, lane, mf, rs);
      f32x16 a = mfma32(csf, mf, T ? acc1 : acc0);
      const f32x16 pr = mfma32(wph, pack8(a, 0), zero);
      const v4u vb = v4u{pk2(a[8] * rs, a[9] * rs), pk2(a[10] * rs, a[11] * rs), pk2(a[12] * rs, a[13] * rs), pk2(a[14] * rs, a[15] * rs)};
      const f32x16 vt = mfma32(vb, selp, zero);
      v4u fp, fm;
      dual_softmax(pr, 0, rs * LOG2E, fp, fm);
      f32x16 pt = mfma32(fp, selp, zero);
      pt = mfma32(fm, selm, pt);
      v4u v0 = pack8(vt, 0), v1 = pack8(vt, 1);
      v0 |= ones; v1 |= ones;
      kv = mfma32(pack8(pt, 0), v0, kv);
      kv = mfma32(pack8(pt, 1), v1, kv);
    }
    const int head = 4 * cg + w;
    if (n <= 16) {
      const int o0 = n < 16 ? B_KVP + LY * 2 * MB + ((head * 8 + h) * 32 + rb) * 256 + n * 16 : B_ZP + LY * 131072 + (head * 32 + rb) * 128 + h * 16;
      const int qs = n < 16 ? 2 * 32 * 256 : 32;
#pragma unroll
      for (int qq = 0; qq < 4; ++qq) st16(r, o0 + qq * qs, v4u{BC(u32, kv[4 * qq]), BC(u32, kv[4 * qq + 1]), BC(u32, kv[4 * qq + 2]), BC(u32, kv[4 * qq + 3])});
    }
    ST(S0 + 1);
    signal(r, F_KVP + ((LY * 32 + head) * 32 + rb) * FSTR, c.ep, lane);
    __builtin_amdgcn_s_setprio(0);
    BAR();
    BAR();
  } else {
    const int hp = (w >> 1) & 1, kh = w & 1;
    {
      LAS unsigned char* xb = xbase<LY>(L, hp, lane) + kh * (LY ? 16384 : 32768);
      float s1 = 0.f, s2 = 0.f, t1 = 0.f, t2 = 0.f;
#pragma unroll
      for (int ks = 0; ks < 16; ++ks) {
        const v4u x = xfrag<LY>(xb, ks);
        s1 = dot2(x.x, 0x3f803f80u, s1); s2 = dot2(x.x, x.x, s2); t1 = dot2(x.y, 0x3f803f80u, t1); t2 = dot2(x.y, x.y, t2);
        s1 = dot2(x.z, 0x3f803f80u, s1); s2 = dot2(x.z, x.z, s2); t1 = dot2(x.w, 0x3f803f80u, t1); t2 = dot2(x.w, x.w, t2);
      }
      *(LAS f32x2*)(L + L_ST + (hp * 32 + n) * 32 + (kh * 2 + h) * 8) = f32x2{s1 + t1, s2 + t2};
    }
    BAR();
    if (LY == 0) {
#pragma unroll
      for (int i = 0; i < 16; ++i) {
        const int row = ((w - 4) * 16 + i) * 4 + G;
        dma16<false>(p.wo, (256 + row) * 1024 + cq * 256 + (((lane & 15) ^ (4 * G)) * 16), R1A + ((w - 4) * 16 + i) * 1024);
      }
    }
    f32x16 acck = zero, acco = zero;
    {
      LAS unsigned char* xk = xbase<LY>(L, kh, lane) + kh * (LY ? 8192 : 16384);
      LAS unsigned char* xo = xbase<LY>(L, kh ^ 1, lane) + kh * (LY ? 8192 : 16384);
      gemm<LY, 16, true>(acco, W, xo);
      gemm<LY, 16, true>(acck, W, xk);
    }
    v4u mf; float rs;
    row_stats(L, kh ^ 1, lane, mf, rs);
    acco = mfma32(csf, mf, acco);
    row_stats(L, kh, lane, mf, rs);
    acck = mfma32(csf, mf, acck);
#pragma unroll
    for (int i = 0; i < 4; ++i) *(LAS f32x4*)(L + L_EX + ((w - 4) * 4 + i) * 1024 + lane * 16) = f32x4{acco[4 * i], acco[4 * i + 1], acco[4 * i + 2], acco[4 * i + 3]};
    if (LY == 0) DRAIN();
    BAR();
#pragma unroll
    for (int i = 0; i < 4; ++i) {
      const f32x4 t = *(LAS f32x4*)(L + L_EX + (((w - 4) ^ 1) * 4 + i) * 1024 + lane * 16);
      acck[4 * i] += t.x; acck[4 * i + 1] += t.y; acck[4 * i + 2] += t.z; acck[4 * i + 3] += t.w;
    }
    if (LY == 0) {
      LAS unsigned char* wb = L + R1A + (4 * h + q) * 256 + (((w - 4) ^ q) * 64) + 32 * (G & 1) + 8 * pp;
#pragma unroll
      for (int i = 0; i < 16; ++i) W[16 + i] = trd2(wb + i * 4096, 2048);
    }
    BAR();
    const v4u z4 = {0u, 0u, 0u, 0u};
    f32x16 pr = mfma32(n < 16 ? wph : z4, pack8(acck, 0), zero);
    pr = mfma32(n < 16 ? z4 : wph, pack8(acck, 1), pr);
    const int o0 = B_PHI + LY * 4 * MB + ((rb * 2 + kh) * 32 + 4 * cg + 2 * hp) * 2048 + lane * 16;
    v4u fp, fm;
    dual_softmax(pr, 0, rs * LOG2E, fp, fm);
    st16(r, o0, fp); st16(r, o0 + 1024, fm);
    dual_softmax(pr, 8, rs * LOG2E, fp, fm);
    st16(r, o0 + 2048, fp); st16(r, o0 + 3072, fm);
    ST(S0 + 1);
    signal(r, F_PHI + ((LY * 256 + b) * 4 + (w - 4)) * FSTR, c.ep, lane);
  }
  ST(S0 + 2);
  if (KV && w == 0) {
    const int head = b >> 3;
    await(r, F_KVP + ((LY * 32 + head) * 32 + n) * FSTR, c.ep);
    ST(S0 + 3);
    v4u t[8];
#pragma unroll
    for (int i = 0; i < 8; ++i) t[i] = ld16(r, B_KVP + LY * 2 * MB + b * 8192 + i * 1024 + lane * 16);
    f32x4 s = ((BC(f32x4, t[0]) + BC(f32x4, t[1])) + (BC(f32x4, t[2]) + BC(f32x4, t[3]))) + ((BC(f32x4, t[4]) + BC(f32x4, t[5])) + (BC(f32x4, t[6]) + BC(f32x4, t[7])));
    float s0 = s.x, s1 = s.y, s2 = s.z, s3 = s.w;
    swap32(s0, s2); swap32(s1, s3);
    float a = s0 + s2, bb = s1 + s3, a2 = a, b2 = bb;
    swap16(a, a2); swap16(bb, b2);
    u32 d0 = pk2(a + a2, bb + b2), d1 = d0;
    swap32u(d0, d1);
    if (lane < 16) st8(r, B_KVR + LY * 32768 + b * 128 + lane * 8, v2u{d0, d1});
    signal(r, F_KVR + (LY * 320 + b) * FSTR, c.ep, lane);
  } else if (KV && w == 1) {
    if ((b & 7) == 0) {
      const int head = b >> 3;
      await(r, F_KVP + ((LY * 32 + head) * 32 + n) * FSTR, c.ep);
      v4u t[4];
#pragma unroll
      for (int i = 0; i < 4; ++i) t[i] = ld16(r, B_ZP + LY * 131072 + head * 4096 + i * 1024 + lane * 16);
      f32x4 s = (BC(f32x4, t[0]) + BC(f32x4, t[1])) + (BC(f32x4, t[2]) + BC(f32x4, t[3]));
      float s0 = s.x, s1 = s.y, s2 = s.z, s3 = s.w;
      swap32(s0, s2); swap32(s1, s3);
      float a = s0 + s2, bb = s1 + s3, a2 = a, b2 = bb;
      swap16(a, a2); swap16(bb, b2);
      a += a2; bb += b2;
      a += dppf<0x128>(a); bb += dppf<0x128>(bb);
      u32 d0 = pk2(a, bb), d1 = d0;
      swap32u(d0, d1);
      if (lane < 8) st8(r, B_ZR + LY * 4096 + head * 128 + lane * 8, v2u{d0, d1});
      signal(r, F_KVR + (LY * 320 + 256 + head) * FSTR, c.ep, lane);
    }
  } else if (KV && LY == 0) {
#pragma unroll
    for (int i = 0; i < 32; ++i) {
      const int row = ((w - 2) * 32 + i) * 4 + G;
      dma16<false>(p.wo, row * 1024 + cq * 256 + (((lane & 15) ^ (4 * G)) * 16), R1A + ((w - 2) * 32 + i) * 1024);
    }
#pragma unroll
    for (int i = 0; i < 8; ++i)
      dma16<true>(p.ws, B_YI + rb * 65536 + cq * 16384 + ((w - 2) * 8 + i) * 1024 + lane * 16, R2 + (cq ^ 1) * 16384 + ((w - 2) * 8 + i) * 1024);
  }
  ST(S0 + 4);
  {
    await(r, F_PHI + ((LY * 256 + w * 32 + rb) * 4 + (lane & 1) * 2 + hf) * FSTR, c.ep);
    v4u ph[8];
#pragma unroll
    for (int i = 0; i < 8; ++i) ph[i] = ld16(r, B_PHI + LY * 4 * MB + ((rb * 2 + hf) * 32 + 4 * w) * 2048 + i * 1024 + lane * 16);
    await(r, F_KVR + (LY * 320 + (lane < 32 ? 32 * w + lane : 256 + 4 * w + (lane & 3))) * FSTR, c.ep);
    ST(S0 + 5);
    v2u kr[16];
    const int o0 = n < 16 ? B_KVR + LY * 32768 + (32 * w + h) * 128 + n * 8 : B_ZR + LY * 4096 + 4 * w * 128 + h * 8;
    const int hs = n < 16 ? 1024 : 128, qs = n < 16 ? 256 : 16;
#pragma unroll
    for (int i = 0; i < 4; ++i)
#pragma unroll
      for (int qq = 0; qq < 4; ++qq) kr[4 * i + qq] = ld8(r, o0 + i * hs + qq * qs);
#pragma unroll
    for (int i = 0; i < 4; ++i) {
      f32x16 o = mfma32(v4u{kr[4 * i].x, kr[4 * i].y, kr[4 * i + 1].x, kr[4 * i + 1].y}, ph[2 * i], zero);
      o = mfma32(v4u{kr[4 * i + 2].x, kr[4 * i + 2].y, kr[4 * i + 3].x, kr[4 * i + 3].y}, ph[2 * i + 1], o);
      const float inv = __builtin_amdgcn_rcpf(o[8] + 1e-6f);
      *(LAS v4u*)(L + L_O + (4 * w + i) * 1024 + lane * 16) = v4u{pk2(o[0] * inv, o[1] * inv), pk2(o[2] * inv, o[3] * inv), pk2(o[4] * inv, o[5] * inv), pk2(o[6] * inv, o[7] * inv)};
      if (i == 1) SB();
    }
  }
  DRAIN();
  BAR();
  ST(S0 + 6);
  if (!KV) {
    const int ct = w - 4, ks0 = 8 * cq + 2 * ct;
    LAS unsigned char* xb = xbase<LY>(L, hf, lane) + ks0 * (LY ? 1024 : 2048);
    f32x16 acc;
    {
      const v4u x0 = xfrag<LY>(xb, 0), x1 = xfrag<LY>(xb, 1);
#pragma unroll
      for (int i = 0; i < 4; ++i) { acc[2 * i] = bflo(x0[i]); acc[2 * i + 1] = bfhi(x0[i]); acc[8 + 2 * i] = bflo(x1[i]); acc[9 + 2 * i] = bfhi(x1[i]); }
    }
    LAS unsigned char* ob = L + L_O + lane * 16;
    LAS unsigned char* wb = L + R1A + (4 * h + q) * 256 + ((ct ^ q) * 64) + 32 * (G & 1) + 8 * pp;
#pragma unroll
    for (int i = 0; i < 16; ++i) acc = mfma32(W[16 + i], *(LAS v4u*)(ob + (16 + i) * 1024), acc);
#pragma unroll
    for (int i = 0; i < 16; ++i) acc = mfma32(trd2(wb + i * 4096, 2048), *(LAS v4u*)(ob + i * 1024), acc);
    ST(S0 + 7);
    if (LY == 0) {
      float s1 = 0.f, s2 = 0.f;
#pragma unroll
      for (int i = 0; i < 16; ++i) { s1 += acc[i]; s2 = fmaf(acc[i], acc[i], s2); }
      swap32(s1, s2);
      float t = s1 + s2, t2 = t;
      swap32(t, t2);
      const int hrb = rb * 2 + hf;
      if (lane < 32) st8(r, B_SP + (hrb * 16 + cq * 4 + ct) * 256 + n * 8, v2u{BC(u32, t), BC(u32, t2)});
      signal(r, F_SP + (hrb * 16 + cq * 4 + ct) * FSTR, c.ep, lane);
      LAS unsigned char* yb = xb + ((cq ^ 1) - cq) * 16384;
      const v4u y0 = xfrag<0>(yb, 0), y1 = xfrag<0>(yb, 1);
      await(r, F_SP + (hrb * 16 + (lane & 15)) * FSTR, c.ep);
      ST(S0 + 8);
      v2u sp[8];
#pragma unroll
      for (int i = 0; i < 8; ++i) sp[i] = ld8(r, B_SP + (hrb * 16 + 8 * h + i) * 256 + n * 8);
      s1 = 0.f; s2 = 0.f;
#pragma unroll
      for (int i = 0; i < 8; ++i) { s1 += BC(float, sp[i].x); s2 += BC(float, sp[i].y); }
      swap32(s1, s2);
      t = s1 + s2; t2 = t;
      swap32(t, t2);
      const float m = t * (1.f / 512.f), rs = __builtin_amdgcn_rsqf(fmaxf(t2 * (1.f / 512.f) - m * m, 0.f) + 1e-5f), mr = -m * rs;
#pragma unroll
      for (int i = 0; i < 4; ++i) {
        acc[2 * i] = fmaf(fmaf(acc[2 * i], rs, mr), bflo(y0[i]), acc[2 * i]);
        acc[2 * i + 1] = fmaf(fmaf(acc[2 * i + 1], rs, mr), bfhi(y0[i]), acc[2 * i + 1]);
        acc[8 + 2 * i] = fmaf(fmaf(acc[8 + 2 * i], rs, mr), bflo(y1[i]), acc[8 + 2 * i]);
        acc[9 + 2 * i] = fmaf(fmaf(acc[9 + 2 * i], rs, mr), bfhi(y1[i]), acc[9 + 2 * i]);
      }
      const int o0 = B_XI1 + rb * 65536 + (hf * 32 + ks0) * 1024 + lane * 16;
      st16(r, o0, pack8(acc, 0)); st16(r, o0 + 1024, pack8(acc, 1));
      signal(r, F_XI1 + (b * 4 + ct) * FSTR, c.ep, lane);
    } else {
      v4u a = pack8(acc, 0), d = pack8(acc, 1);
      u32 a0 = a.x, a1 = a.y, a2 = a.z, a3 = a.w, d0 = d.x, d1 = d.y, d2 = d.z, d3 = d.w;
      swap32u(a0, a2); swap32u(a1, a3); swap32u(d0, d2); swap32u(d1, d3);
      GAS unsigned char* o = (GAS unsigned char*)p.out + (rb * 64 + hf * 32 + n) * 1024 + cq * 256 + ct * 64 + h * 16;
      *(GAS v4u*)o = v4u{a0, a1, a2, a3};
      *(GAS v4u*)(o + 32) = v4u{d0, d1, d2, d3};
    }
  }
  if (LY == 0) {
    BAR();
    await(r, F_XI1 + ((w * 32 + rb) * 4 + (lane & 3)) * FSTR, c.ep);
    ST(S0 + 9);
    const int f0 = (w & 1) * 32 + (w >> 1) * 8;
#pragma unroll
    for (int i = 0; i < 8; ++i) dma16<true>(p.ws, B_XI1 + rb * 65536 + (f0 + i) * 1024 + lane * 16, R2 + (f0 + i) * 1024);
    DRAIN();
    BAR();
    ST(20);
  }
}

DI void stage_w(const Params& p, int stage, int cg, int piece0, int lane, int cnt) {
#pragma unroll
  for (int i = 0; i < cnt; ++i) {
    const int piece = piece0 + i, ty = piece >> 5, row = (piece & 31) * 8 + (lane >> 3);
    const int rbit = (lane >> 4) & 1;
    const int g = ty == 0 ? 2 * rbit : (ty == 1 ? rbit : (rbit ^ 2));
    dma16<false>(p.wqkv, (stage * 256 + row) * 3072 + ty * 1024 + cg * 128 + (((lane & 7) ^ (2 * g)) * 16), R1A + piece * 1024);
  }
}

template <bool KV>
DI void program(const Params& p, const Ctx& c, const v4u wph ST_PARAM) {
  LAS unsigned char* L = c.L;
  const rsrc_t r = c.r;
  const v2u ep = c.ep;
  const int b = c.b, w = c.w, lane = c.lane, tid = w * 64 + lane;
  const int x8 = b & 7, hi = b >> 3, rb = b & 31, cg = b >> 5, pair = x8 * 32 + hi;
  const int G = lane >> 4, q = (lane >> 2) & 3, pp = lane & 3, h = lane >> 5, n = lane & 31;
  v4u W[32];
  if (!KV) {
    const int ch = (w >> 1) & 1, rho = w & 1, i16 = lane & 15;
    LAS unsigned char* ab = L + L_H + ch * 8320 + ((i16 & 1) ? 0 : 4160) + 2 * (((i16 + 1) & ~1) + 8 * G) + 256 * rho;
    LAS unsigned char* bb = L + L_U + ch * 4608 + 4064 - 3584 - 32 * i16 + 16 * G + 256 * rho;
    f32x4 acc[8];
#pragma unroll
    for (int i = 0; i < 8; ++i) acc[i] = f32x4{0.f, 0.f, 0.f, 0.f};
#pragma unroll
    for (int rr = 0; rr < 4; ++rr) {
      v4u A[8], B[8];
#pragma unroll
      for (int i = 0; i < 8; ++i) {
        LAS u32* ap = (LAS u32*)(ab + 512 * i + 64 * rr);
        A[i] = v4u{ap[0], ap[1], ap[2], ap[3]};
        B[i] = *(LAS v4u*)(bb + 512 * (7 - i) + 64 * rr);
      }
#pragma unroll
      for (int cc = 0; cc < 8; ++cc)
#pragma unroll
        for (int nn = 0; nn <= cc; ++nn) acc[cc] = mfma16(A[nn], B[cc - nn], acc[cc]);
    }
#pragma unroll
    for (int cc = 0; cc < 8; ++cc) *(LAS f32x4*)(L + L_P + ((w - 4) * 8 + cc) * 1024 + G * 256 + ((i16 ^ (8 * (G >> 1))) * 16)) = acc[cc];
    ST(6);
    BAR();
    LAS unsigned char* wb = L + R1A + (w & 1) * 16384 + (4 * h + q) * 128 + ((((w & 2) + (G & 1)) ^ (2 * (q >> 1))) * 32) + 8 * pp;
#pragma unroll
    for (int i = 0; i < 8; ++i) W[i] = trd2(wb + i * 2048, 1024);
  } else {
    stage_w(p, 0, cg, w * 24, lane, 24);
    await(r, F_X0 + (((8 * w + (lane & 7)) * 8 + x8) * 8 + (hi >> 2)) * FSTR, ep);
    await(r, F_ST + (64 * w + lane) * FSTR, ep);
    const v4u xa = ld16(r, B_X0 + pair * 8192 + tid * 32), xb = ld16(r, B_X0 + pair * 8192 + tid * 32 + 16);
    v4u st[4];
#pragma unroll
    for (int i = 0; i < 4; ++i) st[i] = ld16(r, B_ST0 + tid * 64 + i * 16);
    const u32 dd = *(const GAS u32*)(p.dv + 2 * pair);
    float x0[2][8], n0[2][8], y[2][8];
#pragma unroll
    for (int cc = 0; cc < 2; ++cc) {
      const v4u ur = *(LAS v4u*)(L + L_U + cc * 4608 + 4080 - 16 * tid);
      const float dc = cc ? bfhi(dd) : bflo(dd);
      const u32 xs[4] = {cc ? xa.z : xa.x, cc ? xa.w : xa.y, cc ? xb.z : xb.x, cc ? xb.w : xb.y};
#pragma unroll
      for (int i = 0; i < 4; ++i) {
        x0[cc][2 * i] = bflo(xs[i]); x0[cc][2 * i + 1] = bfhi(xs[i]);
        n0[cc][2 * i] = fmaf(x0[cc][2 * i], BC(float, st[i].x), BC(float, st[i].y));
        n0[cc][2 * i + 1] = fmaf(x0[cc][2 * i + 1], BC(float, st[i].z), BC(float, st[i].w));
        y[cc][2 * i] = bfhi(ur[3 - i]) * dc; y[cc][2 * i + 1] = bflo(ur[3 - i]) * dc;
      }
    }
    DRAIN();
    ST(6);
    BAR();
    const int o = tid & 1, j = (tid >> 1) & 15, ct = tid >> 5;
    const int pos = (tid >> 3) * 65536 + (2 * pair) * 128 + (((tid & 7) ^ (4 * (pair & 1))) * 16);
#pragma unroll
    for (int cc = 0; cc < 2; ++cc) {
#pragma unroll
      for (int rho = 0; rho < 2; ++rho)
#pragma unroll
        for (int e = 0; e < 2; ++e) {
          const f32x4 t = *(LAS f32x4*)(L + L_P + ((cc * 2 + rho) * 8 + ct) * 1024 + (2 * o + e) * 256 + ((j ^ (8 * o)) * 16));
          y[cc][4 * e] += t.x; y[cc][4 * e + 1] += t.y; y[cc][4 * e + 2] += t.z; y[cc][4 * e + 3] += t.w;
        }
      float x1[8];
#pragma unroll
      for (int i = 0; i < 8; ++i) x1[i] = fmaf(n0[cc][i], y[cc][i], x0[cc][i]);
      st16(r, B_XI0 + pos + cc * 128, v4u{pk2(x1[0], x1[1]), pk2(x1[2], x1[3]), pk2(x1[4], x1[5]), pk2(x1[6], x1[7])});
      st16(r, B_YI + pos + cc * 128, v4u{pk2(y[cc][0], y[cc][1]), pk2(y[cc][2], y[cc][3]), pk2(y[cc][4], y[cc][5]), pk2(y[cc][6], y[cc][7])});
    }
    LAS unsigned char* wb = L + R1A + (1 + (G & 1)) * 32768 + (4 * h + q) * 128 + ((w ^ (q >> 1) ^ (2 * (G & 1))) * 32) + 8 * pp;
#pragma unroll
    for (int i = 0; i < 16; ++i) W[i] = trd2(wb + i * 2048, 1024);
    signal(r, F_XI0 + (pair * 4 + w) * FSTR, ep, lane);
  }
  ST(7);
  BAR();
  stage_w(p, 1, cg, w * 12, lane, 12);
  await(r, F_XI0 + ((32 * w + n) * 4 + (rb >> 3)) * FSTR, ep);
  ST(8);
#pragma unroll
  for (int i = 0; i < 8; ++i) dma16<true>(p.ws, B_XI0 + rb * 65536 + (w * 8 + i) * 1024 + lane * 16, R2 + (w * 8 + i) * 1024);
  BAR();
  float cs = 0.f, cs2 = 0.f;
  if (KV) {
    LAS unsigned char* wb = L + R1A + (1 + (G & 1)) * 32768 + (4 * h + q) * 128 + ((w ^ (q >> 1) ^ (2 * (G & 1))) * 32) + 8 * pp;
#pragma unroll
    for (int i = 0; i < 16; ++i) W[16 + i] = trd2(wb + i * 2048, 1024);
#pragma unroll
    for (int i = 16; i < 32; ++i) { cs = dot2(W[i].x, 0x3f803f80u, cs); cs2 = dot2(W[i].y, 0x3f803f80u, cs2); cs = dot2(W[i].z, 0x3f803f80u, cs); cs2 = dot2(W[i].w, 0x3f803f80u, cs2); }
  } else {
    LAS unsigned char* wb = L + R1A + (w & 1) * 16384 + (4 * h + q) * 128 + ((((w & 2) + (G & 1)) ^ (2 * (q >> 1))) * 32) + 8 * pp;
#pragma unroll
    for (int i = 0; i < 8; ++i) W[8 + i] = trd2(wb + i * 2048, 1024);
  }
#pragma unroll
  for (int i = 0; i < 16; ++i) { cs = dot2(W[i].x, 0x3f803f80u, cs); cs2 = dot2(W[i].y, 0x3f803f80u, cs2); cs = dot2(W[i].z, 0x3f803f80u, cs); cs2 = dot2(W[i].w, 0x3f803f80u, cs2); }
  cs += cs2;
  v4u csf;
  {
    const u32 hh = pk2(cs, cs);
    const float lo = cs - bflo(hh);
    csf = v4u{hh, pk2(lo, lo), 0u, 0u};
    PIN(csf);
  }
  ST(9);
  DRAIN();
  BAR();
  ST(10);
  layer<0, KV>(p, c, W, csf, wph ST_ARG);
  if (KV && b == 0 && tid == 0) st8(r, WS_EPOCH, ep);
  layer<1, KV>(p, c, W, csf, wph ST_ARG);
}

__global__ void __launch_bounds__(512, 2) fused_kernel(Params p) {
  extern __shared__ __attribute__((aligned(16))) unsigned char smem[];
  LAS unsigned char* L = (LAS unsigned char*)smem;
  const int tid = threadIdx.x, lane = tid & 63, w = __builtin_amdgcn_readfirstlane(tid >> 6), b = blockIdx.x;
  const rsrc_t r = __builtin_amdgcn_make_buffer_rsrc((void*)p.ws, 0, WS_BYTES, 0x00020000);
  const int x8 = b & 7, hi = b >> 3, rb = b & 31, cg = b >> 5;
  const int G = lane >> 4, q = (lane >> 2) & 3, pp = lane & 3, h = lane >> 5, n = lane & 31;
  ST(0);
  STC(32);
  {
    const int lr = 8 * w + (lane >> 3);
    dma16<false>(p.k, (64 * hi + lr) * 1024 + x8 * 128 + (((lane & 7) ^ (((lr + 4) >> 2) & 7)) * 16), L_KT + 512 + w * 1024);
    dma16<false>(p.v, (64 * hi + lr) * 1024 + x8 * 128 + (((lane & 7) ^ ((lr >> 2) & 7)) * 16), L_VT + w * 1024);
    if (w == 0 && lane < 32) {
      if (hi > 0) dma16<false>(p.k, (64 * hi - 4 + (lane >> 3)) * 1024 + x8 * 128 + (lane & 7) * 16, L_KT);
      else *(LAS v4u*)(L + L_KT + lane * 16) = v4u{0u, 0u, 0u, 0u};
    }
  }
  const u64 e0 = *(const GAS u64*)(p.ws + WS_EPOCH) + 1ull;
  const v2u ep = {(u32)e0, (u32)(e0 >> 32)};
  const int pl = 4 * w + G;
  const v4u sk = *(const GAS v4u*)(p.shk + (x8 * 64 + 2 * pl) * 4);
  DRAIN();
  BAR();
  ST(1);
  const int pair = x8 * 32 + hi;
  const v4u hv = *(const GAS v4u*)(p.h + pair * 4096 + tid * 8);
  const u32 hn = *(const GAS u32*)(p.h + pair * 4096 + min(tid * 8 + 8, 4088));
  {
    const int lr = 8 * w + (lane >> 3);
    dma16<false>(p.q, (64 * hi + lr) * 1024 + x8 * 128 + (((lane & 7) ^ ((lr >> 2) & 7)) * 16), L_QT + w * 1024);
  }
  {
    const int rq = lane & 15;
    LAS unsigned char* ka = L + L_KT + (4 * rq) * 128 + ((w ^ (rq & 7)) * 16) + G * 4;
    LAS unsigned char* kb = L + L_KT + (4 * rq + 4) * 128 + ((w ^ ((rq + 1) & 7)) * 16) + G * 4;
    LAS unsigned char* va = L + L_VT + (4 * rq) * 128 + ((w ^ (rq & 7)) * 16) + G * 4;
    u32 kk[7], vv[4];
#pragma unroll
    for (int i = 0; i < 3; ++i) kk[i] = *(LAS u32*)(ka + (i + 1) * 128);
#pragma unroll
    for (int i = 0; i < 4; ++i) { kk[3 + i] = *(LAS u32*)(kb + i * 128); vv[i] = *(LAS u32*)(va + i * 128); }
    const f32x2 w0 = {bflo(sk.x), bflo(sk.z)}, w1 = {bfhi(sk.x), bfhi(sk.z)}, w2 = {bflo(sk.y), bflo(sk.w)}, w3 = {bfhi(sk.y), bfhi(sk.w)};
    f32x2 kf[7], u[4];
#pragma unroll
    for (int i = 0; i < 7; ++i) kf[i] = f32x2{bflo(kk[i]), bfhi(kk[i])};
#pragma unroll
    for (int i = 0; i < 4; ++i) u[i] = (kf[i] * w3 + kf[i + 1] * w2 + kf[i + 2] * w1 + kf[i + 3] * w0) * f32x2{bflo(vv[i]), bfhi(vv[i])};
    st16(r, B_UT + (x8 * 32 + pl) * 8192 + (16 * hi + rq) * 16, v4u{pk2(u[3].x, u[2].x), pk2(u[1].x, u[0].x), pk2(u[3].y, u[2].y), pk2(u[1].y, u[0].y)});
    signal(r, F_UT + (b * 8 + w) * FSTR, ep, lane);
  }
  ST(2);
  u32 pf;
  {
    const int line = hi * 512 + tid;
    pf = line < 12288 ? *(const GAS u32*)((const GAS unsigned char*)p.wqkv + line * 128) : *(const GAS u32*)((const GAS unsigned char*)p.wo + (line - 12288) * 128);
  }
  {
    LAS unsigned char* hb = L + L_H + (tid >> 8) * 8320 + (tid & 255) * 16 + 32;
    *(LAS v4u*)hb = hv;
    *(LAS v4u*)(hb + 4160) = v4u{__builtin_amdgcn_alignbit(hv.y, hv.x, 16), __builtin_amdgcn_alignbit(hv.z, hv.y, 16), __builtin_amdgcn_alignbit(hv.w, hv.z, 16), __builtin_amdgcn_alignbit(hn, hv.w, 16)};
    if ((tid & 255) == 0) {
      const v4u z4 = {0u, 0u, 0u, 0u};
      *(LAS v4u*)(hb - 32) = z4; *(LAS v4u*)(hb - 16) = z4; *(LAS v4u*)(hb + 4160 - 32) = z4;
      *(LAS v4u*)(hb + 4160 - 16) = v4u{0u, 0u, 0u, hv.x << 16};
    }
    if (w == 1 && n < 30) *(LAS v4u*)(L + L_U + h * 4608 + 4096 + n * 16) = v4u{0u, 0u, 0u, 0u};
  }
  BAR();
  {
    const int rq = lane & 15;
    LAS unsigned char* qa = L + L_QT + (4 * rq) * 128 + ((w ^ (rq & 7)) * 16) + G * 4;
    const u32 a0 = *(LAS u32*)qa, a1 = *(LAS u32*)(qa + 128), a2 = *(LAS u32*)(qa + 256), a3 = *(LAS u32*)(qa + 384);
    st16(r, B_X0 + (x8 * 32 + pl) * 8192 + (16 * hi + rq) * 16,
         v4u{__builtin_amdgcn_perm(a1, a0, 0x05040100u), __builtin_amdgcn_perm(a3, a2, 0x05040100u), __builtin_amdgcn_perm(a1, a0, 0x07060302u), __builtin_amdgcn_perm(a3, a2, 0x07060302u)});
    signal(r, F_X0 + (b * 8 + w) * FSTR, ep, lane);
  }
  asm volatile("" ::"v"(pf));
  if (w == 7) {
    const GAS v4u* qr = (const GAS v4u*)(p.q + (8 * b + (lane >> 3)) * 512) + (lane & 7);
    v4u x[8];
#pragma unroll
    for (int i = 0; i < 8; ++i) x[i] = qr[8 * i];
    float s1 = 0.f, s2 = 0.f;
#pragma unroll
    for (int i = 0; i < 8; ++i)
#pragma unroll
      for (int j = 0; j < 4; ++j) { s1 = dot2(x[i][j], 0x3f803f80u, s1); s2 = dot2(x[i][j], x[i][j], s2); }
    s1 += dppf<0xb1>(s1); s2 += dppf<0xb1>(s2);
    s1 += dppf<0x4e>(s1); s2 += dppf<0x4e>(s2);
    s1 += dppf<0x141>(s1); s2 += dppf<0x141>(s2);
    const float m = s1 * (1.f / 512.f), rs = __builtin_amdgcn_rsqf(fmaxf(s2 * (1.f / 512.f) - m * m, 0.f) + 1e-5f);
    if ((lane & 7) == 0) st8(r, B_ST0 + (8 * b + (lane >> 3)) * 8, v2u{BC(u32, rs), BC(u32, -m * rs)});
    signal(r, F_ST + b * FSTR, ep, lane);
  }
  v4u wph;
  {
    const int head = w < 4 ? 4 * cg + w : 4 * cg + (w & 2) + (n >> 4);
    const GAS u16* wp = (const GAS u16*)p.wphi + head * 256 + (4 * h) * 16 + (n & 15);
    u32 e[8];
#pragma unroll
    for (int j = 0; j < 8; ++j) e[j] = wp[(8 * (j >> 2) + (j & 3)) * 16];
    wph = v4u{e[0] | (e[1] << 16), e[2] | (e[3] << 16), e[4] | (e[5] << 16), e[6] | (e[7] << 16)};
  }
  ST(3);
  await(r, F_UT + (((4 * w + (lane & 3)) * 8 + x8) * 8 + (hi >> 2)) * FSTR, ep);
  ST(4);
  {
    const v4u u = ld16(r, B_UT + pair * 8192 + tid * 16);
    *(LAS v2u*)(L + L_U + 4088 - 8 * tid) = v2u{u.x, u.y};
    *(LAS v2u*)(L + L_U + 4608 + 4088 - 8 * tid) = v2u{u.z, u.w};
  }
  BAR();
  ST(5);
  const Ctx c = {L, r, ep, b, w, lane};
  if (w < 4) program<true>(p, c, wph ST_ARG);
  else program<false>(p, c, wph ST_ARG);
  ST(30);
  STC(33);
  ST_FLUSH();
}

extern "C" void kernel_launch(void* const* d_in, const int* in_sizes, int n_in, void* d_out, int out_size, void* d_ws, size_t ws_size,
                              hipStream_t stream) {
  Params p;
  p.q = (const u16*)d_in[0]; p.k = (const u16*)d_in[1]; p.v = (const u16*)d_in[2]; p.shk = (const u16*)d_in[3];
  p.h = (const u16*)d_in[4]; p.dv = (const u16*)d_in[5]; p.wqkv = (const u16*)d_in[7]; p.wphi = (const u16*)d_in[8];
  p.wo = (const u16*)d_in[9]; p.out = (u16*)d_out; p.ws = (unsigned char*)d_ws;
  (void)hipFuncSetAttribute((const void*)fused_kernel, hipFuncAttributeMaxDynamicSharedMemorySize, LDS_BYTES);
  hipLaunchKernelGGL(fused_kernel, dim3(256), dim3(512), LDS_BYTES, stream, p);
}
```

```cpp
#include <hip/hip_runtime.h>
#include <hip/hip_bf16.h>

typedef unsigned short u16;
typedef unsigned u32;
typedef unsigned long long u64;
typedef u32 v4u __attribute__((ext_vector_type(4)));
typedef u32 v2u __attribute__((ext_vector_type(2)));
typedef short v4s __attribute__((ext_vector_type(4)));
typedef float f32x2 __attribute__((ext_vector_type(2)));
typedef float f32x4 __attribute__((ext_vector_type(4)));
typedef float f32x16 __attribute__((ext_vector_type(16)));
typedef __bf16 v8bf __attribute__((ext_vector_type(8)));
typedef __bf16 v2bf __attribute__((ext_vector_type(2)));
#define LAS __attribute__((address_space(3)))
#define GAS __attribute__((address_space(1)))
#define DI __device__ __forceinline__
template <class T, class F> DI T bc(F x) { return __builtin_bit_cast(T, x); }
#define BC(T, x) bc<T>(x)
typedef __amdgpu_buffer_rsrc_t rsrc_t;

constexpr int MB = 1 << 20;
constexpr int WS_EPOCH = 0;
constexpr int B_KVR = 4096;
constexpr int B_ZR = B_KVR + 64 * 1024;
constexpr int B_FL = 128 * 1024;
constexpr int B_ZP = 256 * 1024;
constexpr int B_SP = 512 * 1024;
constexpr int B_UT = 1 * MB;
constexpr int B_YI = 3 * MB;
constexpr int B_XI = 5 * MB;
constexpr int B_OI = 9 * MB;
constexpr int B_KVP = 13 * MB;
constexpr int B_STAMP = 25 * MB;
constexpr int WS_BYTES = 26 * MB;

constexpr int R1A = 0, R1B = 65536, R2 = 98304, LDS_BYTES = 163840;
constexpr int L_H = R2, L_U = R2 + 16640, L_P = R2 + 25856, L_S0 = R2 + 59136, L_C0 = R2 + 65536 - 64;
constexpr int L_KT = L_P, L_VT = L_P + 8704;
constexpr int L_QR = R1B;
constexpr int L_ST = R1B, L_EX = R1B + 4096, L_C1 = R1B + 20480, L_Y = R1B + 24576;

constexpr u32 SPIN_CAP = 1u << 14;
constexpr int SC1 = 16;
constexpr float LOG2E = 1.4426950408889634f;
constexpr u32 ONES = 0x3f803f80u;

#define D_UT 0
#define D_YI 0
#define D_KVP 16
#define D_OI 10
#define D_KVR 12
#define D_SP 8
#define D_XI 10
#define D_RETRY 2

struct Params {
  const u16 *q, *k, *v, *shk, *h, *dv, *wqkv, *wphi, *wo;
  u16* out;
  unsigned char* ws;
};

DI float bflo(u32 x) { return BC(float, x << 16); }
DI float bfhi(u32 x) { return BC(float, x & 0xffff0000u); }
DI u32 pk2(float a, float b) { f32x2 p = {a, b}; return BC(u32, __builtin_convertvector(p, v2bf)); }
DI float op(float r) { asm("" : "+v"(r)); return r; }
DI float mx3(float a, float b, float c) { return __builtin_fmaxf(__builtin_fmaxf(a, b), c); }
DI f32x16 mfma32(v4u a, v4u b, f32x16 c) { return __builtin_amdgcn_mfma_f32_32x32x16_bf16(BC(v8bf, a), BC(v8bf, b), c, 0, 0, 0); }
DI f32x4 mfma16(v4u a, v4u b, f32x4 c) { return __builtin_amdgcn_mfma_f32_16x16x32_bf16(BC(v8bf, a), BC(v8bf, b), c, 0, 0, 0); }
DI float dot2(u32 a, u32 b, float c) { return __builtin_amdgcn_fdot2_f32_bf16(BC(v2bf, a), BC(v2bf, b), c, false); }
DI v2u trd(LAS unsigned char* p) { return BC(v2u, __builtin_amdgcn_ds_read_tr16_b64_v4i16((LAS v4s*)p)); }
DI v4u trd2(LAS unsigned char* p, int second) { v2u a = trd(p), b = trd(p + second); return v4u{a.x, a.y, b.x, b.y}; }
DI v4u pack8(const f32x16& x, int s) { return v4u{pk2(x[8 * s], x[8 * s + 1]), pk2(x[8 * s + 2], x[8 * s + 3]), pk2(x[8 * s + 4], x[8 * s + 5]), pk2(x[8 * s + 6], x[8 * s + 7])}; }
DI void swap32(float& a, float& b) { auto r = __builtin_amdgcn_permlane32_swap(BC(u32, a), BC(u32, b), false, false); a = BC(float, r[0]); b = BC(float, r[1]); }
DI void swap16(float& a, float& b) { auto r = __builtin_amdgcn_permlane16_swap(BC(u32, a), BC(u32, b), false, false); a = BC(float, r[0]); b = BC(float, r[1]); }
DI void swap32u(u32& a, u32& b) { auto r = __builtin_amdgcn_permlane32_swap(a, b, false, false); a = r[0]; b = r[1]; }
template <int CTRL> DI u32 dppu(u32 x) { return (u32)__builtin_amdgcn_update_dpp(0, (int)x, CTRL, 0xf, 0xf, true); }
template <int CTRL> DI float dppf(float x) { return BC(float, dppu<CTRL>(BC(u32, x))); }
DI bool all_lanes(bool ok) { return __builtin_amdgcn_ballot_w64(!ok) == 0ull; }
#define BAR() asm volatile("s_waitcnt lgkmcnt(0)\n\ts_barrier" ::: "memory")
#define DRAIN() asm volatile("s_waitcnt vmcnt(0)" ::: "memory")
#define VMW(n) asm volatile("s_waitcnt vmcnt(%0)" ::"n"(n) : "memory")
#define PIN(x) asm volatile("" : "+v"(x))
#define SB() __builtin_amdgcn_sched_barrier(0)
#define FENCE() asm volatile("" ::: "memory")
template <int I> struct IC { static constexpr int v = I; };
template <int I, int N, class F> DI void sfor(F&& f) { if constexpr (I < N) { f(IC<I>{}); sfor<I + 1, N>(f); } }
template <int N> DI void nap() { if (N > 0) __builtin_amdgcn_s_sleep(N); }

template <bool COH> DI void dma16(const void* base, u32 voff, u32 ldst) {
  u32 keep;
  ldst = __builtin_amdgcn_readfirstlane(ldst);
  if (COH) asm volatile("s_mov_b32 %0, m0\n\ts_mov_b32 m0, %3\n\ts_nop 0\n\tglobal_load_lds_dwordx4 %1, %2 sc1\n\ts_mov_b32 m0, %0" : "=&s"(keep) : "v"(voff), "s"(base), "s"(ldst) : "memory");
  else asm volatile("s_mov_b32 %0, m0\n\ts_mov_b32 m0, %3\n\ts_nop 0\n\tglobal_load_lds_dwordx4 %1, %2\n\ts_mov_b32 m0, %0" : "=&s"(keep) : "v"(voff), "s"(base), "s"(ldst) : "memory");
}
DI v4u ld16(rsrc_t r, int off) { return __builtin_amdgcn_raw_buffer_load_b128(r, off, 0, SC1); }
DI void st16(rsrc_t r, int off, v4u v) { __builtin_amdgcn_raw_buffer_store_b128(v, r, off, 0, SC1); }

DI void arrive(LAS unsigned char* ctr, int lane) {
  asm volatile("s_waitcnt lgkmcnt(0)" ::: "memory");
  if (lane == 0) __hip_atomic_fetch_add((LAS u32*)ctr, 1u, __ATOMIC_RELAXED, __HIP_MEMORY_SCOPE_WORKGROUP);
}
template <int NAP = 0> DI void until(LAS unsigned char* ctr, u32 target) {
#pragma nounroll
  while (__builtin_amdgcn_readfirstlane(*(volatile LAS u32*)ctr) < target) nap<NAP>();
  FENCE();
}

template <int N, int MODE> DI u32 fetch(rsrc_t r, v4u (&v)[N], int off, int stride, u32 tag) {
  u32 spins = 0;
#pragma nounroll
  for (;; ++spins) {
    FENCE();
#pragma unroll
    for (int i = 0; i < N; ++i) v[i] = ld16(r, off + i * stride);
    u32 a = 0u;
#pragma unroll
    for (int i = 0; i < N; ++i) {
      if (MODE == 0) a |= (v[i].y ^ tag) | (v[i].w ^ tag);
      if (MODE == 1) a = max(max(a, max(v[i].x, v[i].y)), max(v[i].z, v[i].w));
      if (MODE == 2) { v[i] ^= tag; a |= (v[i].x | v[i].y) | (v[i].z | v[i].w); }
    }
    const bool ok = MODE == 0 ? a == 0u : MODE == 1 ? a != ~0u : (a & 0xffu) == 0u;
    if (all_lanes(ok) || spins > SPIN_CAP) break;
    nap<D_RETRY>();
  }
  FENCE();
  return spins;
}

#define STV(i, v) (void)(v)
#define STC(i)
#define ST_PARAM
#define ST_ARG
#define ST_FLUSH()
#define ST(i)

DI void dual_softmax(const f32x16& x, int o, float c, v4u& plus, v4u& minus) {
  float mx = mx3(mx3(x[o], x[o + 1], x[o + 2]), mx3(x[o + 3], x[o + 4], x[o + 5]), mx3(x[o + 6], x[o + 7], x[o]));
  float nm = mx3(mx3(-x[o], -x[o + 1], -x[o + 2]), mx3(-x[o + 3], -x[o + 4], -x[o + 5]), mx3(-x[o + 6], -x[o + 7], -x[o]));
  swap32(mx, nm);
  float t = mx3(mx, nm, nm) * -c, t2 = t;
  swap32(t, t2);
  float ep[8], em[8];
#pragma unroll
  for (int i = 0; i < 8; ++i) { ep[i] = op(__builtin_fmaf(x[o + i], c, t)); em[i] = op(__builtin_fmaf(x[o + i], -c, t2)); }
#pragma unroll
  for (int i = 0; i < 8; ++i) { ep[i] = __builtin_amdgcn_exp2f(ep[i]); em[i] = __builtin_amdgcn_exp2f(em[i]); }
  float sp = op(op(op(ep[0] + ep[1]) + op(ep[2] + ep[3])) + op(op(ep[4] + ep[5]) + op(ep[6] + ep[7])));
  float sm = op(op(op(em[0] + em[1]) + op(em[2] + em[3])) + op(op(em[4] + em[5]) + op(em[6] + em[7])));
  swap32(sp, sm);
  float ip = __builtin_amdgcn_rcpf(sp + sm), im = ip;
  swap32(ip, im);
  plus = v4u{pk2(op(ep[0] * ip), op(ep[1] * ip)), pk2(op(ep[2] * ip), op(ep[3] * ip)), pk2(op(ep[4] * ip), op(ep[5] * ip)), pk2(op(ep[6] * ip), op(ep[7] * ip))};
  minus = v4u{pk2(op(em[0] * im), op(em[1] * im)), pk2(op(em[2] * im), op(em[3] * im)), pk2(op(em[4] * im), op(em[5] * im)), pk2(op(em[6] * im), op(em[7] * im))};
}

struct Ctx {
  LAS unsigned char* L;
  rsrc_t r;
  u32 tag;
  int b, w, lane;
};

template <int N> DI void gemm(f32x16& acc, const v4u (&W)[32], LAS unsigned char* xb) {
  constexpr int PD = 4;
  v4u x[PD];
#pragma unroll
  for (int i = 0; i < PD; ++i) x[i] = *(LAS v4u*)(xb + i * 1024);
  SB();
#pragma unroll
  for (int i = 0; i < N; ++i) {
    const v4u cur = x[i % PD];
    acc = mfma32(W[i], cur, acc);
    if (i + PD < N) x[i % PD] = *(LAS v4u*)(xb + (i + PD) * 1024);
    SB();
  }
  PIN(acc);
}
DI void row_moments(LAS unsigned char* st, int lane, float& m, float& rs) {
  LAS f32x4* s = (LAS f32x4*)(st + (lane & 31) * 16);
  const f32x4 a = s[0], b = s[32], c = s[64], d = s[96];
  m = op(op(op(a.x + a.z) + op(b.x + b.z)) + op(op(c.x + c.z) + op(d.x + d.z))) * (-1.f / 512.f);
  const float e2 = op(op(op(a.y + a.w) + op(b.y + b.w)) + op(op(c.y + c.w) + op(d.y + d.w))) * (1.f / 512.f);
  rs = __builtin_amdgcn_rsqf(mx3(__builtin_fmaf(-m, m, op(e2)), 0.f, 0.f) + 1e-5f);
}
DI void row_stats(LAS unsigned char* L, int T, int lane, u32& mf, float& rs) {
  float m;
  row_moments(L + L_ST + T * 2048, lane, m, rs);
  const float hi = bflo(pk2(m, m));
  mf = pk2(hi, op(m - hi));
}
DI void acc_stats(v4u x, float& s1, float& s2) {
  s1 = dot2(x.x, ONES, s1); s2 = dot2(x.x, x.x, s2); s1 = dot2(x.y, ONES, s1); s2 = dot2(x.y, x.y, s2);
  s1 = dot2(x.z, ONES, s1); s2 = dot2(x.z, x.z, s2); s1 = dot2(x.w, ONES, s1); s2 = dot2(x.w, x.w, s2);
}
template <int N0, int N1> DI v2u colsum(const v4u (&W)[32]) {
  float cs = 0.f, cs2 = 0.f;
#pragma unroll
  for (int i = N0; i < N1; ++i) { cs = dot2(W[i].x, ONES, cs); cs2 = dot2(W[i].y, ONES, cs2); cs = dot2(W[i].z, ONES, cs); cs2 = dot2(W[i].w, ONES, cs2); }
  cs += cs2;
  const u32 hh = pk2(cs, cs);
  const float lo = cs - bflo(hh);
  v2u o = {hh, pk2(lo, lo)};
  PIN(o);
  return o;
}
DI u32 load_image(const Params& p, LAS unsigned char* L, int LY, int rb, int w, int lane) {
  const int f0 = (w & 1) * 32 + (w >> 1) * 8;
  float a1, a2, b1, b2;
  u32 spins = 0;
  nap<D_XI>();
#pragma nounroll
  for (;; ++spins) {
#pragma unroll
    for (int i = 0; i < 8; ++i) dma16<true>(p.ws, B_XI + LY * 2 * MB + rb * 65536 + (f0 + i) * 1024 + lane * 16, R2 + (f0 + i) * 1024);
    a1 = a2 = b1 = b2 = 0.f;
    LAS v4u* x0 = (LAS v4u*)(L + R2 + f0 * 1024 + lane * 16);
    VMW(6); acc_stats(x0[0], a1, a2); acc_stats(x0[64], b1, b2);
    VMW(4); acc_stats(x0[128], a1, a2); acc_stats(x0[192], b1, b2);
    VMW(2); acc_stats(x0[256], a1, a2); acc_stats(x0[320], b1, b2);
    VMW(0); acc_stats(x0[384], a1, a2); acc_stats(x0[448], b1, b2);
    const float chk = a2 + b2;
    if (all_lanes(chk == chk) || spins > SPIN_CAP) break;
    nap<D_RETRY>();
  }
  *(LAS f32x2*)(L + L_ST + (w & 1) * 2048 + (w >> 1) * 512 + (lane & 31) * 16 + (lane >> 5) * 8) = f32x2{a1 + b1, a2 + b2};
  return spins;
}
DI void stage64(const void* base, int off, int stride, int w4, int lane) {
#pragma unroll
  for (int i = 0; i < 16; ++i) dma16<false>(base, off + (i * 16 + (lane >> 2)) * stride + (lane & 3) * 16, R1A + w4 * 16384 + i * 1024);
}
DI void gate_publish(f32x16& acc, float rs, float mr, v4u y0, v4u y1, rsrc_t r, int off) {
#pragma unroll
  for (int i = 0; i < 4; ++i) {
    acc[2 * i] = __builtin_fmaf(__builtin_fmaf(acc[2 * i], rs, mr), bflo(y0[i]), acc[2 * i]);
    acc[2 * i + 1] = __builtin_fmaf(__builtin_fmaf(acc[2 * i + 1], rs, mr), bfhi(y0[i]), acc[2 * i + 1]);
    acc[8 + 2 * i] = __builtin_fmaf(__builtin_fmaf(acc[8 + 2 * i], rs, mr), bflo(y1[i]), acc[8 + 2 * i]);
    acc[9 + 2 * i] = __builtin_fmaf(__builtin_fmaf(acc[9 + 2 * i], rs, mr), bfhi(y1[i]), acc[9 + 2 * i]);
  }
  st16(r, off, pack8(acc, 0)); st16(r, off + 1024, pack8(acc, 1));
}

struct Post {
  v4u selp, selm, kb, vb, fp, fm, v0, v1;
  u32 ones;
  f32x16 pr, vt, pt;
  float vs[8], ep[8], em[8], mx, nm, t, t2, cc, s0, s1, s2, s3;
  DI void init(int n, int h) {
    const int n16 = n & 15, j = 4 * (n16 >> 3) + (n16 & 3);
    const u32 one = (((n16 >> 2) & 1) == h) ? (0x3f80u << (16 * (j & 1))) : 0u;
    const u32 op_ = n < 16 ? one : 0u, om = n < 16 ? 0u : one;
    selp = v4u{(j >> 1) == 0 ? op_ : 0u, (j >> 1) == 1 ? op_ : 0u, (j >> 1) == 2 ? op_ : 0u, (j >> 1) == 3 ? op_ : 0u};
    selm = v4u{(j >> 1) == 0 ? om : 0u, (j >> 1) == 1 ? om : 0u, (j >> 1) == 2 ? om : 0u, (j >> 1) == 3 ? om : 0u};
    ones = n < 16 ? 0u : ONES;
    PIN(selp); PIN(selm); PIN(ones);
  }
  template <int S> DI void step(const f32x16& a, float rs, v4u wph, f32x16& kv, bool first) {
    f32x16 zero;
#pragma unroll
    for (int i = 0; i < 16; ++i) zero[i] = 0.f;
    if constexpr (S == 0) { kb = pack8(a, 0); cc = rs * LOG2E; }
    if constexpr (S == 1) { pr = mfma32(wph, kb, zero); _Pragma("unroll") for (int i = 0; i < 8; ++i) vs[i] = op(a[8 + i] * rs); }
    if constexpr (S == 2) vb = v4u{pk2(vs[0], vs[1]), pk2(vs[2], vs[3]), pk2(vs[4], vs[5]), pk2(vs[6], vs[7])};
    if constexpr (S == 3) {
      vt = mfma32(vb, selp, zero);
      mx = mx3(mx3(pr[0], pr[1], pr[2]), mx3(pr[3], pr[4], pr[5]), mx3(pr[6], pr[7], pr[0]));
      nm = mx3(mx3(-pr[0], -pr[1], -pr[2]), mx3(-pr[3], -pr[4], -pr[5]), mx3(-pr[6], -pr[7], -pr[0]));
    }
    if constexpr (S == 4) { swap32(mx, nm); t = mx3(mx, nm, nm) * -cc; t2 = t; }
    if constexpr (S == 5) { swap32(t, t2); v0.x = pk2(vt[0], vt[1]) | ones; }
    if constexpr (S == 6) _Pragma("unroll") for (int i = 0; i < 5; ++i) ep[i] = op(__builtin_fmaf(pr[i], cc, t));
    if constexpr (S == 7) { _Pragma("unroll") for (int i = 5; i < 8; ++i) ep[i] = op(__builtin_fmaf(pr[i], cc, t)); _Pragma("unroll") for (int i = 0; i < 2; ++i) em[i] = op(__builtin_fmaf(pr[i], -cc, t2)); }
    if constexpr (S == 8) _Pragma("unroll") for (int i = 2; i < 7; ++i) em[i] = op(__builtin_fmaf(pr[i], -cc, t2));
    if constexpr (S == 9) { em[7] = op(__builtin_fmaf(pr[7], -cc, t2)); ep[0] = __builtin_amdgcn_exp2f(ep[0]); ep[1] = __builtin_amdgcn_exp2f(ep[1]); }
    if constexpr (S == 10) { ep[2] = __builtin_amdgcn_exp2f(ep[2]); ep[3] = __builtin_amdgcn_exp2f(ep[3]); s0 = op(ep[0] + ep[1]); }
    if constexpr (S == 11) { ep[4] = __builtin_amdgcn_exp2f(ep[4]); ep[5] = __builtin_amdgcn_exp2f(ep[5]); s1 = op(ep[2] + ep[3]); }
    if constexpr (S == 12) { ep[6] = __builtin_amdgcn_exp2f(ep[6]); ep[7] = __builtin_amdgcn_exp2f(ep[7]); s0 = op(s0 + s1); }
    if constexpr (S == 13) { em[0] = __builtin_amdgcn_exp2f(em[0]); em[1] = __builtin_amdgcn_exp2f(em[1]); s1 = op(ep[4] + ep[5]); }
    if constexpr (S == 14) { em[2] = __builtin_amdgcn_exp2f(em[2]); em[3] = __builtin_amdgcn_exp2f(em[3]); s2 = op(ep[6] + ep[7]); }
    if constexpr (S == 15) { em[4] = __builtin_amdgcn_exp2f(em[4]); em[5] = __builtin_amdgcn_exp2f(em[5]); s1 = op(s1 + s2); }
    if constexpr (S == 16) { em[6] = __builtin_amdgcn_exp2f(em[6]); em[7] = __builtin_amdgcn_exp2f(em[7]); s0 = op(s0 + s1); }
    if constexpr (S == 17) { s1 = op(em[0] + em[1]); s2 = op(em[2] + em[3]); s3 = op(em[4] + em[5]); t = op(em[6] + em[7]); s1 = op(s1 + s2); }
    if constexpr (S == 18) { s3 = op(s3 + t); v0.y = pk2(vt[2], vt[3]) | ones; s1 = op(s1 + s3); }
    if constexpr (S == 19) { swap32(s0, s1); t = __builtin_amdgcn_rcpf(s0 + s1); }
    if constexpr (S == 20) { t2 = t; v0.z = pk2(vt[4], vt[5]) | ones; swap32(t, t2); }
    if constexpr (S == 21) _Pragma("unroll") for (int i = 0; i < 5; ++i) ep[i] = op(ep[i] * t);
    if constexpr (S == 22) { _Pragma("unroll") for (int i = 5; i < 8; ++i) ep[i] = op(ep[i] * t); fp.x = pk2(ep[0], ep[1]); fp.y = pk2(ep[2], ep[3]); }
    if constexpr (S == 23) { fp.z = pk2(ep[4], ep[5]); fp.w = pk2(ep[6], ep[7]); _Pragma("unroll") for (int i = 0; i < 3; ++i) em[i] = op(em[i] * t2); }
    if constexpr (S == 24) { pt = mfma32(fp, selp, zero); _Pragma("unroll") for (int i = 3; i < 8; ++i) em[i] = op(em[i] * t2); fm = v4u{pk2(em[0], em[1]), pk2(em[2], em[3]), pk2(em[4], em[5]), pk2(em[6], em[7])}; }
    if constexpr (S == 25) { pt = mfma32(fm, selm, pt); v0.w = pk2(vt[6], vt[7]) | ones; v1.x = pk2(vt[8], vt[9]) | ones; v1.y = pk2(vt[10], vt[11]) | ones; v1.z = pk2(vt[12], vt[13]) | ones; v1.w = pk2(vt[14], vt[15]) | ones; }
    if constexpr (S == 27) fp = pack8(pt, 0);
    if constexpr (S == 28) { kv = mfma32(fp, v0, first ? zero : kv); fm = pack8(pt, 1); }
    if constexpr (S == 29) kv = mfma32(fm, v1, kv);
  }
};

template <int LY, bool KV>
DI void layer(const Params& p, const Ctx& c, v4u (&W)[32], v2u& csf, const v4u wph ST_PARAM) {
  LAS unsigned char* L = c.L;
  const rsrc_t r = c.r;
  int lane = c.lane;
  PIN(lane);
  const int b = c.b, w = c.w, rb = b & 31, cg = b >> 5, hf = cg & 1, cq = cg >> 1;
  const int G = lane >> 4, q = (lane >> 2) & 3, pp = lane & 3, h = lane >> 5, n = lane & 31;
  constexpr int S0 = LY ? 20 : 10, S1 = LY ? 50 : 40;
  constexpr u32 C4 = LY ? 8 : 4;
  const u32 tagl = c.tag + LY;
  const v4u z4 = {0u, 0u, 0u, 0u};
  f32x16 zero;
#pragma unroll
  for (int i = 0; i < 16; ++i) zero[i] = 0.f;
  if (KV) {
    const v4u cs4 = {csf.x, csf.y, 0u, 0u};
    f32x16 acc0 = zero, acc1, kv;
    Post ps;
    u32 mf0, mf1; float rs0, rs1;
    {
      constexpr int PD = 4;
      LAS unsigned char* xb = L + R2 + lane * 16;
      v4u x[PD];
#pragma unroll
      for (int i = 0; i < PD; ++i) x[i] = *(LAS v4u*)(xb + i * 1024);
      SB();
      sfor<0, 64>([&](auto I) __attribute__((always_inline)) {
        constexpr int i = decltype(I)::v;
        const v4u cur = x[i % PD];
        if constexpr (i < 32) acc0 = mfma32(W[i & 31], cur, acc0); else acc1 = mfma32(W[i & 31], cur, acc1);
        if constexpr (i + PD < 64) x[i % PD] = *(LAS v4u*)(xb + (i + PD) * 1024);
        if constexpr (i == 1) { row_stats(L, 0, lane, mf0, rs0); PIN(mf0); PIN(rs0); }
        if constexpr (i == 8) { row_stats(L, 1, lane, mf1, rs1); PIN(mf1); PIN(rs1); }
        if constexpr (i == 15) ps.init(n, h);
        if constexpr (i == 24) acc0 = mfma32(cs4, v4u{mf0, mf0, 0u, 0u}, acc0);
        if constexpr (i == 28) acc1 = mfma32(cs4, v4u{mf1, mf1, 0u, 0u}, zero);
        if constexpr (i >= 33) ps.template step<i - 33>(acc0, rs0, wph, kv, true);
        if constexpr (i < 1 || i >= 22 || i == 7 || i == 14) SB();
      });
    }
    ST(S1 + 2);
    arrive(L + L_C1 + 16, lane);
    sfor<0, 31>([&](auto I) __attribute__((always_inline)) { ps.template step<decltype(I)::v>(acc1, rs1, wph, kv, false); });
    if (n <= 16) {
      const int head = 4 * cg + w;
      const int o0 = n < 16 ? B_KVP + ((head * 8 + h * 2 + (n >> 3)) * 32 + rb) * 256 + (n & 7) * 16 : B_ZP + (head * 32 + rb) * 128 + h * 32;
      const u32 tb = tagl & 0xffu;
#pragma unroll
      for (int qq = 0; qq < 4; ++qq)
        st16(r, o0 + (n < 16 ? (qq >> 1) * 32768 + (qq & 1) * 128 : (qq >> 1) * 64 + (qq & 1) * 16),
             v4u{(BC(u32, kv[4 * qq]) & ~0xffu) | tb, (BC(u32, kv[4 * qq + 1]) & ~0xffu) | tb, (BC(u32, kv[4 * qq + 2]) & ~0xffu) | tb, (BC(u32, kv[4 * qq + 3]) & ~0xffu) | tb});
    }
    ST(S0 + 1);
    if (w == 0) {
      nap<D_KVP>();
      v4u t[8];
      STV(58 + LY, (fetch<8, 2>(r, t, B_KVP + b * 8192 + lane * 16, 1024, tagl & 0xffu)));
      ST(S0 + 3);
      float s[4];
#pragma unroll
      for (int i = 0; i < 4; ++i)
        s[i] = op(op(op(BC(float, t[0][i]) + BC(float, t[1][i])) + op(BC(float, t[2][i]) + BC(float, t[3][i]))) + op(op(BC(float, t[4][i]) + BC(float, t[5][i])) + op(BC(float, t[6][i]) + BC(float, t[7][i]))));
      swap32(s[0], s[2]); swap32(s[1], s[3]);
      float a = op(s[0] + s[2]), bb = op(s[1] + s[3]), a2 = a, b2 = bb;
      swap16(a, a2); swap16(bb, b2);
      u32 d0 = pk2(op(a + a2), op(bb + b2)), d1 = d0;
      swap32u(d0, d1);
      const u32 e0 = dppu<0x128>(d0), e1 = dppu<0x128>(d1);
      if (lane < 8) st16(r, B_KVR + LY * 32768 + b * 128 + lane * 16, v4u{d0, d1, e0, e1});
      arrive(L + L_C1 + 8, lane);
    } else if (w == 1 && (b & 7) == 0) {
      nap<D_KVP>();
      v4u t[4];
      fetch<4, 2>(r, t, B_ZP + (b >> 3) * 4096 + lane * 16, 1024, tagl & 0xffu);
      float s[4];
#pragma unroll
      for (int i = 0; i < 4; ++i) s[i] = op(op(BC(float, t[0][i]) + BC(float, t[1][i])) + op(BC(float, t[2][i]) + BC(float, t[3][i])));
      swap32(s[0], s[2]); swap32(s[1], s[3]);
      float a = op(s[0] + s[2]), bb = op(s[1] + s[3]), a2 = a, b2 = bb;
      swap16(a, a2); swap16(bb, b2);
      a = op(a + a2); bb = op(bb + b2);
      a = op(a + dppf<0x128>(a)); bb = op(bb + dppf<0x128>(bb));
      u32 d0 = pk2(a, bb), d1 = d0;
      swap32u(d0, d1);
      const u32 e0 = dppu<0xb1>(d0), e1 = dppu<0xb1>(d1);
      if (lane < 8 && !(lane & 1)) st16(r, B_ZR + LY * 4096 + (b >> 3) * 128 + lane * 8, v4u{d0, d1, e0, e1});
    } else if (w == 2) {
      until<2>(L + L_C1 + 8, LY + 1);
      v4u fl[1];
      STV(60 + LY, (fetch<1, 0>(r, fl, B_FL + (2 + LY) * 32768 + ((lane & 7) * 32 + rb) * 128, 0, tagl)));
    }
  } else {
    const int hp = (w >> 1) & 1, kh = w & 1;
    LAS unsigned char* wb = L + R1A + (w - 4) * 16384 + (4 * h + q) * 64 + 32 * (G & 1) + 8 * pp;
    const int oo = B_OI + ((rb * 2 + kh) * 32 + 4 * cg + 2 * hp) * 1024 + lane * 16;
    {
      const v4u s = {~0u, ~0u, ~0u, ~0u};
      if (LY == 0) {
        stage64(p.wqkv, kh * 256 * 3072 + cg * 128 + hp * 64, 3072, w - 4, lane);
        DRAIN();
        ST(S1 + 0);
#pragma unroll
        for (int i = 0; i < 16; ++i) W[i] = trd2(wb + i * 1024, 512);
        asm volatile("s_waitcnt lgkmcnt(0)" ::: "memory");
        const int o0 = B_XI + 2 * MB + rb * 65536 + (hf * 32 + 8 * cq + 2 * (w - 4)) * 1024 + lane * 16;
        st16(r, o0, s); st16(r, o0 + 1024, s);
      }
      st16(r, oo, s); st16(r, oo + 1024, s);
      if (LY == 0) {
        stage64(p.wo, 256 * 1024 + cq * 256 + (w - 4) * 64, 1024, w - 4, lane);
        VMW(16);
      } else DRAIN();
      arrive(L + L_C1 + 4, lane);
      if (w == 7) {
        until(L + L_C1 + 4, C4);
        if (lane == 0) st16(r, B_FL + (2 + LY) * 32768 + b * 128, v4u{tagl, tagl, tagl, tagl});
      }
    }
    ST(S1 + 1);
    until<1>(L + L_C1 + 16, C4);
    ST(S1 + 2);
    if (LY == 0) csf = colsum<0, 16>(W);
    const v4u cs4 = {csf.x, csf.y, 0u, 0u};
    u32 mf; float rs;
    row_stats(L, kh ^ 1, lane, mf, rs);
    f32x16 acco = mfma32(cs4, v4u{mf, mf, 0u, 0u}, zero);
    row_stats(L, kh, lane, mf, rs);
    f32x16 acck = mfma32(cs4, v4u{mf, mf, 0u, 0u}, zero);
    gemm<16>(acco, W, L + R2 + (kh ^ 1) * 32768 + kh * 16384 + lane * 16);
    if (LY == 0) {
      DRAIN();
#pragma unroll
      for (int i = 0; i < 16; ++i) W[16 + i] = trd2(wb + i * 1024, 512);
      asm volatile("s_waitcnt lgkmcnt(0)" ::: "memory");
      stage64(p.wo, cq * 256 + (w - 4) * 64, 1024, w - 4, lane);
    }
    gemm<16>(acck, W, L + R2 + kh * 32768 + kh * 16384 + lane * 16);
    ST(S1 + 3);
#pragma unroll
    for (int i = 0; i < 4; ++i) *(LAS f32x4*)(L + L_EX + ((w - 4) * 4 + i) * 1024 + lane * 16) = f32x4{acco[4 * i], acco[4 * i + 1], acco[4 * i + 2], acco[4 * i + 3]};
    arrive(L + L_C1, lane);
    until(L + L_C1, C4);
    ST(S1 + 5);
#pragma unroll
    for (int i = 0; i < 4; ++i) {
      const f32x4 t = *(LAS f32x4*)(L + L_EX + (((w - 4) ^ 1) * 4 + i) * 1024 + lane * 16);
      acck[4 * i] += t.x; acck[4 * i + 1] += t.y; acck[4 * i + 2] += t.z; acck[4 * i + 3] += t.w;
    }
    f32x16 pr = mfma32(n < 16 ? wph : z4, pack8(acck, 0), zero);
    pr = mfma32(n < 16 ? z4 : wph, pack8(acck, 1), pr);
    v4u ph[4], kr[4];
    dual_softmax(pr, 0, rs * LOG2E, ph[0], ph[1]);
    dual_softmax(pr, 8, rs * LOG2E, ph[2], ph[3]);
    PIN(ph[0]); PIN(ph[1]); PIN(ph[2]); PIN(ph[3]);
    ST(S0 + 1);
    until<1>(L + L_C1 + 8, LY + 1);
    ST(S0 + 3);
    nap<D_KVR>();
    {
      const int hd = 4 * cg + 2 * hp;
      const int o0 = n < 16 ? B_KVR + LY * 32768 + hd * 1024 + h * 256 + n * 16 : B_ZR + LY * 4096 + hd * 128 + h * 16;
      const int hs = n < 16 ? 1024 : 128, ss = n < 16 ? 512 : 32;
      u32 spins = 0;
#pragma nounroll
      for (;; ++spins) {
        FENCE();
        u32 a = 0u;
#pragma unroll
        for (int i = 0; i < 4; ++i) kr[i] = ld16(r, o0 + (i >> 1) * hs + (i & 1) * ss);
#pragma unroll
        for (int i = 0; i < 4; ++i) a = max(max(a, max(kr[i].x, kr[i].y)), max(kr[i].z, kr[i].w));
        if (all_lanes(a != ~0u) || spins > SPIN_CAP) break;
        nap<D_RETRY>();
      }
      FENCE();
      STV(62 + LY, spins);
    }
    ST(S0 + 5);
#pragma unroll
    for (int i = 0; i < 2; ++i) {
      f32x16 o = mfma32(kr[2 * i], ph[2 * i], zero);
      o = mfma32(kr[2 * i + 1], ph[2 * i + 1], o);
      const float inv = __builtin_amdgcn_rcpf(o[8] + 1e-6f);
      st16(r, oo + i * 1024, v4u{pk2(o[0] * inv, o[1] * inv), pk2(o[2] * inv, o[3] * inv), pk2(o[4] * inv, o[5] * inv), pk2(o[6] * inv, o[7] * inv)});
    }
  }
  ST(S0 + 2);
  BAR();
  ST(S0 + 4);
  LAS unsigned char* ob = L + R2 + (hf ^ 1) * 32768 + lane * 16;
  {
    nap<D_OI>();
    u32 spins = 0;
#pragma nounroll
    for (;; ++spins) {
#pragma unroll
      for (int i = 0; i < 4; ++i) dma16<true>(p.ws, B_OI + ((rb * 2 + hf) * 32 + 4 * w + i) * 1024 + lane * 16, R2 + (hf ^ 1) * 32768 + (4 * w + i) * 1024);
      DRAIN();
      u32 a = 0u;
#pragma unroll
      for (int i = 0; i < 4; ++i) { const v4u t = *(LAS v4u*)(ob + (4 * w + i) * 1024); a = max(max(a, max(t.x, t.y)), max(t.z, t.w)); }
      if (all_lanes(a != ~0u) || spins > SPIN_CAP) break;
      nap<D_RETRY>();
    }
    STV(48 + LY, spins);
  }
  BAR();
  ST(S0 + 6);
  if (!KV) {
    const int ct = w - 4, ks0 = 8 * cq + 2 * ct;
    LAS unsigned char* xb = L + R2 + (hf * 32 + ks0) * 1024 + lane * 16;
    LAS unsigned char* wb = L + R1A + ct * 16384 + (4 * h + q) * 64 + 32 * (G & 1) + 8 * pp;
    f32x16 acc = zero;
    v4u x0, x1;
    {
      constexpr int PD = 4;
      v4u o[PD], a[PD];
#pragma unroll
      for (int i = 0; i < PD; ++i) o[i] = *(LAS v4u*)(ob + (16 + i) * 1024);
      SB();
#pragma unroll
      for (int i = 0; i < 32; ++i) {
        acc = mfma32(i < 16 ? W[16 + (i & 15)] : a[i % PD], o[i % PD], acc);
        if (i + PD < 32) o[i % PD] = *(LAS v4u*)(ob + ((16 + i + PD) & 31) * 1024);
        if (i + PD >= 16 && i + PD < 32) a[i % PD] = trd2(wb + (i + PD - 16) * 1024, 512);
        if (i == 2) x0 = *(LAS v4u*)xb;
        if (i == 4) x1 = *(LAS v4u*)(xb + 1024);
        SB();
      }
    }
#pragma unroll
    for (int i = 0; i < 4; ++i) { acc[2 * i] += bflo(x0[i]); acc[2 * i + 1] += bfhi(x0[i]); acc[8 + 2 * i] += bflo(x1[i]); acc[9 + 2 * i] += bfhi(x1[i]); }
    ST(S0 + 7);
    if (LY == 0) {
      f32x2 s1 = {0.f, 0.f}, s2 = {0.f, 0.f};
#pragma unroll
      for (int i = 0; i < 8; ++i) { const f32x2 t = {acc[2 * i], acc[2 * i + 1]}; s1 += t; s2 += t * t; }
      float u1 = s1.x + s1.y, u2 = s2.x + s2.y;
      swap32(u1, u2);
      float t = u1 + u2, t2 = t;
      swap32(t, t2);
      const int hrb = rb * 2 + hf;
      if (lane < 32) st16(r, B_SP + (hrb * 16 + cq * 4 + ct) * 512 + n * 16, v4u{BC(u32, t), c.tag, BC(u32, t2), c.tag});
      const v4u y0 = *(LAS v4u*)(L + L_Y + ct * 2048 + lane * 16), y1 = *(LAS v4u*)(L + L_Y + ct * 2048 + 1024 + lane * 16);
      nap<D_SP>();
      v4u sp[8];
      STV(37, (fetch<8, 0>(r, sp, B_SP + (hrb * 16 + 8 * h) * 512 + n * 16, 512, c.tag)));
      ST(S0 + 8);
      u1 = 0.f; u2 = 0.f;
#pragma unroll
      for (int i = 0; i < 8; ++i) { u1 += BC(float, sp[i].x); u2 += BC(float, sp[i].z); }
      swap32(u1, u2);
      t = u1 + u2; t2 = t;
      swap32(t, t2);
      const float m = t * (1.f / 512.f), rs = __builtin_amdgcn_rsqf(mx3(t2 * (1.f / 512.f) - m * m, 0.f, 0.f) + 1e-5f);
      gate_publish(acc, rs, -m * rs, y0, y1, r, B_XI + 2 * MB + rb * 65536 + (hf * 32 + ks0) * 1024 + lane * 16);
    } else {
      v4u a = pack8(acc, 0), d = pack8(acc, 1);
      u32 a0 = a.x, a1 = a.y, a2 = a.z, a3 = a.w, d0 = d.x, d1 = d.y, d2 = d.z, d3 = d.w;
      swap32u(a0, a2); swap32u(a1, a3); swap32u(d0, d2); swap32u(d1, d3);
      GAS unsigned char* o = (GAS unsigned char*)p.out + (rb * 64 + hf * 32 + n) * 1024 + cq * 256 + ct * 64 + h * 16;
      *(GAS v4u*)o = v4u{a0, a1, a2, a3};
      *(GAS v4u*)(o + 32) = v4u{d0, d1, d2, d3};
    }
  }
  if (LY == 0) {
    BAR();
    ST(S0 + 9);
    STV(38, load_image(p, L, 1, rb, w, lane));
    BAR();
    ST(20);
  }
}

DI void stage_kv(const Params& p, int stage, int cg, int w, int lane) {
  const int ty = w >> 1, g = ((lane >> 4) & 1) ^ (2 * ty);
#pragma unroll
  for (int i = 0; i < 16; ++i)
    dma16<false>(p.wqkv, (stage * 256 + ((w & 1) * 16 + i) * 8 + (lane >> 3)) * 3072 + (1 + ty) * 1024 + cg * 128 + (((lane & 7) ^ (2 * g)) * 16), R1A + (w * 16 + i) * 1024);
}

template <bool KV>
DI void program(const Params& p, const Ctx& c ST_PARAM) {
  LAS unsigned char* L = c.L;
  const rsrc_t r = c.r;
  const u32 tag = c.tag;
  const int b = c.b, w = c.w, lane = c.lane, t8 = (w & 3) * 64 + lane;
  const int x8 = b & 7, hi = b >> 3, rb = b & 31, cg = b >> 5, hf = cg & 1, cq = cg >> 1, pair = x8 * 32 + hi;
  const int G = lane >> 4, q = (lane >> 2) & 3, pp = lane & 3, h = lane >> 5, n = lane & 31;
  const v4u z4 = {0u, 0u, 0u, 0u}, s4 = {~0u, ~0u, ~0u, ~0u};
  v4u hv[2]; u32 hn[2];
  if (!KV) {
#pragma unroll
    for (int cc = 0; cc < 2; ++cc) {
      hv[cc] = *(const GAS v4u*)(p.h + pair * 4096 + cc * 2048 + t8 * 8);
      hn[cc] = *(const GAS u32*)(p.h + pair * 4096 + cc * 2048 + min(t8 * 8 + 8, 2040));
    }
  }
  {
    const int rq = lane & 15, pl = 4 * w + G;
    const v4u sk = *(LAS v4u*)(L + L_C0 - 512 + pl * 16);
    LAS unsigned char* ka = L + L_KT + (4 * rq) * 128 + ((w ^ (rq & 7)) * 16) + G * 4;
    LAS unsigned char* kb = L + L_KT + (4 * rq + 4) * 128 + ((w ^ ((rq + 1) & 7)) * 16) + G * 4;
    LAS unsigned char* va = L + L_VT + (4 * rq) * 128 + ((w ^ (rq & 7)) * 16) + G * 4;
    u32 kk[7], vv[4];
#pragma unroll
    for (int i = 0; i < 3; ++i) kk[i] = *(LAS u32*)(ka + (i + 1) * 128);
#pragma unroll
    for (int i = 0; i < 4; ++i) { kk[3 + i] = *(LAS u32*)(kb + i * 128); vv[i] = *(LAS u32*)(va + i * 128); }
    const f32x2 w0 = {bflo(sk.x), bflo(sk.z)}, w1 = {bfhi(sk.x), bfhi(sk.z)}, w2 = {bflo(sk.y), bflo(sk.w)}, w3 = {bfhi(sk.y), bfhi(sk.w)};
    f32x2 kf[7], u[4];
#pragma unroll
    for (int i = 0; i < 7; ++i) kf[i] = f32x2{bflo(kk[i]), bfhi(kk[i])};
#pragma unroll
    for (int i = 0; i < 4; ++i) u[i] = (kf[i] * w3 + kf[i + 1] * w2 + kf[i + 2] * w1 + kf[i + 3] * w0) * f32x2{bflo(vv[i]), bfhi(vv[i])};
    st16(r, B_UT + (x8 * 32 + pl) * 8192 + (16 * hi + rq) * 16, v4u{pk2(u[3].x, u[2].x), pk2(u[1].x, u[0].x), pk2(u[3].y, u[2].y), pk2(u[1].y, u[0].y)});
  }
  ST(2);
  v4u W[32];
  v2u csf = {0u, 0u};
  v4u wph;
#define LOAD_WPH() { \
    const int head = KV ? 4 * cg + w : 4 * cg + (w & 2) + (n >> 4); \
    const GAS u16* wp = (const GAS u16*)p.wphi + head * 256 + (4 * h) * 16 + (n & 15); \
    u32 e[8]; \
    _Pragma("unroll") for (int j = 0; j < 8; ++j) e[j] = wp[(8 * (j >> 2) + (j & 3)) * 16]; \
    wph = v4u{e[0] | (e[1] << 16), e[2] | (e[3] << 16), e[4] | (e[5] << 16), e[6] | (e[7] << 16)}; \
    PIN(wph); }
  const int ypos = B_YI + ((w & 3) * 16 + (lane >> 3)) * 32768 + pair * 128 + (lane & 7) * 16;
  if (!KV) {
#pragma unroll
    for (int cc = 0; cc < 2; ++cc) {
      LAS unsigned char* hb = L + L_H + cc * 8320 + t8 * 16 + 32;
      const v4u a = hv[cc];
      *(LAS v4u*)hb = a;
      *(LAS v4u*)(hb + 4160) = v4u{__builtin_amdgcn_alignbit(a.y, a.x, 16), __builtin_amdgcn_alignbit(a.z, a.y, 16), __builtin_amdgcn_alignbit(a.w, a.z, 16), __builtin_amdgcn_alignbit(hn[cc], a.w, 16)};
      if (t8 == 0) {
        *(LAS v4u*)(hb - 32) = z4; *(LAS v4u*)(hb - 16) = z4; *(LAS v4u*)(hb + 4160 - 32) = z4;
        *(LAS v4u*)(hb + 4160 - 16) = v4u{0u, 0u, 0u, a.x << 16};
      }
    }
    if (w == 5 && n < 30) *(LAS v4u*)(L + L_U + h * 4608 + 4096 + n * 16) = z4;
    const u32 dd = *(const GAS u32*)(p.dv + 2 * pair);
    arrive(L + L_C0, lane);
    ST(3);
    const int ch = (w >> 1) & 1, rho = w & 1, i16 = lane & 15;
    v4u fl[1], u[2];
    STV(34, (fetch<1, 0>(r, fl, B_FL + ((8 * (w - 4) + (lane & 7)) * 8 + x8) * 128, 0, tag)));
    nap<D_UT>();
#pragma unroll
    for (int i = 0; i < 2; ++i) u[i] = ld16(r, B_UT + pair * 8192 + (w - 4) * 2048 + lane * 16 + i * 1024);
    until(L + L_C0, 4);
    {
      LAS unsigned char* ab = L + L_H + ch * 8320 + ((i16 & 1) ? 0 : 4160) + 2 * (((i16 + 1) & ~1) + 8 * G) + 256 * rho;
#pragma unroll
      for (int i = 0; i < 32; ++i) {
        LAS u32* ap = (LAS u32*)(ab + 512 * (i & 7) + 64 * (i >> 3));
        W[i] = v4u{ap[0], ap[1], ap[2], ap[3]};
      }
    }
    if (!all_lanes(max(max(max(u[0].x, u[0].y), max(u[0].z, u[0].w)), max(max(u[1].x, u[1].y), max(u[1].z, u[1].w))) != ~0u))
      STV(35, (fetch<2, 1>(r, u, B_UT + pair * 8192 + (w - 4) * 2048 + lane * 16, 1024, 0u)));
    ST(4);
#pragma unroll
    for (int i = 0; i < 2; ++i) {
      *(LAS v2u*)(L + L_U + 4088 - 8 * ((w - 4) * 128 + i * 64 + lane)) = v2u{u[i].x, u[i].y};
      *(LAS v2u*)(L + L_U + 4608 + 4088 - 8 * ((w - 4) * 128 + i * 64 + lane)) = v2u{u[i].z, u[i].w};
    }
    LAS unsigned char* bb = L + L_U + ch * 4608 + 4064 - 3584 - 32 * i16 + 16 * G + 256 * rho;
    LAS unsigned char* pb = L + L_P + (w - 4) * 8192 + G * 256 + ((i16 ^ (8 * (G >> 1)) ^ (4 * ch)) * 16);
    BAR();
    ST(5);
    f32x4 acc[8];
#pragma unroll
    for (int i = 0; i < 8; ++i) acc[i] = f32x4{0.f, 0.f, 0.f, 0.f};
    v4u B[8];
#pragma unroll
    for (int i = 0; i < 8; ++i) B[i] = *(LAS v4u*)(bb + 512 * (7 - i));
    SB();
    const int cc2 = (lane >> 2) & 1;
    v4u ur[2];
#pragma unroll
    for (int rr = 0; rr < 4; ++rr) {
#pragma unroll
      for (int m = 0; m < 8; ++m) {
#pragma unroll
        for (int nn = 0; nn + m < 8; ++nn) {
          acc[nn + m] = mfma16(W[8 * rr + nn], B[m], acc[nn + m]);
          if (rr == 3 && m >= 1 && nn == 0) *(LAS f32x4*)(pb + (m - 1) * 1024) = acc[m - 1];
          if (rr == 3 && m == 0 && (nn == 2 || nn == 3)) ur[nn - 2] = *(LAS v4u*)(L + L_U + cc2 * 4608 + 4080 - 16 * ((w - 4) * 64 + (nn - 2) * 32 + 4 * (lane >> 3) + (lane & 3)));
          SB();
        }
        if (rr < 3) B[m] = *(LAS v4u*)(bb + 512 * (7 - m) + 64 * (rr + 1));
        SB();
      }
    }
    *(LAS f32x4*)(pb + 7 * 1024) = acc[7];
    ST(6);
    arrive(L + L_C0 + 4, lane);
    until(L + L_C0 + 4, 8);
    {
      const float dc = cc2 ? bfhi(dd) : bflo(dd);
      const int o = lane & 1, j = 2 * (lane >> 3) + ((lane >> 1) & 1);
#pragma unroll
      for (int i = 0; i < 2; ++i) {
        float y[8];
#pragma unroll
        for (int e = 0; e < 4; ++e) { y[2 * e] = bfhi(ur[i][3 - e]) * dc; y[2 * e + 1] = bflo(ur[i][3 - e]) * dc; }
#pragma unroll
        for (int rh = 0; rh < 2; ++rh)
#pragma unroll
          for (int e = 0; e < 2; ++e) {
            const f32x4 t = *(LAS f32x4*)(L + L_P + ((cc2 * 2 + rh) * 8 + 2 * (w - 4) + i) * 1024 + (2 * o + e) * 256 + ((j ^ (8 * o) ^ (4 * cc2)) * 16));
            y[4 * e] += t.x; y[4 * e + 1] += t.y; y[4 * e + 2] += t.z; y[4 * e + 3] += t.w;
          }
        st16(r, ypos + i * 8 * 32768, v4u{pk2(y[0], y[1]), pk2(y[2], y[3]), pk2(y[4], y[5]), pk2(y[6], y[7])});
      }
    }
    ST(7);
    const int ct = w - 4, ks0 = 8 * cq + 2 * ct;
    {
      v4u fl[1];
      const int pr = 64 * cq + 16 * ct + (lane & 15);
      STV(39, (fetch<1, 0>(r, fl, B_FL + 32768 + (lane < 16 ? (pr & 31) * 8 + (pr >> 5) : (lane & 7) * 32 + rb) * 128, 0, tag)));
    }
    nap<D_YI>();
    const int ysrc = B_YI + (rb * 2 + hf) * 32768 + ks0 * 1024 + lane * 16;
    dma16<true>(p.ws, ysrc, L_H + ct * 2048);
    dma16<true>(p.ws, ysrc + 1024, L_H + ct * 2048 + 1024);
    ST(29);
    until<1>(L + L_C0 + 12, 4);
    f32x16 x;
    {
      LAS unsigned char* qb = L + L_QR + n * 1024;
      float s1 = 0.f, s2 = 0.f;
#pragma unroll
      for (int i = 0; i < 8; ++i) acc_stats(*(LAS v4u*)(qb + ((16 * ct + ((8 * h + i) ^ (n & 15))) * 16)), s1, s2);
      *(LAS f32x2*)(L + L_S0 + ct * 512 + n * 16 + h * 8) = f32x2{s1, s2};
#pragma unroll
      for (int i = 0; i < 4; ++i) {
        const v2u t = *(LAS v2u*)(qb + (((2 * ks0 + i) ^ (n & 15)) * 16) + h * 8);
        x[4 * i] = bflo(t.x); x[4 * i + 1] = bfhi(t.x); x[4 * i + 2] = bflo(t.y); x[4 * i + 3] = bfhi(t.y);
      }
    }
    arrive(L + L_C0 + 20, lane);
    until(L + L_C0 + 20, 4);
    float m, rs;
    row_moments(L + L_S0, lane, m, rs);
    ST(8);
    v4u y0, y1;
    u32 spins = 0;
#pragma nounroll
    for (;; ++spins) {
      DRAIN();
      LAS unsigned char* yb = L + L_H + ct * 2048 + (4 * h + q) * 64 + 32 * (G & 1) + 8 * pp;
      y0 = trd2(yb, 512); y1 = trd2(yb + 1024, 512);
      float s1 = 0.f;
#pragma unroll
      for (int i = 0; i < 4; ++i) { s1 = dot2(y0[i], y0[i], s1); s1 = dot2(y1[i], y1[i], s1); }
      if (all_lanes(s1 == s1) || spins > SPIN_CAP) break;
      nap<D_RETRY>();
      dma16<true>(p.ws, ysrc, L_H + ct * 2048);
      dma16<true>(p.ws, ysrc + 1024, L_H + ct * 2048 + 1024);
    }
    STV(36, spins);
    ST(9);
    gate_publish(x, rs, m * rs, y0, y1, r, B_XI + rb * 65536 + (hf * 32 + ks0) * 1024 + lane * 16);
    *(LAS v4u*)(L + L_Y + ct * 2048 + lane * 16) = y0; *(LAS v4u*)(L + L_Y + ct * 2048 + 1024 + lane * 16) = y1;
    if (w == 4 && lane < 8) *(LAS u32*)(L + L_C1 + lane * 4) = 0u;
  } else {
    LOAD_WPH();
    ST(3);
    BAR();
    ST(4);
    st16(r, ypos, s4); st16(r, ypos + 8 * 32768, s4);
    {
      const int o0 = B_XI + rb * 65536 + (hf * 32 + 8 * cq + 2 * w) * 1024 + lane * 16;
      st16(r, o0, s4); st16(r, o0 + 1024, s4);
      const bool kvr = w < 2, on = lane < 8 && (kvr || (b & 7) == 0);
      if (on) st16(r, (kvr ? B_KVR + w * 32768 + b * 128 : B_ZR + (w - 2) * 4096 + (b >> 3) * 128) + lane * 16, s4);
    }
    FENCE();
    stage_kv(p, 0, cg, w, lane);
    VMW(16);
    arrive(L + L_C0 + 4, lane);
    arrive(L + L_C0 + 24, lane);
#pragma unroll
    for (int i = 0; i < 8; ++i)
      dma16<false>(p.q, (rb * 64 + hf * 32 + w * 8 + i) * 1024 + ((lane ^ ((w * 8 + i) & 15)) * 16), L_QR + (w * 8 + i) * 1024);
    if (w == 3) {
      until(L + L_C0 + 24, 4);
      if (lane == 0) st16(r, B_FL + 32768 + b * 128, v4u{tag, tag, tag, tag});
    }
    {
      const GAS unsigned char* src = (const GAS unsigned char*)p.wqkv + (256 + (w & 1) * 128 + (lane >> 3)) * 3072 + (1 + (w >> 1)) * 1024 + cg * 128 + (((lane & 7) ^ (2 * ((G & 1) ^ (w & 2)))) * 16);
#pragma unroll
      for (int i = 0; i < 16; ++i) W[16 + i] = *(const GAS v4u*)(src + i * 8 * 3072);
    }
    VMW(16);
    arrive(L + L_C0 + 12, lane);
    until<1>(L + L_C0 + 12, 4);
    ST(5);
    LAS unsigned char* wb = L + R1A + (G & 1) * 32768 + (4 * h + q) * 128 + ((w ^ (q >> 1) ^ (2 * (G & 1))) * 32) + 8 * pp;
#pragma unroll
    for (int i = 0; i < 16; ++i) W[i] = trd2(wb + i * 2048, 1024);
    arrive(L + L_C0 + 8, lane);
    until<1>(L + L_C0 + 8, 4);
    ST(6);
#pragma unroll
    for (int i = 0; i < 16; ++i) *(LAS v4u*)(L + R1A + (w * 16 + i) * 1024 + lane * 16) = W[16 + i];
    arrive(L + L_C0 + 16, lane);
    until<1>(L + L_C0 + 16, 4);
    ST(7);
#pragma unroll
    for (int i = 0; i < 16; ++i) W[16 + i] = trd2(wb + i * 2048, 1024);
    until<1>(L + L_C0 + 4, 8);
    csf = colsum<0, 32>(W);
    ST(9);
  }
  BAR();
  ST(28);
  if (!KV) LOAD_WPH();
  STV(57, load_image(p, L, 0, rb, w, lane));
  BAR();
  ST(10);
  layer<0, KV>(p, c, W, csf, wph ST_ARG);
  if (KV && b == 0 && w == 0 && lane == 0) __builtin_amdgcn_raw_buffer_store_b32(tag + 1u, r, WS_EPOCH, 0, SC1);
  layer<1, KV>(p, c, W, csf, wph ST_ARG);
}

__global__ void __launch_bounds__(512, 2) fused_kernel(Params p) {
  extern __shared__ __attribute__((aligned(16))) unsigned char smem[];
  LAS unsigned char* L = (LAS unsigned char*)smem;
  const int tid = threadIdx.x, lane = tid & 63, w = __builtin_amdgcn_readfirstlane(tid >> 6), b = blockIdx.x;
  const rsrc_t r = __builtin_amdgcn_make_buffer_rsrc((void*)p.ws, 0, WS_BYTES, 0x00020000);
  const int x8 = b & 7, hi = b >> 3;
  ST(0);
  STC(32);
  const u32 tag = __builtin_amdgcn_readfirstlane(*(const GAS u32*)(p.ws + WS_EPOCH)) + 1u;
  {
    const int lr = 8 * w + (lane >> 3);
    dma16<false>(p.k, (64 * hi + lr) * 1024 + x8 * 128 + (((lane & 7) ^ (((lr + 4) >> 2) & 7)) * 16), L_KT + 512 + w * 1024);
    dma16<false>(p.v, (64 * hi + lr) * 1024 + x8 * 128 + (((lane & 7) ^ ((lr >> 2) & 7)) * 16), L_VT + w * 1024);
    if (lane < 32) {
      if (w == 0) {
        if (hi > 0) dma16<false>(p.k, (64 * hi - 4 + (lane >> 3)) * 1024 + x8 * 128 + (lane & 7) * 16, L_KT);
        else *(LAS v4u*)(L + L_KT + lane * 16) = v4u{0u, 0u, 0u, 0u};
      }
      if (w == 1) dma16<false>(p.shk, x8 * 512 + lane * 16, L_C0 - 512);
      if (w == 2 && lane < 8) *(LAS u32*)(L + L_C0 + lane * 4) = 0u;
    }
  }
  st16(r, B_UT + (x8 * 32 + 4 * w + (lane >> 4)) * 8192 + (16 * hi + (lane & 15)) * 16, v4u{~0u, ~0u, ~0u, ~0u});
  DRAIN();
  BAR();
  if (tid == 64) st16(r, B_FL + b * 128, v4u{tag, tag, tag, tag});
  ST(1);
  const Ctx c = {L, r, tag, b, w, lane};
  if (w < 4) program<true>(p, c ST_ARG);
  else program<false>(p, c ST_ARG);
  ST(30);
  STC(33);
  ST_FLUSH();
}

extern "C" void kernel_launch(void* const* d_in, const int* in_sizes, int n_in, void* d_out, int out_size, void* d_ws, size_t ws_size,
                              hipStream_t stream) {
  Params p;
  p.q = (const u16*)d_in[0]; p.k = (const u16*)d_in[1]; p.v = (const u16*)d_in[2]; p.shk = (const u16*)d_in[3];
  p.h = (const u16*)d_in[4]; p.dv = (const u16*)d_in[5]; p.wqkv = (const u16*)d_in[7]; p.wphi = (const u16*)d_in[8];
  p.wo = (const u16*)d_in[9]; p.out = (u16*)d_out; p.ws = (unsigned char*)d_ws + (ws_size - WS_BYTES);
  (void)hipFuncSetAttribute((const void*)fused_kernel, hipFuncAttributeMaxDynamicSharedMemorySize, LDS_BYTES);
  hipLaunchKernelGGL(fused_kernel, dim3(256), dim3(512), LDS_BYTES, stream, p);
}
```

```cpp
#include <hip/hip_runtime.h>
#include <hip/hip_bf16.h>

typedef unsigned short u16;
typedef unsigned u32;
typedef unsigned long long u64;
typedef u32 v4u __attribute__((ext_vector_type(4)));
typedef u32 v2u __attribute__((ext_vector_type(2)));
typedef short v4s __attribute__((ext_vector_type(4)));
typedef float f32x2 __attribute__((ext_vector_type(2)));
typedef float f32x4 __attribute__((ext_vector_type(4)));
typedef float f32x16 __attribute__((ext_vector_type(16)));
typedef __bf16 v8bf __attribute__((ext_vector_type(8)));
typedef __bf16 v2bf __attribute__((ext_vector_type(2)));
#define LAS __attribute__((address_space(3)))
#define GAS __attribute__((address_space(1)))
#define DI __device__ __forceinline__
template <class T, class F> DI T bc(F x) { return __builtin_bit_cast(T, x); }
#define BC(T, x) bc<T>(x)
typedef __amdgpu_buffer_rsrc_t rsrc_t;

constexpr int MB = 1 << 20;
constexpr int WS_EPOCH = 0;
constexpr int B_KVR = 4096;
constexpr int B_ZR = B_KVR + 64 * 1024;
constexpr int B_FL = 128 * 1024;
constexpr int B_ZP = 256 * 1024;
constexpr int B_SP = 512 * 1024;
constexpr int B_UT = 1 * MB;
constexpr int B_YI = 3 * MB;
constexpr int B_XI = 5 * MB;
constexpr int B_OI = 9 * MB;
constexpr int B_XR = 11 * MB;
constexpr int B_OR = B_XR + 512 * 1024;
constexpr int B_KVP = 13 * MB;
constexpr int B_STAMP = 25 * MB;
constexpr int WS_BYTES = 26 * MB;

constexpr int R1A = 0, R1B = 65536, R2 = 98304, LDS_BYTES = 163840;
constexpr int L_H = R2, L_U = R2 + 16640, L_P = R2 + 25856, L_S0 = R2 + 59136, L_C0 = R2 + 65536 - 64;
constexpr int L_KT = L_P, L_VT = L_P + 8704;
constexpr int L_QR = R1B;
constexpr int L_ST = R1B, L_EX = R1B + 4096, L_C1 = R1B + 20480, L_Y = R1B + 24576;

constexpr u32 SPIN_CAP = 1u << 14;
constexpr int SC1 = 16;
constexpr float LOG2E = 1.4426950408889634f;
constexpr u32 ONES = 0x3f803f80u;

#define D_UT 0
#define D_YI 0
#define D_KVP 16
#define D_OI 10
#define D_KVR 12
#define D_SP 8
#define D_XI 10
#define PF_Q 1
#define PF_W 0
#define D_H 0
#define C_EARLY 1
#define D_CL 8
#define Q_EARLY 0
#define D_F1 0
#define PRIO 0
#define D_RETRY 2

struct Params {
  const u16 *q, *k, *v, *shk, *h, *dv, *wqkv, *wphi, *wo;
  u16* out;
  unsigned char* ws;
};

DI float bflo(u32 x) { return BC(float, x << 16); }
DI float bfhi(u32 x) { return BC(float, x & 0xffff0000u); }
DI u32 pk2(float a, float b) { f32x2 p = {a, b}; return BC(u32, __builtin_convertvector(p, v2bf)); }
DI float op(float r) { asm("" : "+v"(r)); return r; }
DI float mx3(float a, float b, float c) { return __builtin_fmaxf(__builtin_fmaxf(a, b), c); }
DI f32x16 mfma32(v4u a, v4u b, f32x16 c) { return __builtin_amdgcn_mfma_f32_32x32x16_bf16(BC(v8bf, a), BC(v8bf, b), c, 0, 0, 0); }
DI f32x4 mfma16(v4u a, v4u b, f32x4 c) { return __builtin_amdgcn_mfma_f32_16x16x32_bf16(BC(v8bf, a), BC(v8bf, b), c, 0, 0, 0); }
DI float dot2(u32 a, u32 b, float c) { return __builtin_amdgcn_fdot2_f32_bf16(BC(v2bf, a), BC(v2bf, b), c, false); }
DI v2u trd(LAS unsigned char* p) { return BC(v2u, __builtin_amdgcn_ds_read_tr16_b64_v4i16((LAS v4s*)p)); }
DI v4u trd2(LAS unsigned char* p, int second) { v2u a = trd(p), b = trd(p + second); return v4u{a.x, a.y, b.x, b.y}; }
DI v4u pack8(const f32x16& x, int s) { return v4u{pk2(x[8 * s], x[8 * s + 1]), pk2(x[8 * s + 2], x[8 * s + 3]), pk2(x[8 * s + 4], x[8 * s + 5]), pk2(x[8 * s + 6], x[8 * s + 7])}; }
DI void swap32(float& a, float& b) { auto r = __builtin_amdgcn_permlane32_swap(BC(u32, a), BC(u32, b), false, false); a = BC(float, r[0]); b = BC(float, r[1]); }
DI void swap16(float& a, float& b) { auto r = __builtin_amdgcn_permlane16_swap(BC(u32, a), BC(u32, b), false, false); a = BC(float, r[0]); b = BC(float, r[1]); }
DI void swap32u(u32& a, u32& b) { auto r = __builtin_amdgcn_permlane32_swap(a, b, false, false); a = r[0]; b = r[1]; }
template <int CTRL> DI u32 dppu(u32 x) { return (u32)__builtin_amdgcn_update_dpp(0, (int)x, CTRL, 0xf, 0xf, true); }
template <int CTRL> DI float dppf(float x) { return BC(float, dppu<CTRL>(BC(u32, x))); }
DI bool all_lanes(bool ok) { return __builtin_amdgcn_ballot_w64(!ok) == 0ull; }
#define BAR() asm volatile("s_waitcnt lgkmcnt(0)\n\ts_barrier" ::: "memory")
#define DRAIN() asm volatile("s_waitcnt vmcnt(0)" ::: "memory")
#define VMW(n) asm volatile("s_waitcnt vmcnt(%0)" ::"n"(n) : "memory")
#define PIN(x) asm volatile("" : "+v"(x))
#define SB() __builtin_amdgcn_sched_barrier(0)
#define FENCE() asm volatile("" ::: "memory")
template <int I> struct IC { static constexpr int v = I; };
template <int I, int N, class F> DI void sfor(F&& f) { if constexpr (I < N) { f(IC<I>{}); sfor<I + 1, N>(f); } }
template <int N> DI void nap() { if (N > 0) __builtin_amdgcn_s_sleep(N); }

template <bool COH> DI void dma16(const void* base, u32 voff, u32 ldst) {
  u32 keep;
  ldst = __builtin_amdgcn_readfirstlane(ldst);
  if (COH) asm volatile("s_mov_b32 %0, m0\n\ts_mov_b32 m0, %3\n\ts_nop 0\n\tglobal_load_lds_dwordx4 %1, %2 sc1\n\ts_mov_b32 m0, %0" : "=&s"(keep) : "v"(voff), "s"(base), "s"(ldst) : "memory");
  else asm volatile("s_mov_b32 %0, m0\n\ts_mov_b32 m0, %3\n\ts_nop 0\n\tglobal_load_lds_dwordx4 %1, %2\n\ts_mov_b32 m0, %0" : "=&s"(keep) : "v"(voff), "s"(base), "s"(ldst) : "memory");
}
template <bool COH, int N> DI void dma_run(const void* base, u32 voff, u32 ss, u32 ldst) {
  u64 a = (u64)base;
  u32 keep;
  ldst = __builtin_amdgcn_readfirstlane(ldst);
  asm volatile("s_mov_b32 %0, m0\n\ts_mov_b32 m0, %1" : "=&s"(keep) : "s"(ldst));
#pragma unroll
  for (int i = 0; i < N; ++i) {
    if (COH) asm volatile("s_nop 0\n\tglobal_load_lds_dwordx4 %0, %1 sc1\n\ts_add_u32 m0, m0, 0x400" ::"v"(voff), "s"(a) : "memory", "scc");
    else asm volatile("s_nop 0\n\tglobal_load_lds_dwordx4 %0, %1\n\ts_add_u32 m0, m0, 0x400" ::"v"(voff), "s"(a) : "memory", "scc");
    a += ss;
  }
  asm volatile("s_mov_b32 m0, %0" ::"s"(keep));
}
DI v4u ld16(rsrc_t r, int off) { return __builtin_amdgcn_raw_buffer_load_b128(r, off, 0, SC1); }
DI u32 ld4(rsrc_t r, int off) { return __builtin_amdgcn_raw_buffer_load_b32(r, off, 0, SC1); }
DI u32 sum4(v4u x, u32 a) { return (a + x.x + x.y) + x.z + x.w; }
DI void st16(rsrc_t r, int off, v4u v) { __builtin_amdgcn_raw_buffer_store_b128(v, r, off, 0, SC1); }

DI void publish2(rsrc_t r, int off, int roff, v4u a, v4u b, u32 tag) {
  st16(r, off, a); st16(r, off + 1024, b);
  __builtin_amdgcn_raw_buffer_store_b32(sum4(b, sum4(a, tag)), r, roff, 0, SC1);
}

DI void arrive(u32 ctr) {
  asm volatile("s_mov_b64 exec, 1\n\tds_add_u32 %0, %1\n\ts_mov_b64 exec, -1" ::"v"(ctr), "v"(1u) : "memory");
}
template <int NAP = 0> DI void until(u32 ctr, u32 target) {
  u32 t, u;
  if (NAP) asm volatile("s_branch 2f\n1:\n\ts_sleep %4\n2:\n\tds_read_b32 %0, %2\n\ts_waitcnt lgkmcnt(0)\n\tv_readfirstlane_b32 %1, %0\n\ts_cmp_lt_u32 %1, %3\n\ts_cbranch_scc1 1b" : "=&v"(t), "=&s"(u) : "v"(ctr), "s"(target), "n"(NAP) : "memory", "scc");
  else asm volatile("1:\n\tds_read_b32 %0, %2\n\ts_waitcnt lgkmcnt(0)\n\tv_readfirstlane_b32 %1, %0\n\ts_cmp_lt_u32 %1, %3\n\ts_cbranch_scc1 1b" : "=&v"(t), "=&s"(u) : "v"(ctr), "s"(target) : "memory", "scc");
}

template <int N, int MODE> DI u32 fetch(rsrc_t r, v4u (&v)[N], int off, int stride, u32 tag) {
  u32 spins = 0;
#pragma nounroll
  for (;; ++spins) {
    FENCE();
#pragma unroll
    for (int i = 0; i < N; ++i) v[i] = ld16(r, off + i * stride);
    u32 a = 0u;
#pragma unroll
    for (int i = 0; i < N; ++i) {
      if (MODE == 0) a |= (v[i].y ^ tag) | (v[i].w ^ tag);
      if (MODE == 1) a = max(max(a, max(v[i].x, v[i].y)), max(v[i].z, v[i].w));
      if (MODE == 2) { v[i] ^= tag; a |= (v[i].x | v[i].y) | (v[i].z | v[i].w); }
    }
    const bool ok = MODE == 0 ? a == 0u : MODE == 1 ? a != ~0u : (a & 0xffu) == 0u;
    if (all_lanes(ok) || spins > SPIN_CAP) break;
    nap<D_RETRY>();
  }
  FENCE();
  return spins;
}

#define STV(i, v) (void)(v)
#define STC(i)
#define ST_PARAM
#define ST_ARG
#define ST_FLUSH()
#define ST(i)

DI void dual_softmax(const f32x16& x, int o, float c, v4u& plus, v4u& minus) {
  float mx = mx3(mx3(x[o], x[o + 1], x[o + 2]), mx3(x[o + 3], x[o + 4], x[o + 5]), mx3(x[o + 6], x[o + 7], x[o]));
  float nm = mx3(mx3(-x[o], -x[o + 1], -x[o + 2]), mx3(-x[o + 3], -x[o + 4], -x[o + 5]), mx3(-x[o + 6], -x[o + 7], -x[o]));
  swap32(mx, nm);
  float t = mx3(mx, nm, nm) * -c, t2 = t;
  swap32(t, t2);
  float ep[8], em[8];
#pragma unroll
  for (int i = 0; i < 8; ++i) { ep[i] = op(__builtin_fmaf(x[o + i], c, t)); em[i] = op(__builtin_fmaf(x[o + i], -c, t2)); }
#pragma unroll
  for (int i = 0; i < 8; ++i) { ep[i] = __builtin_amdgcn_exp2f(ep[i]); em[i] = __builtin_amdgcn_exp2f(em[i]); }
  float sp = op(op(op(ep[0] + ep[1]) + op(ep[2] + ep[3])) + op(op(ep[4] + ep[5]) + op(ep[6] + ep[7])));
  float sm = op(op(op(em[0] + em[1]) + op(em[2] + em[3])) + op(op(em[4] + em[5]) + op(em[6] + em[7])));
  swap32(sp, sm);
  float ip = __builtin_amdgcn_rcpf(sp + sm), im = ip;
  swap32(ip, im);
  plus = v4u{pk2(op(ep[0] * ip), op(ep[1] * ip)), pk2(op(ep[2] * ip), op(ep[3] * ip)), pk2(op(ep[4] * ip), op(ep[5] * ip)), pk2(op(ep[6] * ip), op(ep[7] * ip))};
  minus = v4u{pk2(op(em[0] * im), op(em[1] * im)), pk2(op(em[2] * im), op(em[3] * im)), pk2(op(em[4] * im), op(em[5] * im)), pk2(op(em[6] * im), op(em[7] * im))};
}

struct Ctx {
  LAS unsigned char* L;
  rsrc_t r;
  u32 tag;
  int b, w, lane;
};

template <int N> DI void gemm(f32x16& acc, const v4u (&W)[32], LAS unsigned char* xb) {
  constexpr int PD = 4;
  v4u x[PD];
#pragma unroll
  for (int i = 0; i < PD; ++i) x[i] = *(LAS v4u*)(xb + i * 1024);
  SB();
#pragma unroll
  for (int i = 0; i < N; ++i) {
    const v4u cur = x[i % PD];
    acc = mfma32(W[i], cur, acc);
    if (i + PD < N) x[i % PD] = *(LAS v4u*)(xb + (i + PD) * 1024);
    SB();
  }
  PIN(acc);
}
DI void row_moments(LAS unsigned char* st, int lane, float& m, float& rs) {
  LAS f32x4* s = (LAS f32x4*)(st + (lane & 31) * 16);
  const f32x4 a = s[0], b = s[32], c = s[64], d = s[96];
  m = op(op(op(a.x + a.z) + op(b.x + b.z)) + op(op(c.x + c.z) + op(d.x + d.z))) * (-1.f / 512.f);
  const float e2 = op(op(op(a.y + a.w) + op(b.y + b.w)) + op(op(c.y + c.w) + op(d.y + d.w))) * (1.f / 512.f);
  rs = __builtin_amdgcn_rsqf(mx3(__builtin_fmaf(-m, m, op(e2)), 0.f, 0.f) + 1e-5f);
}
DI void row_stats(LAS unsigned char* L, int T, int lane, u32& mf, float& rs) {
  float m;
  row_moments(L + L_ST + T * 2048, lane, m, rs);
  const float hi = bflo(pk2(m, m));
  mf = pk2(hi, op(m - hi));
}
DI void acc_stats(v4u x, float& s1, float& s2) {
  s1 = dot2(x.x, ONES, s1); s2 = dot2(x.x, x.x, s2); s1 = dot2(x.y, ONES, s1); s2 = dot2(x.y, x.y, s2);
  s1 = dot2(x.z, ONES, s1); s2 = dot2(x.z, x.z, s2); s1 = dot2(x.w, ONES, s1); s2 = dot2(x.w, x.w, s2);
}
template <int N0, int N1> DI v2u colsum(const v4u (&W)[32]) {
  float cs = 0.f, cs2 = 0.f;
#pragma unroll
  for (int i = N0; i < N1; ++i) { cs = dot2(W[i].x, ONES, cs); cs2 = dot2(W[i].y, ONES, cs2); cs = dot2(W[i].z, ONES, cs); cs2 = dot2(W[i].w, ONES, cs2); }
  cs += cs2;
  const u32 hh = pk2(cs, cs);
  const float lo = cs - bflo(hh);
  v2u o = {hh, pk2(lo, lo)};
  PIN(o);
  return o;
}
DI void stage64(const void* base, int off, int stride, int w4, int lane) {
  dma_run<false, 16>(base, off + (lane >> 2) * stride + (lane & 3) * 16, 16 * stride, R1A + w4 * 16384);
}
DI u32 load_image(const Params& p, rsrc_t r, LAS unsigned char* L, int LY, int rb, int w, int lane, u32 tag) {
  const int f0 = (w & 1) * 32 + (w >> 1) * 8;
  float a1, a2, b1, b2;
  u32 spins = 0;
  nap<D_XI>();
#pragma nounroll
  for (;; ++spins) {
    dma_run<true, 8>(p.ws, B_XI + LY * 2 * MB + rb * 65536 + f0 * 1024 + lane * 16, 1024, R2 + f0 * 1024);
    v4u rec;
#pragma unroll
    for (int i = 0; i < 4; ++i) rec[i] = ld4(r, B_XR + LY * 262144 + rb * 8192 + f0 * 128 + i * 256 + lane * 4);
    a1 = a2 = b1 = b2 = 0.f;
    u32 cs = 4u * tag;
    LAS v4u* x0 = (LAS v4u*)(L + R2 + f0 * 1024 + lane * 16);
    v4u s, t;
    VMW(10); s = x0[0]; t = x0[64]; acc_stats(s, a1, a2); acc_stats(t, b1, b2); cs = sum4(t, sum4(s, cs));
    VMW(8); s = x0[128]; t = x0[192]; acc_stats(s, a1, a2); acc_stats(t, b1, b2); cs = sum4(t, sum4(s, cs));
    VMW(6); s = x0[256]; t = x0[320]; acc_stats(s, a1, a2); acc_stats(t, b1, b2); cs = sum4(t, sum4(s, cs));
    VMW(4); s = x0[384]; t = x0[448]; acc_stats(s, a1, a2); acc_stats(t, b1, b2); cs = sum4(t, sum4(s, cs));
    FENCE();
    if (all_lanes(cs == sum4(rec, 0u)) || spins > SPIN_CAP) break;
    nap<D_RETRY>();
  }
  *(LAS f32x2*)(L + L_ST + (w & 1) * 2048 + (w >> 1) * 512 + (lane & 31) * 16 + (lane >> 5) * 8) = f32x2{a1 + b1, a2 + b2};
  return spins;
}
DI void gate_publish(f32x16& acc, float rs, float mr, v4u y0, v4u y1, rsrc_t r, int LY, int rb, int f, int lane, u32 tag) {
#pragma unroll
  for (int i = 0; i < 4; ++i) {
    acc[2 * i] = __builtin_fmaf(__builtin_fmaf(acc[2 * i], rs, mr), bflo(y0[i]), acc[2 * i]);
    acc[2 * i + 1] = __builtin_fmaf(__builtin_fmaf(acc[2 * i + 1], rs, mr), bfhi(y0[i]), acc[2 * i + 1]);
    acc[8 + 2 * i] = __builtin_fmaf(__builtin_fmaf(acc[8 + 2 * i], rs, mr), bflo(y1[i]), acc[8 + 2 * i]);
    acc[9 + 2 * i] = __builtin_fmaf(__builtin_fmaf(acc[9 + 2 * i], rs, mr), bfhi(y1[i]), acc[9 + 2 * i]);
  }
  publish2(r, B_XI + LY * 2 * MB + rb * 65536 + f * 1024 + lane * 16, B_XR + LY * 262144 + rb * 8192 + f * 128 + lane * 4, pack8(acc, 0), pack8(acc, 1), tag);
}

struct Post {
  v4u selp, selm, kb, vb, fp, fm, v0, v1;
  u32 ones;
  f32x16 pr, vt, pt;
  float vs[8], ep[8], em[8], mx, nm, t, t2, cc, s0, s1, s2, s3;
  DI void init(int n, int h) {
    const int n16 = n & 15, j = 4 * (n16 >> 3) + (n16 & 3);
    const u32 one = (((n16 >> 2) & 1) == h) ? (0x3f80u << (16 * (j & 1))) : 0u;
    const u32 op_ = n < 16 ? one : 0u, om = n < 16 ? 0u : one;
    selp = v4u{(j >> 1) == 0 ? op_ : 0u, (j >> 1) == 1 ? op_ : 0u, (j >> 1) == 2 ? op_ : 0u, (j >> 1) == 3 ? op_ : 0u};
    selm = v4u{(j >> 1) == 0 ? om : 0u, (j >> 1) == 1 ? om : 0u, (j >> 1) == 2 ? om : 0u, (j >> 1) == 3 ? om : 0u};
    ones = n < 16 ? 0u : ONES;
    PIN(selp); PIN(selm); PIN(ones);
  }
  template <int S> DI void step(const f32x16& a, float rs, v4u wph, f32x16& kv, bool first) {
    f32x16 zero;
#pragma unroll
    for (int i = 0; i < 16; ++i) zero[i] = 0.f;
    if constexpr (S == 0) { kb = pack8(a, 0); cc = rs * LOG2E; }
    if constexpr (S == 1) { pr = mfma32(wph, kb, zero); _Pragma("unroll") for (int i = 0; i < 8; ++i) vs[i] = op(a[8 + i] * rs); }
    if constexpr (S == 2) vb = v4u{pk2(vs[0], vs[1]), pk2(vs[2], vs[3]), pk2(vs[4], vs[5]), pk2(vs[6], vs[7])};
    if constexpr (S == 3) {
      vt = mfma32(vb, selp, zero);
      mx = mx3(mx3(pr[0], pr[1], pr[2]), mx3(pr[3], pr[4], pr[5]), mx3(pr[6], pr[7], pr[0]));
      nm = mx3(mx3(-pr[0], -pr[1], -pr[2]), mx3(-pr[3], -pr[4], -pr[5]), mx3(-pr[6], -pr[7], -pr[0]));
    }
    if constexpr (S == 4) { swap32(mx, nm); t = mx3(mx, nm, nm) * -cc; t2 = t; }
    if constexpr (S == 5) { swap32(t, t2); v0.x = pk2(vt[0], vt[1]) | ones; }
    if constexpr (S == 6) _Pragma("unroll") for (int i = 0; i < 5; ++i) ep[i] = op(__builtin_fmaf(pr[i], cc, t));
    if constexpr (S == 7) { _Pragma("unroll") for (int i = 5; i < 8; ++i) ep[i] = op(__builtin_fmaf(pr[i], cc, t)); _Pragma("unroll") for (int i = 0; i < 2; ++i) em[i] = op(__builtin_fmaf(pr[i], -cc, t2)); }
    if constexpr (S == 8) _Pragma("unroll") for (int i = 2; i < 7; ++i) em[i] = op(__builtin_fmaf(pr[i], -cc, t2));
    if constexpr (S == 9) { em[7] = op(__builtin_fmaf(pr[7], -cc, t2)); ep[0] = __builtin_amdgcn_exp2f(ep[0]); ep[1] = __builtin_amdgcn_exp2f(ep[1]); }
    if constexpr (S == 10) { ep[2] = __builtin_amdgcn_exp2f(ep[2]); ep[3] = __builtin_amdgcn_exp2f(ep[3]); s0 = op(ep[0] + ep[1]); }
    if constexpr (S == 11) { ep[4] = __builtin_amdgcn_exp2f(ep[4]); ep[5] = __builtin_amdgcn_exp2f(ep[5]); s1 = op(ep[2] + ep[3]); }
    if constexpr (S == 12) { ep[6] = __builtin_amdgcn_exp2f(ep[6]); ep[7] = __builtin_amdgcn_exp2f(ep[7]); s0 = op(s0 + s1); }
    if constexpr (S == 13) { em[0] = __builtin_amdgcn_exp2f(em[0]); em[1] = __builtin_amdgcn_exp2f(em[1]); s1 = op(ep[4] + ep[5]); }
    if constexpr (S == 14) { em[2] = __builtin_amdgcn_exp2f(em[2]); em[3] = __builtin_amdgcn_exp2f(em[3]); s2 = op(ep[6] + ep[7]); }
    if constexpr (S == 15) { em[4] = __builtin_amdgcn_exp2f(em[4]); em[5] = __builtin_amdgcn_exp2f(em[5]); s1 = op(s1 + s2); }
    if constexpr (S == 16) { em[6] = __builtin_amdgcn_exp2f(em[6]); em[7] = __builtin_amdgcn_exp2f(em[7]); s0 = op(s0 + s1); }
    if constexpr (S == 17) { s1 = op(em[0] + em[1]); s2 = op(em[2] + em[3]); s3 = op(em[4] + em[5]); t = op(em[6] + em[7]); s1 = op(s1 + s2); }
    if constexpr (S == 18) { s3 = op(s3 + t); v0.y = pk2(vt[2], vt[3]) | ones; s1 = op(s1 + s3); }
    if constexpr (S == 19) { swap32(s0, s1); t = __builtin_amdgcn_rcpf(s0 + s1); }
    if constexpr (S == 20) { t2 = t; v0.z = pk2(vt[4], vt[5]) | ones; swap32(t, t2); }
    if constexpr (S == 21) _Pragma("unroll") for (int i = 0; i < 5; ++i) ep[i] = op(ep[i] * t);
    if constexpr (S == 22) { _Pragma("unroll") for (int i = 5; i < 8; ++i) ep[i] = op(ep[i] * t); fp.x = pk2(ep[0], ep[1]); fp.y = pk2(ep[2], ep[3]); }
    if constexpr (S == 23) { fp.z = pk2(ep[4], ep[5]); fp.w = pk2(ep[6], ep[7]); _Pragma("unroll") for (int i = 0; i < 3; ++i) em[i] = op(em[i] * t2); }
    if constexpr (S == 24) { pt = mfma32(fp, selp, zero); _Pragma("unroll") for (int i = 3; i < 8; ++i) em[i] = op(em[i] * t2); fm = v4u{pk2(em[0], em[1]), pk2(em[2], em[3]), pk2(em[4], em[5]), pk2(em[6], em[7])}; }
    if constexpr (S == 25) { pt = mfma32(fm, selm, pt); v0.w = pk2(vt[6], vt[7]) | ones; v1.x = pk2(vt[8], vt[9]) | ones; v1.y = pk2(vt[10], vt[11]) | ones; v1.z = pk2(vt[12], vt[13]) | ones; v1.w = pk2(vt[14], vt[15]) | ones; }
    if constexpr (S == 27) fp = pack8(pt, 0);
    if constexpr (S == 28) { kv = mfma32(fp, v0, first ? zero : kv); fm = pack8(pt, 1); }
    if constexpr (S == 29) kv = mfma32(fm, v1, kv);
  }
};

template <int LY, bool KV>
DI void layer(const Params& p, const Ctx& c, v4u (&W)[32], v2u& csf, const v4u wph ST_PARAM) {
  LAS unsigned char* L = c.L;
  const rsrc_t r = c.r;
  int lane = c.lane;
  PIN(lane);
  const int b = c.b, w = c.w, rb = b & 31, cg = b >> 5, hf = cg & 1, cq = cg >> 1;
  const int G = lane >> 4, q = (lane >> 2) & 3, pp = lane & 3, h = lane >> 5, n = lane & 31;
  constexpr int S0 = LY ? 20 : 10, S1 = LY ? 50 : 40;
  constexpr u32 C4 = LY ? 8 : 4;
  const u32 tagl = c.tag + LY;
  const v4u z4 = {0u, 0u, 0u, 0u};
  f32x16 zero;
#pragma unroll
  for (int i = 0; i < 16; ++i) zero[i] = 0.f;
  if (KV) {
    const v4u cs4 = {csf.x, csf.y, 0u, 0u};
    f32x16 acc0 = zero, acc1, kv;
    Post ps;
    u32 mf0, mf1; float rs0, rs1;
    {
      constexpr int PD = 4;
      LAS unsigned char* xb = L + R2 + lane * 16;
      v4u x[PD];
#pragma unroll
      for (int i = 0; i < PD; ++i) x[i] = *(LAS v4u*)(xb + i * 1024);
      SB();
      sfor<0, 64>([&](auto I) __attribute__((always_inline)) {
        constexpr int i = decltype(I)::v;
        const v4u cur = x[i % PD];
        if constexpr (i < 32) acc0 = mfma32(W[i & 31], cur, acc0); else acc1 = mfma32(W[i & 31], cur, acc1);
        if constexpr (i + PD < 64) x[i % PD] = *(LAS v4u*)(xb + (i + PD) * 1024);
        if constexpr (i == 1) { row_stats(L, 0, lane, mf0, rs0); PIN(mf0); PIN(rs0); }
        if constexpr (i == 8) { row_stats(L, 1, lane, mf1, rs1); PIN(mf1); PIN(rs1); }
        if constexpr (i == 15) ps.init(n, h);
        if constexpr (i == 24) acc0 = mfma32(cs4, v4u{mf0, mf0, 0u, 0u}, acc0);
        if constexpr (i == 28) acc1 = mfma32(cs4, v4u{mf1, mf1, 0u, 0u}, zero);
        if constexpr (i >= 33) ps.template step<i - 33>(acc0, rs0, wph, kv, true);
        if constexpr (i < 1 || i >= 22 || i == 7 || i == 14) SB();
      });
    }
    ST(S1 + 2);
    arrive(L_C1 + 16);
    sfor<0, 31>([&](auto I) __attribute__((always_inline)) { ps.template step<decltype(I)::v>(acc1, rs1, wph, kv, false); });
    if (n <= 16) {
      const int head = 4 * cg + w;
      const int o0 = n < 16 ? B_KVP + ((head * 8 + h * 2 + (n >> 3)) * 32 + rb) * 256 + (n & 7) * 16 : B_ZP + (head * 32 + rb) * 128 + h * 32;
      const u32 tb = tagl & 0xffu;
#pragma unroll
      for (int qq = 0; qq < 4; ++qq)
        st16(r, o0 + (n < 16 ? (qq >> 1) * 32768 + (qq & 1) * 128 : (qq >> 1) * 64 + (qq & 1) * 16),
             v4u{(BC(u32, kv[4 * qq]) & ~0xffu) | tb, (BC(u32, kv[4 * qq + 1]) & ~0xffu) | tb, (BC(u32, kv[4 * qq + 2]) & ~0xffu) | tb, (BC(u32, kv[4 * qq + 3]) & ~0xffu) | tb});
    }
    ST(S0 + 1);
    if (w == 0) {
      nap<D_KVP>();
      v4u t[8];
      STV(58 + LY, (fetch<8, 2>(r, t, B_KVP + b * 8192 + lane * 16, 1024, tagl & 0xffu)));
      ST(S0 + 3);
      float s[4];
#pragma unroll
      for (int i = 0; i < 4; ++i)
        s[i] = op(op(op(BC(float, t[0][i]) + BC(float, t[1][i])) + op(BC(float, t[2][i]) + BC(float, t[3][i]))) + op(op(BC(float, t[4][i]) + BC(float, t[5][i])) + op(BC(float, t[6][i]) + BC(float, t[7][i]))));
      swap32(s[0], s[2]); swap32(s[1], s[3]);
      float a = op(s[0] + s[2]), bb = op(s[1] + s[3]), a2 = a, b2 = bb;
      swap16(a, a2); swap16(bb, b2);
      u32 d0 = pk2(op(a + a2), op(bb + b2)), d1 = d0;
      swap32u(d0, d1);
      const u32 e0 = dppu<0x128>(d0), e1 = dppu<0x128>(d1);
      if (lane < 8) st16(r, B_KVR + LY * 32768 + b * 128 + lane * 16, v4u{d0, d1, e0, e1});
      arrive(L_C1 + 8);
    } else if (w == 1 && (b & 7) == 0) {
      nap<D_KVP>();
      v4u t[4];
      fetch<4, 2>(r, t, B_ZP + (b >> 3) * 4096 + lane * 16, 1024, tagl & 0xffu);
      float s[4];
#pragma unroll
      for (int i = 0; i < 4; ++i) s[i] = op(op(BC(float, t[0][i]) + BC(float, t[1][i])) + op(BC(float, t[2][i]) + BC(float, t[3][i])));
      swap32(s[0], s[2]); swap32(s[1], s[3]);
      float a = op(s[0] + s[2]), bb = op(s[1] + s[3]), a2 = a, b2 = bb;
      swap16(a, a2); swap16(bb, b2);
      a = op(a + a2); bb = op(bb + b2);
      a = op(a + dppf<0x128>(a)); bb = op(bb + dppf<0x128>(bb));
      u32 d0 = pk2(a, bb), d1 = d0;
      swap32u(d0, d1);
      const u32 e0 = dppu<0xb1>(d0), e1 = dppu<0xb1>(d1);
      if (lane < 8 && !(lane & 1)) st16(r, B_ZR + LY * 4096 + (b >> 3) * 128 + lane * 8, v4u{d0, d1, e0, e1});
    }
  } else {
    const int hp = (w >> 1) & 1, kh = w & 1;
    LAS unsigned char* wb = L + R1A + (w - 4) * 16384 + (4 * h + q) * 64 + 32 * (G & 1) + 8 * pp;
    if (LY == 0) {
      stage64(p.wqkv, kh * 256 * 3072 + cg * 128 + hp * 64, 3072, w - 4, lane);
      const rsrc_t rw = __builtin_amdgcn_make_buffer_rsrc((void*)p.wo, 0, 512 * 1024, 0x00020000);
#pragma unroll
      for (int i = 0; i < 16; ++i) W[16 + i] = __builtin_amdgcn_raw_buffer_load_b128(rw, (256 + (lane >> 2)) * 1024 + cq * 256 + (w - 4) * 64 + (lane & 3) * 16, i * 16384, 0);
      VMW(16);
      ST(S1 + 0);
#pragma unroll
      for (int i = 0; i < 16; ++i) W[i] = trd2(wb + i * 1024, 512);
      asm volatile("s_waitcnt lgkmcnt(0)" ::: "memory");
      stage64(p.wo, cq * 256 + (w - 4) * 64, 1024, w - 4, lane);
    }
    ST(S1 + 1);
    until<1>(L_C1 + 16, C4);
    ST(S1 + 2);
    if (LY == 0) csf = colsum<0, 16>(W);
    const v4u cs4 = {csf.x, csf.y, 0u, 0u};
    u32 mf; float rs;
    row_stats(L, kh ^ 1, lane, mf, rs);
    f32x16 acco = mfma32(cs4, v4u{mf, mf, 0u, 0u}, zero);
    row_stats(L, kh, lane, mf, rs);
    f32x16 acck = mfma32(cs4, v4u{mf, mf, 0u, 0u}, zero);
    gemm<16>(acco, W, L + R2 + (kh ^ 1) * 32768 + kh * 16384 + lane * 16);
    gemm<16>(acck, W, L + R2 + kh * 32768 + kh * 16384 + lane * 16);
    ST(S1 + 3);
#pragma unroll
    for (int i = 0; i < 4; ++i) *(LAS f32x4*)(L + L_EX + ((w - 4) * 4 + i) * 1024 + lane * 16) = f32x4{acco[4 * i], acco[4 * i + 1], acco[4 * i + 2], acco[4 * i + 3]};
    arrive(L_C1);
    until(L_C1, C4);
    ST(S1 + 5);
#pragma unroll
    for (int i = 0; i < 4; ++i) {
      const f32x4 t = *(LAS f32x4*)(L + L_EX + (((w - 4) ^ 1) * 4 + i) * 1024 + lane * 16);
      acck[4 * i] += t.x; acck[4 * i + 1] += t.y; acck[4 * i + 2] += t.z; acck[4 * i + 3] += t.w;
    }
    f32x16 pr = mfma32(n < 16 ? wph : z4, pack8(acck, 0), zero);
    pr = mfma32(n < 16 ? z4 : wph, pack8(acck, 1), pr);
    v4u ph[4], kr[4];
    dual_softmax(pr, 0, rs * LOG2E, ph[0], ph[1]);
    dual_softmax(pr, 8, rs * LOG2E, ph[2], ph[3]);
    PIN(ph[0]); PIN(ph[1]); PIN(ph[2]); PIN(ph[3]);
    ST(S0 + 1);
    until<1>(L_C1 + 8, LY + 1);
    ST(S0 + 3);
    nap<D_KVR>();
    {
      const int hd = 4 * cg + 2 * hp;
      const int o0 = n < 16 ? B_KVR + LY * 32768 + hd * 1024 + h * 256 + n * 16 : B_ZR + LY * 4096 + hd * 128 + h * 16;
      const int hs = n < 16 ? 1024 : 128, ss = n < 16 ? 512 : 32;
      u32 spins = 0;
#pragma nounroll
      for (;; ++spins) {
        FENCE();
        u32 a = 0u;
#pragma unroll
        for (int i = 0; i < 4; ++i) kr[i] = ld16(r, o0 + (i >> 1) * hs + (i & 1) * ss);
#pragma unroll
        for (int i = 0; i < 4; ++i) a = max(max(a, max(kr[i].x, kr[i].y)), max(kr[i].z, kr[i].w));
        if (all_lanes(a != ~0u) || spins > SPIN_CAP) break;
        nap<D_RETRY>();
      }
      FENCE();
      STV(62 + LY, spins);
    }
    ST(S0 + 5);
    v4u of[2];
#pragma unroll
    for (int i = 0; i < 2; ++i) {
      f32x16 o = mfma32(kr[2 * i], ph[2 * i], zero);
      o = mfma32(kr[2 * i + 1], ph[2 * i + 1], o);
      const float inv = __builtin_amdgcn_rcpf(o[8] + 1e-6f);
      of[i] = v4u{pk2(o[0] * inv, o[1] * inv), pk2(o[2] * inv, o[3] * inv), pk2(o[4] * inv, o[5] * inv), pk2(o[6] * inv, o[7] * inv)};
    }
    const int fo = (rb * 2 + kh) * 32 + 4 * cg + 2 * hp;
    publish2(r, B_OI + fo * 1024 + lane * 16, B_OR + fo * 128 + lane * 4, of[0], of[1], tagl);
  }
  ST(S0 + 2);
  BAR();
  ST(S0 + 4);
  LAS unsigned char* ob = L + R2 + (hf ^ 1) * 32768 + lane * 16;
  {
    if (!KV && LY == 0) {
      LAS unsigned char* eb = L + L_EX + (w - 4) * 4096;
#pragma unroll
      for (int j = 0; j < 4; ++j) {
#pragma unroll
        for (int i = 0; i < 4; ++i) *(LAS v4u*)(eb + i * 1024 + lane * 16) = W[16 + 4 * j + i];
        FENCE();
#pragma unroll
        for (int i = 0; i < 4; ++i) W[16 + 4 * j + i] = trd2(eb + i * 1024 + (4 * h + q) * 64 + 32 * (G & 1) + 8 * pp, 512);
        FENCE();
      }
    } else nap<D_OI>();
    u32 spins = 0;
    const int fo = (rb * 2 + hf) * 32 + 4 * w;
#pragma nounroll
    for (;; ++spins) {
      dma_run<true, 4>(p.ws, B_OI + fo * 1024 + lane * 16, 1024, R2 + (hf ^ 1) * 32768 + 4 * w * 1024);
      const u32 r0 = ld4(r, B_OR + fo * 128 + lane * 4), r1 = ld4(r, B_OR + fo * 128 + 256 + lane * 4);
      VMW(2);
      u32 a = 2u * tagl;
#pragma unroll
      for (int i = 0; i < 4; ++i) a = sum4(*(LAS v4u*)(ob + (4 * w + i) * 1024), a);
      FENCE();
      if (all_lanes(a == r0 + r1) || spins > SPIN_CAP) break;
      nap<D_RETRY>();
    }
    STV(48 + LY, spins);
  }
  BAR();
  ST(S0 + 6);
  if (!KV) {
    const int ct = w - 4, ks0 = 8 * cq + 2 * ct;
    LAS unsigned char* xb = L + R2 + (hf * 32 + ks0) * 1024 + lane * 16;
    LAS unsigned char* wb = L + R1A + ct * 16384 + (4 * h + q) * 64 + 32 * (G & 1) + 8 * pp;
    f32x16 acc = zero;
    v4u x0, x1;
    {
      constexpr int PD = 4;
      v4u o[PD], a[PD];
#pragma unroll
      for (int i = 0; i < PD; ++i) o[i] = *(LAS v4u*)(ob + (16 + i) * 1024);
      SB();
#pragma unroll
      for (int i = 0; i < 32; ++i) {
        acc = mfma32(i < 16 ? W[16 + (i & 15)] : a[i % PD], o[i % PD], acc);
        if (i + PD < 32) o[i % PD] = *(LAS v4u*)(ob + ((16 + i + PD) & 31) * 1024);
        if (i + PD >= 16 && i + PD < 32) a[i % PD] = trd2(wb + (i + PD - 16) * 1024, 512);
        if (i == 2) x0 = *(LAS v4u*)xb;
        if (i == 4) x1 = *(LAS v4u*)(xb + 1024);
        SB();
      }
    }
#pragma unroll
    for (int i = 0; i < 4; ++i) { acc[2 * i] += bflo(x0[i]); acc[2 * i + 1] += bfhi(x0[i]); acc[8 + 2 * i] += bflo(x1[i]); acc[9 + 2 * i] += bfhi(x1[i]); }
    ST(S0 + 7);
    if (LY == 0) {
      f32x2 s1 = {0.f, 0.f}, s2 = {0.f, 0.f};
#pragma unroll
      for (int i = 0; i < 8; ++i) { const f32x2 t = {acc[2 * i], acc[2 * i + 1]}; s1 += t; s2 += t * t; }
      float u1 = s1.x + s1.y, u2 = s2.x + s2.y;
      swap32(u1, u2);
      float t = u1 + u2, t2 = t;
      swap32(t, t2);
      const int hrb = rb * 2 + hf;
      if (lane < 32) st16(r, B_SP + (hrb * 16 + cq * 4 + ct) * 512 + n * 16, v4u{BC(u32, t), c.tag, BC(u32, t2), c.tag});
      const v4u y0 = *(LAS v4u*)(L + L_Y + ct * 2048 + lane * 16), y1 = *(LAS v4u*)(L + L_Y + ct * 2048 + 1024 + lane * 16);
      nap<D_SP>();
      v4u sp[8];
      STV(37, (fetch<8, 0>(r, sp, B_SP + (hrb * 16 + 8 * h) * 512 + n * 16, 512, c.tag)));
      ST(S0 + 8);
      u1 = 0.f; u2 = 0.f;
#pragma unroll
      for (int i = 0; i < 8; ++i) { u1 += BC(float, sp[i].x); u2 += BC(float, sp[i].z); }
      swap32(u1, u2);
      t = u1 + u2; t2 = t;
      swap32(t, t2);
      const float m = t * (1.f / 512.f), rs = __builtin_amdgcn_rsqf(mx3(t2 * (1.f / 512.f) - m * m, 0.f, 0.f) + 1e-5f);
      gate_publish(acc, rs, -m * rs, y0, y1, r, 1, rb, hf * 32 + ks0, lane, c.tag);
    } else {
      v4u a = pack8(acc, 0), d = pack8(acc, 1);
      u32 a0 = a.x, a1 = a.y, a2 = a.z, a3 = a.w, d0 = d.x, d1 = d.y, d2 = d.z, d3 = d.w;
      swap32u(a0, a2); swap32u(a1, a3); swap32u(d0, d2); swap32u(d1, d3);
      GAS unsigned char* o = (GAS unsigned char*)p.out + (rb * 64 + hf * 32 + n) * 1024 + cq * 256 + ct * 64 + h * 16;
      *(GAS v4u*)o = v4u{a0, a1, a2, a3};
      *(GAS v4u*)(o + 32) = v4u{d0, d1, d2, d3};
    }
  }
  if (LY == 0) {
    BAR();
    ST(S0 + 9);
    STV(38, load_image(p, r, L, 1, rb, w, lane, c.tag));
    BAR();
    ST(20);
  }
}

DI void stage_kv(const Params& p, int stage, int cg, int w, int lane) {
  const int ty = w >> 1, g = ((lane >> 4) & 1) ^ (2 * ty);
  dma_run<false, 16>(p.wqkv, (stage * 256 + (w & 1) * 128 + (lane >> 3)) * 3072 + (1 + ty) * 1024 + cg * 128 + (((lane & 7) ^ (2 * g)) * 16), 8 * 3072, R1A + w * 16384);
}

template <bool KV>
DI void program(const Params& p, Ctx c ST_PARAM) {
  LAS unsigned char* L = c.L;
  const rsrc_t r = c.r;
  u32 tag;
  asm volatile("s_load_dword %0, %1, 0x0" : "=s"(tag) : "s"(p.ws));
  const int b = c.b, w = c.w, lane = c.lane, t8 = (w & 3) * 64 + lane;
  const int x8 = b & 7, hi = b >> 3, rb = b & 31, cg = b >> 5, hf = cg & 1, cq = cg >> 1, pair = x8 * 32 + hi;
  const int G = lane >> 4, q = (lane >> 2) & 3, pp = lane & 3, h = lane >> 5, n = lane & 31;
  const v4u z4 = {0u, 0u, 0u, 0u}, s4 = {~0u, ~0u, ~0u, ~0u};
  v4u W[32];
  v2u csf = {0u, 0u};
  v4u wph;
#define LOAD_WPH() { \
    const int head = KV ? 4 * cg + w : 4 * cg + (w & 2) + (n >> 4); \
    const GAS u16* wp = (const GAS u16*)p.wphi + head * 256 + (4 * h) * 16 + (n & 15); \
    u32 e[8]; \
    _Pragma("unroll") for (int j = 0; j < 8; ++j) e[j] = wp[(8 * (j >> 2) + (j & 3)) * 16]; \
    wph = v4u{e[0] | (e[1] << 16), e[2] | (e[3] << 16), e[4] | (e[5] << 16), e[6] | (e[7] << 16)}; \
    PIN(wph); }
#define LOAD_WPH_ISSUE() \
    u32 we[8]; { \
    const GAS u16* wp = (const GAS u16*)p.wphi + (4 * cg + (w & 2) + (n >> 4)) * 256 + (4 * h) * 16 + (n & 15); \
    _Pragma("unroll") for (int j = 0; j < 8; ++j) we[j] = wp[(8 * (j >> 2) + (j & 3)) * 16]; }
#define LOAD_WPH_FINISH() { wph = v4u{we[0] | (we[1] << 16), we[2] | (we[3] << 16), we[4] | (we[5] << 16), we[6] | (we[7] << 16)}; PIN(wph); }
#define PREFETCH_KV() { const int line = hi * 256 + t8; pf = *(const GAS u32*)((const GAS unsigned char*)p.wqkv + (line >> 4) * 3072 + 1024 + (line & 15) * 128); }
  const int ypos = B_YI + ((w & 3) * 16 + (lane >> 3)) * 32768 + pair * 128 + (lane & 7) * 16;
  if (!KV) {
    const int ct = w - 4, ks0 = 8 * cq + 2 * ct;
    if (w == 4 && lane < 16) *(LAS u32*)(L + L_C0 + lane * 4) = 0u;
    if (w == 5 && n < 30) *(LAS v4u*)(L + L_U + h * 4608 + 4096 + n * 16) = z4;
    nap<D_H>();
    v4u hv[2]; u32 hn[2];
#pragma unroll
    for (int cc = 0; cc < 2; ++cc) {
      hv[cc] = *(const GAS v4u*)(p.h + pair * 4096 + cc * 2048 + t8 * 8);
      hn[cc] = *(const GAS u32*)(p.h + pair * 4096 + cc * 2048 + min(t8 * 8 + 8, 2040));
    }
#define Q_ROWS() _Pragma("unroll") for (int i = 0; i < 8; ++i) \
      dma16<false>(p.q, (rb * 64 + hf * 32 + ct * 8 + i) * 1024 + ((lane ^ ((ct * 8 + i) & 15)) * 16), L_QR + (ct * 8 + i) * 1024);
    LOAD_WPH_ISSUE();
    const u32 dd = *(const GAS u32*)(p.dv + 2 * pair);
    asm volatile("s_waitcnt lgkmcnt(0)\n\ts_barrier\n\ts_add_u32 %0, %0, 1" : "+s"(tag) :: "memory", "scc");
    c.tag = tag;
    ST(1);
#pragma unroll
    for (int cc = 0; cc < 2; ++cc) {
      LAS unsigned char* hb = L + L_H + cc * 8320 + t8 * 16 + 32;
      const v4u a = hv[cc];
      *(LAS v4u*)hb = a;
      *(LAS v4u*)(hb + 4160) = v4u{__builtin_amdgcn_alignbit(a.y, a.x, 16), __builtin_amdgcn_alignbit(a.z, a.y, 16), __builtin_amdgcn_alignbit(a.w, a.z, 16), __builtin_amdgcn_alignbit(hn[cc], a.w, 16)};
      if (t8 == 0) {
        *(LAS v4u*)(hb - 32) = z4; *(LAS v4u*)(hb - 16) = z4; *(LAS v4u*)(hb + 4160 - 32) = z4;
        *(LAS v4u*)(hb + 4160 - 16) = v4u{0u, 0u, 0u, a.x << 16};
      }
    }
    arrive(L_C0);
    LOAD_WPH_FINISH();
    ST(2);
    const int f1 = B_FL + ((8 * ct + (lane & 7)) * 8 + x8) * 128;
    v4u fl[1], u[2];
    nap<D_F1>();
    fl[0] = ld16(r, f1);
    FENCE();
    until(L_C0, 4);
    const int ch = (w >> 1) & 1, rho = w & 1, i16 = lane & 15;
    LAS unsigned char* ab = L + L_H + ch * 8320 + ((i16 & 1) ? 0 : 4160) + 2 * (((i16 + 1) & ~1) + 8 * G) + 256 * rho;
#pragma unroll
    for (int i = 0; i < 16; ++i) {
      LAS u32* ap = (LAS u32*)(ab + 512 * (i & 7) + 64 * (i >> 3));
      W[i] = v4u{ap[0], ap[1], ap[2], ap[3]};
    }
    FENCE();
    if (!all_lanes(fl[0].y == tag && fl[0].w == tag)) STV(34, (fetch<1, 0>(r, fl, f1, 0, tag)));
    FENCE();
    ST(3);
    nap<D_UT>();
#pragma unroll
    for (int i = 0; i < 2; ++i) u[i] = ld16(r, B_UT + pair * 8192 + ct * 2048 + lane * 16 + i * 1024);
    FENCE();
#pragma unroll
    for (int i = 16; i < 32; ++i) {
      LAS u32* ap = (LAS u32*)(ab + 512 * (i & 7) + 64 * (i >> 3));
      W[i] = v4u{ap[0], ap[1], ap[2], ap[3]};
    }
    FENCE();
    if (!all_lanes(max(max(max(u[0].x, u[0].y), max(u[0].z, u[0].w)), max(max(u[1].x, u[1].y), max(u[1].z, u[1].w))) != ~0u))
      STV(35, (fetch<2, 1>(r, u, B_UT + pair * 8192 + ct * 2048 + lane * 16, 1024, 0u)));
    ST(4);
#pragma unroll
    for (int i = 0; i < 2; ++i) {
      *(LAS v2u*)(L + L_U + 4088 - 8 * (ct * 128 + i * 64 + lane)) = v2u{u[i].x, u[i].y};
      *(LAS v2u*)(L + L_U + 4608 + 4088 - 8 * (ct * 128 + i * 64 + lane)) = v2u{u[i].z, u[i].w};
    }
    LAS unsigned char* bb = L + L_U + ch * 4608 + 4064 - 3584 - 32 * i16 + 16 * G + 256 * rho;
    LAS unsigned char* pb = L + L_P + ct * 8192 + G * 256 + ((i16 ^ (8 * (G >> 1)) ^ (4 * ch)) * 16);
    if (PRIO) __builtin_amdgcn_s_setprio(2);
    BAR();
    ST(5);
    f32x4 acc[8];
#pragma unroll
    for (int i = 0; i < 8; ++i) acc[i] = f32x4{0.f, 0.f, 0.f, 0.f};
    v4u B[8];
#pragma unroll
    for (int i = 0; i < 8; ++i) B[i] = *(LAS v4u*)(bb + 512 * (7 - i));
    SB();
    const int cc2 = (lane >> 2) & 1;
    const float dc = cc2 ? bfhi(dd) : bflo(dd);
    v4u ur[2];
    float y[2][8];
#pragma unroll
    for (int rr = 0; rr < 4; ++rr) {
#pragma unroll
      for (int m = 0; m < 8; ++m) {
#pragma unroll
        for (int nn = 0; nn + m < 8; ++nn) {
          acc[nn + m] = mfma16(W[8 * rr + nn], B[m], acc[nn + m]);
          if (rr == 3 && m >= 1 && nn == 0) *(LAS f32x4*)(pb + (m - 1) * 1024) = acc[m - 1];
          if (rr == 2 && m == 5 && (nn == 0 || nn == 1)) ur[nn] = *(LAS v4u*)(L + L_U + cc2 * 4608 + 4080 - 16 * (ct * 64 + nn * 32 + 4 * (lane >> 3) + (lane & 3)));
          if (rr == 3 && m < 4 && nn >= 1 && nn <= 4) {
            const int e = 4 * m + nn - 1, i = e >> 3, k = e & 7;
            y[i][k] = ((k & 1) ? bflo(ur[i][3 - (k >> 1)]) : bfhi(ur[i][3 - (k >> 1)])) * dc;
            PIN(y[i][k]);
          }
          SB();
        }
        if (rr < 3) B[m] = *(LAS v4u*)(bb + 512 * (7 - m) + 64 * (rr + 1));
        SB();
      }
    }
    *(LAS f32x4*)(pb + 7 * 1024) = acc[7];
    ST(6);
    arrive(L_C0 + 4);
    until(L_C0 + 4, 8);
    {
      const int o = lane & 1, j = 2 * (lane >> 3) + ((lane >> 1) & 1);
#pragma unroll
      for (int i = 0; i < 2; ++i) {
#pragma unroll
        for (int rh = 0; rh < 2; ++rh)
#pragma unroll
          for (int e = 0; e < 2; ++e) {
            const f32x4 t = *(LAS f32x4*)(L + L_P + ((cc2 * 2 + rh) * 8 + 2 * ct + i) * 1024 + (2 * o + e) * 256 + ((j ^ (8 * o) ^ (4 * cc2)) * 16));
            y[i][4 * e] += t.x; y[i][4 * e + 1] += t.y; y[i][4 * e + 2] += t.z; y[i][4 * e + 3] += t.w;
          }
        st16(r, ypos + i * 8 * 32768, v4u{pk2(y[i][0], y[i][1]), pk2(y[i][2], y[i][3]), pk2(y[i][4], y[i][5]), pk2(y[i][6], y[i][7])});
      }
    }
    ST(7);
    until(L_C0 + 28, 4);
    f32x16 x;
    {
      LAS unsigned char* qb = L + L_QR + n * 1024;
      float s1 = 0.f, s2 = 0.f;
#pragma unroll
      for (int i = 0; i < 8; ++i) acc_stats(*(LAS v4u*)(qb + ((16 * ct + ((8 * h + i) ^ (n & 15))) * 16)), s1, s2);
      *(LAS f32x2*)(L + L_S0 + ct * 512 + n * 16 + h * 8) = f32x2{s1, s2};
#pragma unroll
      for (int i = 0; i < 4; ++i) {
        const v2u t = *(LAS v2u*)(qb + (((2 * ks0 + i) ^ (n & 15)) * 16) + h * 8);
        x[4 * i] = bflo(t.x); x[4 * i + 1] = bfhi(t.x); x[4 * i + 2] = bflo(t.y); x[4 * i + 3] = bfhi(t.y);
      }
    }
    arrive(L_C0 + 32);
    ST(29);
    nap<D_YI>();
    until(L_C0 + 20, 4);
    ST(8);
    const int ysrc = B_YI + (rb * 2 + hf) * 32768 + ks0 * 1024 + lane * 16;
    dma_run<true, 2>(p.ws, ysrc, 1024, L_H + ct * 2048);
    until(L_C0 + 32, 4);
    float m, rs;
    row_moments(L + L_S0, lane, m, rs);
    PIN(x); PIN(m); PIN(rs);
    v4u y0, y1;
    u32 spins = 0;
#pragma nounroll
    for (;; ++spins) {
      DRAIN();
      LAS unsigned char* yb = L + L_H + ct * 2048 + (4 * h + q) * 64 + 32 * (G & 1) + 8 * pp;
      y0 = trd2(yb, 512); y1 = trd2(yb + 1024, 512);
      float s1 = 0.f;
#pragma unroll
      for (int i = 0; i < 4; ++i) { s1 = dot2(y0[i], y0[i], s1); s1 = dot2(y1[i], y1[i], s1); }
      if (all_lanes(s1 == s1) || spins > SPIN_CAP) break;
      nap<D_RETRY>();
      dma_run<true, 2>(p.ws, ysrc, 1024, L_H + ct * 2048);
    }
    STV(36, spins);
    ST(9);
    gate_publish(x, rs, m * rs, y0, y1, r, 0, rb, hf * 32 + ks0, lane, tag);
    *(LAS v4u*)(L + L_Y + ct * 2048 + lane * 16) = y0; *(LAS v4u*)(L + L_Y + ct * 2048 + 1024 + lane * 16) = y1;
    if (w == 4 && lane < 8) *(LAS u32*)(L + L_C1 + lane * 4) = 0u;
    if (PRIO) __builtin_amdgcn_s_setprio(0);
  } else {
#pragma unroll
    for (int i = 0; i < 2; ++i) {
      const int lr = 8 * (2 * w + i) + (lane >> 3);
      dma16<false>(p.k, (64 * hi + lr) * 1024 + x8 * 128 + (((lane & 7) ^ (((lr + 4) >> 2) & 7)) * 16), L_KT + 512 + (2 * w + i) * 1024);
      dma16<false>(p.v, (64 * hi + lr) * 1024 + x8 * 128 + (((lane & 7) ^ ((lr >> 2) & 7)) * 16), L_VT + (2 * w + i) * 1024);
    }
    if (lane < 32) {
      if (w == 0) {
        if (hi > 0) dma16<false>(p.k, (64 * hi - 4 + (lane >> 3)) * 1024 + x8 * 128 + (lane & 7) * 16, L_KT);
        else *(LAS v4u*)(L + L_KT + lane * 16) = z4;
      }
      if (w == 1) dma16<false>(p.shk, x8 * 512 + lane * 16, L_C0 - 512);
    }
    const int rq = lane & 15, upos = B_UT + (x8 * 32 + 4 * w + G) * 8192 + (16 * hi + rq) * 16;
    st16(r, upos, s4); st16(r, upos + 16 * 8192, s4);
    DRAIN();
    asm volatile("s_waitcnt lgkmcnt(0)\n\ts_barrier\n\ts_add_u32 %0, %0, 1" : "+s"(tag) :: "memory", "scc");
    c.tag = tag;
    if (w == 3 && lane == 0) st16(r, B_FL + b * 128, v4u{tag, tag, tag, tag});
    ST(1);
#pragma unroll
    for (int it = 0; it < 2; ++it) {
      const int ck = w + 4 * it;
      const v4u sk = *(LAS v4u*)(L + L_C0 - 512 + (4 * ck + G) * 16);
      LAS unsigned char* ka = L + L_KT + (4 * rq) * 128 + ((ck ^ (rq & 7)) * 16) + G * 4;
      LAS unsigned char* kb = L + L_KT + (4 * rq + 4) * 128 + ((ck ^ ((rq + 1) & 7)) * 16) + G * 4;
      LAS unsigned char* va = L + L_VT + (4 * rq) * 128 + ((ck ^ (rq & 7)) * 16) + G * 4;
      u32 kk[7], vv[4];
#pragma unroll
      for (int i = 0; i < 3; ++i) kk[i] = *(LAS u32*)(ka + (i + 1) * 128);
#pragma unroll
      for (int i = 0; i < 4; ++i) { kk[3 + i] = *(LAS u32*)(kb + i * 128); vv[i] = *(LAS u32*)(va + i * 128); }
      const f32x2 w0 = {bflo(sk.x), bflo(sk.z)}, w1 = {bfhi(sk.x), bfhi(sk.z)}, w2 = {bflo(sk.y), bflo(sk.w)}, w3 = {bfhi(sk.y), bfhi(sk.w)};
      f32x2 kf[7], u[4];
#pragma unroll
      for (int i = 0; i < 7; ++i) kf[i] = f32x2{bflo(kk[i]), bfhi(kk[i])};
#pragma unroll
      for (int i = 0; i < 4; ++i) u[i] = (kf[i] * w3 + kf[i + 1] * w2 + kf[i + 2] * w1 + kf[i + 3] * w0) * f32x2{bflo(vv[i]), bfhi(vv[i])};
      st16(r, upos + it * 16 * 8192, v4u{pk2(u[3].x, u[2].x), pk2(u[1].x, u[0].x), pk2(u[3].y, u[2].y), pk2(u[1].y, u[0].y)});
    }
    ST(2);
#define CLEARS() { \
      st16(r, ypos, s4); st16(r, ypos + 8 * 32768, s4); \
      const bool kvr = w < 2, on = lane < 8 && (kvr || (b & 7) == 0); \
      if (on) st16(r, (kvr ? B_KVR + w * 32768 + b * 128 : B_ZR + (w - 2) * 4096 + (b >> 3) * 128) + lane * 16, s4); \
      FENCE(); }
#define CLEARED() { \
      arrive(L_C0 + 4); \
      arrive(L_C0 + 24); \
      if (w == 3) { \
        until(L_C0 + 24, 4); \
        if (lane == 0) st16(r, B_FL + 32768 + b * 128, v4u{tag, tag, tag, tag}); \
      } }
    LOAD_WPH();
    nap<D_CL>();
    CLEARS();
    u32 pf = 0;
    if (PF_W == 1) PREFETCH_KV();
    ST(3);
    BAR();
    ST(4);
    DRAIN();
    CLEARED();
    {
      const int ct = w;
      Q_ROWS();
    }
    if (PF_W == 2) PREFETCH_KV();
    stage_kv(p, 0, cg, w, lane);
    VMW(PF_W == 2 ? 17 : 16);
    arrive(L_C0 + 28);
    {
      const rsrc_t rw = __builtin_amdgcn_make_buffer_rsrc((void*)p.wqkv, 0, 512 * 3072, 0x00020000);
      const int src = (256 + (w & 1) * 128 + (lane >> 3)) * 3072 + (1 + (w >> 1)) * 1024 + cg * 128 + (((lane & 7) ^ (2 * ((G & 1) ^ (w & 2)))) * 16);
#pragma unroll
      for (int i = 0; i < 16; ++i) W[16 + i] = __builtin_amdgcn_raw_buffer_load_b128(rw, src, i * 8 * 3072, 0);
      FENCE();
    }
    VMW(16);
    arrive(L_C0 + 12);
    {
      v4u fl[1];
      const int pr = 64 * cq + 16 * w + (lane & 15);
      STV(39, (fetch<1, 0>(r, fl, B_FL + 32768 + ((pr & 31) * 8 + (pr >> 5)) * 128, 0, tag)));
      arrive(L_C0 + 20);
    }
    asm volatile("" ::"v"(pf));
    until<1>(L_C0 + 12, 4);
    ST(5);
    LAS unsigned char* wb = L + R1A + (G & 1) * 32768 + (4 * h + q) * 128 + ((w ^ (q >> 1) ^ (2 * (G & 1))) * 32) + 8 * pp;
#pragma unroll
    for (int i = 0; i < 16; ++i) W[i] = trd2(wb + i * 2048, 1024);
    arrive(L_C0 + 8);
    until<1>(L_C0 + 8, 4);
    ST(6);
#pragma unroll
    for (int i = 0; i < 16; ++i) *(LAS v4u*)(L + R1A + (w * 16 + i) * 1024 + lane * 16) = W[16 + i];
    arrive(L_C0 + 16);
    until<1>(L_C0 + 16, 4);
    ST(7);
#pragma unroll
    for (int i = 0; i < 16; ++i) W[16 + i] = trd2(wb + i * 2048, 1024);
    until<1>(L_C0 + 32, 4);
    csf = colsum<0, 32>(W);
    ST(9);
  }
  BAR();
  ST(28);
  STV(57, load_image(p, r, L, 0, rb, w, lane, tag));
  BAR();
  ST(10);
  layer<0, KV>(p, c, W, csf, wph ST_ARG);
  if (KV && b == 0 && w == 0 && lane == 0) __builtin_amdgcn_raw_buffer_store_b32(tag + 1u, r, WS_EPOCH, 0, SC1);
  layer<1, KV>(p, c, W, csf, wph ST_ARG);
}

__global__ void __launch_bounds__(512, 2) fused_kernel(Params p) {
  extern __shared__ __attribute__((aligned(16))) unsigned char smem[];
  LAS unsigned char* L = (LAS unsigned char*)smem;
  const int tid = threadIdx.x, lane = tid & 63, w = __builtin_amdgcn_readfirstlane(tid >> 6), b = blockIdx.x;
  const rsrc_t r = __builtin_amdgcn_make_buffer_rsrc((void*)p.ws, 0, WS_BYTES, 0x00020000);
  ST(0);
  STC(32);
  asm volatile("" ::"s"(p.q), "s"(p.k), "s"(p.v), "s"(p.shk), "s"(p.h), "s"(p.ws));
  const Ctx c = {L, r, 0u, b, w, lane};
  if (w >= 4) program<false>(p, c ST_ARG);
  else program<true>(p, c ST_ARG);
  ST(30);
  STC(33);
  ST_FLUSH();
}

extern "C" void kernel_launch(void* const* d_in, const int* in_sizes, int n_in, void* d_out, int out_size, void* d_ws, size_t ws_size,
                              hipStream_t stream) {
  Params p;
  p.q = (const u16*)d_in[0]; p.k = (const u16*)d_in[1]; p.v = (const u16*)d_in[2]; p.shk = (const u16*)d_in[3];
  p.h = (const u16*)d_in[4]; p.dv = (const u16*)d_in[5]; p.wqkv = (const u16*)d_in[7]; p.wphi = (const u16*)d_in[8];
  p.wo = (const u16*)d_in[9]; p.out = (u16*)d_out; p.ws = (unsigned char*)d_ws + (ws_size - WS_BYTES);
  (void)hipFuncSetAttribute((const void*)fused_kernel, hipFuncAttributeMaxDynamicSharedMemorySize, LDS_BYTES);
  hipLaunchKernelGGL(fused_kernel, dim3(256), dim3(512), LDS_BYTES, stream, p);
}
```

```cpp
#include <hip/hip_runtime.h>
#include <hip/hip_bf16.h>

typedef unsigned short u16;
typedef unsigned u32;
typedef unsigned long long u64;
typedef u32 v4u __attribute__((ext_vector_type(4)));
typedef u32 v2u __attribute__((ext_vector_type(2)));
typedef short v4s __attribute__((ext_vector_type(4)));
typedef float f32x2 __attribute__((ext_vector_type(2)));
typedef float f32x4 __attribute__((ext_vector_type(4)));
typedef float f32x16 __attribute__((ext_vector_type(16)));
typedef __bf16 v8bf __attribute__((ext_vector_type(8)));
typedef __bf16 v2bf __attribute__((ext_vector_type(2)));
#define LAS __attribute__((address_space(3)))
#define GAS __attribute__((address_space(1)))
#define DI __device__ __forceinline__
template <class T, class F> DI T bc(F x) { return __builtin_bit_cast(T, x); }
#define BC(T, x) bc<T>(x)
typedef __amdgpu_buffer_rsrc_t rsrc_t;

constexpr int MB = 1 << 20;
constexpr int WS_EPOCH = 0;
constexpr int B_KVR = 4096;
constexpr int B_ZR = B_KVR + 64 * 1024;
constexpr int B_ZP = 256 * 1024;
constexpr int B_SP = 384 * 1024;
constexpr int B_OR = 640 * 1024;
constexpr int B_UR = 896 * 1024;
constexpr int B_YR = 1152 * 1024;
constexpr int B_XR = 1408 * 1024;
constexpr int B_UT = 2 * MB;
constexpr int B_YI = 4 * MB;
constexpr int B_STAMP = 6 * MB;
constexpr int WS_BYTES = 8 * MB;

constexpr int R1A = 0, R1B = 65536, R2 = 98304, LDS_BYTES = 163840;
constexpr int L_H = R2, L_U = R2 + 16640, L_P = R2 + 25856, L_S0 = R2 + 59136, L_C0 = R2 + 65536 - 64;
constexpr int L_KT = L_P, L_VT = L_P + 9216;
constexpr int L_QR = R1B;
constexpr int L_ST = R1B, L_EX = R1B + 4096, L_C1 = R1B + 20480, L_Y = R1B + 24576;

constexpr u32 SPIN_CAP = 1u << 14;
constexpr int SC1 = 16;
constexpr float LOG2E = 1.4426950408889634f;
constexpr u32 ONES = 0x3f803f80u;

#define D_UT 8
#define D_YI 10
#define D_KVP 12
#define D_OI 14
#define D_KVR 16
#define D_SP 10
#define D_XI 14
#define D_XQ 2
#define Q_LATE
#define OUT_AUX 16
#define PFA 0
#define D_PF 0
#define TLB 1
#define D_H 0
#define D_CL 8
#define PRIO 1
#define HOLD 1
#define TRIG 24
#define TOUCH 0
#define PF 1
#define D_RETRY 2

struct Params {
  const u16 *q, *k, *v, *shk, *h, *dv, *wqkv, *wphi, *wo;
  u16* out;
  unsigned char* ws;
};

DI float bflo(u32 x) { return BC(float, x << 16); }
DI float bfhi(u32 x) { return BC(float, x & 0xffff0000u); }
DI u32 pk2(float a, float b) { f32x2 p = {a, b}; return BC(u32, __builtin_convertvector(p, v2bf)); }
DI float op(float r) { asm("" : "+v"(r)); return r; }
DI float mx3(float a, float b, float c) { return __builtin_fmaxf(__builtin_fmaxf(a, b), c); }
DI f32x16 mfma32(v4u a, v4u b, f32x16 c) { return __builtin_amdgcn_mfma_f32_32x32x16_bf16(BC(v8bf, a), BC(v8bf, b), c, 0, 0, 0); }
DI f32x4 mfma16(v4u a, v4u b, f32x4 c) { return __builtin_amdgcn_mfma_f32_16x16x32_bf16(BC(v8bf, a), BC(v8bf, b), c, 0, 0, 0); }
DI float dot2(u32 a, u32 b, float c) { return __builtin_amdgcn_fdot2_f32_bf16(BC(v2bf, a), BC(v2bf, b), c, false); }
DI v2u trd(LAS unsigned char* p) { return BC(v2u, __builtin_amdgcn_ds_read_tr16_b64_v4i16((LAS v4s*)p)); }
DI v4u trd2(LAS unsigned char* p, int second) { v2u a = trd(p), b = trd(p + second); return v4u{a.x, a.y, b.x, b.y}; }
DI v4u pack8(const f32x16& x, int s) { return v4u{pk2(x[8 * s], x[8 * s + 1]), pk2(x[8 * s + 2], x[8 * s + 3]), pk2(x[8 * s + 4], x[8 * s + 5]), pk2(x[8 * s + 6], x[8 * s + 7])}; }
DI void swap32(float& a, float& b) { auto r = __builtin_amdgcn_permlane32_swap(BC(u32, a), BC(u32, b), false, false); a = BC(float, r[0]); b = BC(float, r[1]); }
DI void swap16(float& a, float& b) { auto r = __builtin_amdgcn_permlane16_swap(BC(u32, a), BC(u32, b), false, false); a = BC(float, r[0]); b = BC(float, r[1]); }
DI void swap32u(u32& a, u32& b) { auto r = __builtin_amdgcn_permlane32_swap(a, b, false, false); a = r[0]; b = r[1]; }
template <int CTRL> DI u32 dppu(u32 x) { return (u32)__builtin_amdgcn_update_dpp(0, (int)x, CTRL, 0xf, 0xf, true); }
template <int CTRL> DI float dppf(float x) { return BC(float, dppu<CTRL>(BC(u32, x))); }
DI bool all_lanes(bool ok) { return __builtin_amdgcn_ballot_w64(!ok) == 0ull; }
#define BAR() asm volatile("s_waitcnt lgkmcnt(0)\n\ts_barrier" ::: "memory")
#define DRAIN() asm volatile("s_waitcnt vmcnt(0)" ::: "memory")
#define VMW(n) asm volatile("s_waitcnt vmcnt(%0)" ::"n"(n) : "memory")
#define PIN(x) asm volatile("" : "+v"(x))
#define SB() __builtin_amdgcn_sched_barrier(0)
#define FENCE() asm volatile("" ::: "memory")
template <int I> struct IC { static constexpr int v = I; };
template <int I, int N, class F> DI void sfor(F&& f) { if constexpr (I < N) { f(IC<I>{}); sfor<I + 1, N>(f); } }
template <int N> DI void nap() { if (N > 0) __builtin_amdgcn_s_sleep(N); }

template <bool COH> DI void dma16(const void* base, u32 voff, u32 ldst) {
  u32 keep;
  ldst = __builtin_amdgcn_readfirstlane(ldst);
  if (COH) asm volatile("s_mov_b32 %0, m0\n\ts_mov_b32 m0, %3\n\ts_nop 0\n\tglobal_load_lds_dwordx4 %1, %2 sc1\n\ts_mov_b32 m0, %0" : "=&s"(keep) : "v"(voff), "s"(base), "s"(ldst) : "memory");
  else asm volatile("s_mov_b32 %0, m0\n\ts_mov_b32 m0, %3\n\ts_nop 0\n\tglobal_load_lds_dwordx4 %1, %2\n\ts_mov_b32 m0, %0" : "=&s"(keep) : "v"(voff), "s"(base), "s"(ldst) : "memory");
}
template <bool COH, int N, bool PAIRS = false> DI void dma_run(const void* base, u32 voff, u32 ss, u32 ldst) {
  u64 a = (u64)base;
  u32 keep;
  ldst = __builtin_amdgcn_readfirstlane(ldst);
  asm volatile("s_mov_b32 %0, m0\n\ts_mov_b32 m0, %1" : "=&s"(keep) : "s"(ldst));
#pragma unroll
  for (int i = 0; i < N; ++i) {
    if (COH) asm volatile("s_nop 0\n\tglobal_load_lds_dwordx4 %0, %1 sc1\n\ts_add_u32 m0, m0, 0x400" ::"v"(voff), "s"(a) : "memory", "scc");
    else asm volatile("s_nop 0\n\tglobal_load_lds_dwordx4 %0, %1\n\ts_add_u32 m0, m0, 0x400" ::"v"(voff), "s"(a) : "memory", "scc");
    a += PAIRS ? ((i & 1) ? ss - 1024u : 1024u) : ss;
  }
  asm volatile("s_mov_b32 m0, %0" ::"s"(keep));
}
DI v4u ld16(rsrc_t r, int off) { return __builtin_amdgcn_raw_buffer_load_b128(r, off, 0, SC1); }
DI u32 ld4(rsrc_t r, int off) { return __builtin_amdgcn_raw_buffer_load_b32(r, off, 0, SC1); }
DI u32 sum4(v4u x, u32 a) { return (a + x.x + x.y) + x.z + x.w; }
DI void st16(rsrc_t r, int off, v4u v) { __builtin_amdgcn_raw_buffer_store_b128(v, r, off, 0, SC1); }

template <int STEP = 1024> DI void publish2(rsrc_t r, int off, int roff, v4u a, v4u b, u32 tag) {
  st16(r, off, a); st16(r, off + STEP, b);
  __builtin_amdgcn_raw_buffer_store_b32(sum4(b, sum4(a, tag)), r, roff, 0, SC1);
}

DI void arrive(u32 ctr) {
  asm volatile("s_mov_b64 exec, 1\n\tds_add_u32 %0, %1\n\ts_mov_b64 exec, -1" ::"v"(ctr), "v"(1u) : "memory");
}
template <int NAP = 0> DI void until(u32 ctr, u32 target) {
  u32 t, u;
  if (NAP) asm volatile("s_branch 2f\n1:\n\ts_sleep %4\n2:\n\tds_read_b32 %0, %2\n\ts_waitcnt lgkmcnt(0)\n\tv_readfirstlane_b32 %1, %0\n\ts_cmp_lt_u32 %1, %3\n\ts_cbranch_scc1 1b" : "=&v"(t), "=&s"(u) : "v"(ctr), "s"(target), "n"(NAP) : "memory", "scc");
  else asm volatile("1:\n\tds_read_b32 %0, %2\n\ts_waitcnt lgkmcnt(0)\n\tv_readfirstlane_b32 %1, %0\n\ts_cmp_lt_u32 %1, %3\n\ts_cbranch_scc1 1b" : "=&v"(t), "=&s"(u) : "v"(ctr), "s"(target) : "memory", "scc");
}

DI v4u tag4(u32 t) { return v4u{t & 0xffu, (t + 64u) & 0xffu, (t + 128u) & 0xffu, (t + 192u) & 0xffu}; }
template <int N, int MODE> DI u32 fetch(rsrc_t r, v4u (&v)[N], int off, int stride, u32 tag) {
  u32 spins = 0;
#pragma nounroll
  for (;; ++spins) {
    FENCE();
#pragma unroll
    for (int i = 0; i < N; ++i) v[i] = ld16(r, off + i * stride);
    u32 a = 0u;
#pragma unroll
    for (int i = 0; i < N; ++i) {
      if (MODE == 0) a |= (v[i].y ^ tag) | (v[i].w ^ tag);
      if (MODE == 1) a = max(max(a, max(v[i].x, v[i].y)), max(v[i].z, v[i].w));
      if (MODE == 2) { v[i] ^= tag4(tag); a |= (v[i].x | v[i].y) | (v[i].z | v[i].w); }
    }
    const bool ok = MODE == 0 ? a == 0u : MODE == 1 ? a != ~0u : (a & 0xffu) == 0u;
    if (all_lanes(ok) || spins > SPIN_CAP) break;
    nap<D_RETRY>();
  }
  FENCE();
  return spins;
}

#define STV(i, v) (void)(v)
#define STC(i)
#define ST_PARAM
#define ST_ARG
#define ST_FLUSH()
#define ST(i)

DI void dual_softmax(const f32x16& x, int o, float c, v4u& plus, v4u& minus) {
  float mx = mx3(mx3(x[o], x[o + 1], x[o + 2]), mx3(x[o + 3], x[o + 4], x[o + 5]), mx3(x[o + 6], x[o + 7], x[o]));
  float nm = mx3(mx3(-x[o], -x[o + 1], -x[o + 2]), mx3(-x[o + 3], -x[o + 4], -x[o + 5]), mx3(-x[o + 6], -x[o + 7], -x[o]));
  swap32(mx, nm);
  float t = mx3(mx, nm, nm) * -c, t2 = t;
  swap32(t, t2);
  f32x2 ep[4], em[4];
  const f32x2 c2 = {c, c}, tp = {t, t}, tm = {t2, t2};
#pragma unroll
  for (int i = 0; i < 4; ++i) { const f32x2 v = {x[o + 2 * i], x[o + 2 * i + 1]}; ep[i] = __builtin_elementwise_fma(v, c2, tp); em[i] = __builtin_elementwise_fma(-v, c2, tm); }
#pragma unroll
  for (int i = 0; i < 4; ++i) {
    ep[i] = f32x2{__builtin_amdgcn_exp2f(ep[i].x), __builtin_amdgcn_exp2f(ep[i].y)};
    em[i] = f32x2{__builtin_amdgcn_exp2f(em[i].x), __builtin_amdgcn_exp2f(em[i].y)};
  }
  const f32x2 sp2 = (ep[0] + ep[1]) + (ep[2] + ep[3]), sm2 = (em[0] + em[1]) + (em[2] + em[3]);
  float sp = op(sp2.x + sp2.y), sm = op(sm2.x + sm2.y);
  swap32(sp, sm);
  float ip = __builtin_amdgcn_rcpf(sp + sm), im = ip;
  swap32(ip, im);
#pragma unroll
  for (int i = 0; i < 4; ++i) {
    plus[i] = BC(u32, __builtin_convertvector(ep[i] * ip, v2bf));
    minus[i] = BC(u32, __builtin_convertvector(em[i] * im, v2bf));
  }
}

struct Ctx {
  LAS unsigned char* L;
  rsrc_t r;
  u32 tag;
  int b, w, lane;
};

template <int N> DI void gemm(f32x16& acc, const v4u (&W)[32], LAS unsigned char* xb) {
  constexpr int PD = 4;
  v4u x[PD];
#pragma unroll
  for (int i = 0; i < PD; ++i) x[i] = *(LAS v4u*)(xb + i * 1024);
  SB();
#pragma unroll
  for (int i = 0; i < N; ++i) {
    const v4u cur = x[i % PD];
    acc = mfma32(W[i], cur, acc);
    if (i + PD < N) x[i % PD] = *(LAS v4u*)(xb + (i + PD) * 1024);
    SB();
  }
  PIN(acc);
}
DI void row_moments(LAS unsigned char* st, int lane, float& m, float& rs) {
  LAS f32x4* s = (LAS f32x4*)(st + (lane & 31) * 16);
  const f32x4 a = s[0], b = s[32], c = s[64], d = s[96];
  m = op(op(op(a.x + a.z) + op(b.x + b.z)) + op(op(c.x + c.z) + op(d.x + d.z))) * (-1.f / 512.f);
  const float e2 = op(op(op(a.y + a.w) + op(b.y + b.w)) + op(op(c.y + c.w) + op(d.y + d.w))) * (1.f / 512.f);
  rs = __builtin_amdgcn_rsqf(mx3(__builtin_fmaf(-m, m, op(e2)), 0.f, 0.f) + 1e-5f);
}
DI void row_stats(LAS unsigned char* L, int T, int lane, u32& mf, float& rs) {
  LAS f32x4* s = (LAS f32x4*)(L + L_ST + T * 1024 + (lane & 31) * 16);
  const f32x4 a = s[0], b = s[32];
  const float m = op(op(a.x + a.y) + op(a.z + a.w)) * (-1.f / 512.f);
  const float e2 = op(op(b.x + b.y) + op(b.z + b.w)) * (1.f / 512.f);
  rs = __builtin_amdgcn_rsqf(mx3(__builtin_fmaf(-m, m, op(e2)), 0.f, 0.f) + 1e-5f);
  const float hi = bflo(pk2(m, m));
  mf = pk2(hi, op(m - hi));
}
struct Stat {
  f32x4 a, b;
  float m, e2, rs, hi;
  u32 mf;
  template <int S> DI void step(LAS unsigned char* s) {
    if constexpr (S == 0) { a = *(LAS f32x4*)s; b = *(LAS f32x4*)(s + 512); }
    if constexpr (S == 3) { m = op(a.x + a.y); hi = op(a.z + a.w); e2 = op(b.x + b.y); rs = op(b.z + b.w); }
    if constexpr (S == 4) { m = op(op(m + hi) * (-1.f / 512.f)); e2 = op(op(e2 + rs) * (1.f / 512.f)); }
    if constexpr (S == 5) { e2 = op(mx3(__builtin_fmaf(-m, m, e2), 0.f, 0.f) + 1e-5f); hi = op(bflo(pk2(m, m))); }
    if constexpr (S == 6) { rs = __builtin_amdgcn_rsqf(e2); mf = pk2(hi, op(m - hi)); PIN(rs); PIN(mf); }
  }
};
DI void acc_stats(v4u x, float& s1, float& s2) {
  s1 = dot2(x.x, ONES, s1); s2 = dot2(x.x, x.x, s2); s1 = dot2(x.y, ONES, s1); s2 = dot2(x.y, x.y, s2);
  s1 = dot2(x.z, ONES, s1); s2 = dot2(x.z, x.z, s2); s1 = dot2(x.w, ONES, s1); s2 = dot2(x.w, x.w, s2);
}
template <int N0, int N1> DI v2u colsum(const v4u (&W)[32]) {
  float cs = 0.f, cs2 = 0.f;
#pragma unroll
  for (int i = N0; i < N1; ++i) { cs = dot2(W[i].x, ONES, cs); cs2 = dot2(W[i].y, ONES, cs2); cs = dot2(W[i].z, ONES, cs); cs2 = dot2(W[i].w, ONES, cs2); }
  cs += cs2;
  const u32 hh = pk2(cs, cs);
  const float lo = cs - bflo(hh);
  v2u o = {hh, pk2(lo, lo)};
  PIN(o);
  return o;
}
DI u32 load_image(const Params& p, rsrc_t r, LAS unsigned char* L, int rb, int w, int lane, u32 tag) {
  const int f0 = w * 8;
  u32 spins = 0;
  v2u rec[4];
#pragma nounroll
  for (;; ++spins) {
    dma_run<true, 8, true>(p.ws, B_UT + (w & 3) * 524288 + (rb * 2 + (w >> 2)) * 2048 + lane * 16, 131072, R2 + f0 * 1024);
#pragma unroll
    for (int i = 0; i < 4; ++i) rec[i] = __builtin_amdgcn_raw_buffer_load_b64(r, B_XR + rb * 16384 + f0 * 256 + i * 512 + lane * 8, 0, SC1);
    u32 cs = 4u * tag;
    LAS v4u* x0 = (LAS v4u*)(L + R2 + f0 * 1024 + lane * 16);
    VMW(8); cs = sum4(x0[64], sum4(x0[0], cs)); cs = sum4(x0[192], sum4(x0[128], cs));
    VMW(4); cs = sum4(x0[320], sum4(x0[256], cs)); cs = sum4(x0[448], sum4(x0[384], cs));
    FENCE();
    if (all_lanes(cs + (rec[0].x + rec[1].x + rec[2].x + rec[3].x) == rec[0].y + rec[1].y + rec[2].y + rec[3].y) || spins > SPIN_CAP) break;
    nap<D_RETRY>();
  }
  *(LAS float*)(L + L_ST + (w >> 2) * 1024 + lane * 16 + (w & 3) * 4) = op(BC(float, rec[0].x) + BC(float, rec[1].x)) + op(BC(float, rec[2].x) + BC(float, rec[3].x));
  arrive(L_C1 + 20 + (w >> 2) * 4);
  return spins;
}
DI void gate_publish(f32x16& acc, float rs, float mr, v4u y0, v4u y1, rsrc_t r, int rb, int hf, int ks0, int lane, u32 tag) {
#pragma unroll
  for (int i = 0; i < 4; ++i) {
    acc[2 * i] = __builtin_fmaf(__builtin_fmaf(acc[2 * i], rs, mr), bflo(y0[i]), acc[2 * i]);
    acc[2 * i + 1] = __builtin_fmaf(__builtin_fmaf(acc[2 * i + 1], rs, mr), bfhi(y0[i]), acc[2 * i + 1]);
    acc[8 + 2 * i] = __builtin_fmaf(__builtin_fmaf(acc[8 + 2 * i], rs, mr), bflo(y1[i]), acc[8 + 2 * i]);
    acc[9 + 2 * i] = __builtin_fmaf(__builtin_fmaf(acc[9 + 2 * i], rs, mr), bfhi(y1[i]), acc[9 + 2 * i]);
  }
  const v4u a = pack8(acc, 0), b = pack8(acc, 1);
  const int off = B_UT + ks0 * 65536 + (rb * 2 + hf) * 2048 + lane * 16;
  st16(r, off, a); st16(r, off + 1024, b);
  FENCE();
  float s1 = 0.f, s2 = 0.f, t1 = 0.f, t2 = 0.f;
  acc_stats(a, s1, s2); acc_stats(b, t1, t2);
  s1 += t1; s2 += t2;
  swap32(s1, s2);
  const u32 st = BC(u32, s1 + s2);
  __builtin_amdgcn_raw_buffer_store_b64(v2u{st, sum4(b, sum4(a, tag + st))}, r, B_XR + rb * 16384 + (hf * 32 + ks0) * 256 + lane * 8, 0, SC1);
}

struct Post {
  v4u selp, selm, kb, vb, fp, fm, v0, v1;
  u32 ones;
  f32x16 pr, vt, pt;
  float vs[8], ep[8], em[8], mx, nm, t, t2, cc, s0, s1, s2, s3;
  DI void init(int n, int h) {
    const int n16 = n & 15, j = 4 * (n16 >> 3) + (n16 & 3);
    const u32 one = (((n16 >> 2) & 1) == h) ? (0x3f80u << (16 * (j & 1))) : 0u;
    const u32 op_ = n < 16 ? one : 0u, om = n < 16 ? 0u : one;
    selp = v4u{(j >> 1) == 0 ? op_ : 0u, (j >> 1) == 1 ? op_ : 0u, (j >> 1) == 2 ? op_ : 0u, (j >> 1) == 3 ? op_ : 0u};
    selm = v4u{(j >> 1) == 0 ? om : 0u, (j >> 1) == 1 ? om : 0u, (j >> 1) == 2 ? om : 0u, (j >> 1) == 3 ? om : 0u};
    ones = n < 16 ? 0u : ONES;
    PIN(selp); PIN(selm); PIN(ones);
  }
  template <int S> DI void step(const f32x16& a, float rs, v4u wph, f32x16& kv, bool first) {
    f32x16 zero;
#pragma unroll
    for (int i = 0; i < 16; ++i) zero[i] = 0.f;
    if constexpr (S == 0) { kb = pack8(a, 0); cc = rs * LOG2E; }
    if constexpr (S == 1) { pr = mfma32(wph, kb, zero); _Pragma("unroll") for (int i = 0; i < 8; ++i) vs[i] = op(a[8 + i] * rs); }
    if constexpr (S == 2) vb = v4u{pk2(vs[0], vs[1]), pk2(vs[2], vs[3]), pk2(vs[4], vs[5]), pk2(vs[6], vs[7])};
    if constexpr (S == 3) {
      PIN(pr);
      vt = mfma32(vb, selp, zero);
      mx = mx3(mx3(pr[0], pr[1], pr[2]), mx3(pr[3], pr[4], pr[5]), mx3(pr[6], pr[7], pr[0]));
      nm = mx3(mx3(-pr[0], -pr[1], -pr[2]), mx3(-pr[3], -pr[4], -pr[5]), mx3(-pr[6], -pr[7], -pr[0]));
    }
    if constexpr (S == 4) { swap32(mx, nm); t = mx3(mx, nm, nm) * -cc; t2 = t; }
    if constexpr (S == 5) { swap32(t, t2); v0.x = pk2(vt[0], vt[1]) | ones; }
    if constexpr (S == 6) _Pragma("unroll") for (int i = 0; i < 5; ++i) ep[i] = op(__builtin_fmaf(pr[i], cc, t));
    if constexpr (S == 7) { _Pragma("unroll") for (int i = 5; i < 8; ++i) ep[i] = op(__builtin_fmaf(pr[i], cc, t)); _Pragma("unroll") for (int i = 0; i < 2; ++i) em[i] = op(__builtin_fmaf(pr[i], -cc, t2)); }
    if constexpr (S == 8) _Pragma("unroll") for (int i = 2; i < 7; ++i) em[i] = op(__builtin_fmaf(pr[i], -cc, t2));
    if constexpr (S == 9) { em[7] = op(__builtin_fmaf(pr[7], -cc, t2)); ep[0] = __builtin_amdgcn_exp2f(ep[0]); ep[1] = __builtin_amdgcn_exp2f(ep[1]); }
    if constexpr (S == 10) { ep[2] = __builtin_amdgcn_exp2f(ep[2]); ep[3] = __builtin_amdgcn_exp2f(ep[3]); s0 = op(ep[0] + ep[1]); }
    if constexpr (S == 11) { ep[4] = __builtin_amdgcn_exp2f(ep[4]); ep[5] = __builtin_amdgcn_exp2f(ep[5]); s1 = op(ep[2] + ep[3]); }
    if constexpr (S == 12) { ep[6] = __builtin_amdgcn_exp2f(ep[6]); ep[7] = __builtin_amdgcn_exp2f(ep[7]); s0 = op(s0 + s1); }
    if constexpr (S == 13) { em[0] = __builtin_amdgcn_exp2f(em[0]); em[1] = __builtin_amdgcn_exp2f(em[1]); s1 = op(ep[4] + ep[5]); }
    if constexpr (S == 14) { em[2] = __builtin_amdgcn_exp2f(em[2]); em[3] = __builtin_amdgcn_exp2f(em[3]); s2 = op(ep[6] + ep[7]); }
    if constexpr (S == 15) { em[4] = __builtin_amdgcn_exp2f(em[4]); em[5] = __builtin_amdgcn_exp2f(em[5]); s1 = op(s1 + s2); }
    if constexpr (S == 16) { em[6] = __builtin_amdgcn_exp2f(em[6]); em[7] = __builtin_amdgcn_exp2f(em[7]); s0 = op(s0 + s1); }
    if constexpr (S == 17) { s1 = op(em[0] + em[1]); s2 = op(em[2] + em[3]); s3 = op(em[4] + em[5]); t = op(em[6] + em[7]); s1 = op(s1 + s2); }
    if constexpr (S == 18) { s3 = op(s3 + t); v0.y = pk2(vt[2], vt[3]) | ones; s1 = op(s1 + s3); }
    if constexpr (S == 19) { swap32(s0, s1); t = __builtin_amdgcn_rcpf(s0 + s1); }
    if constexpr (S == 20) { t2 = t; v0.z = pk2(vt[4], vt[5]) | ones; swap32(t, t2); }
    if constexpr (S == 21) _Pragma("unroll") for (int i = 0; i < 5; ++i) ep[i] = op(ep[i] * t);
    if constexpr (S == 22) { _Pragma("unroll") for (int i = 5; i < 8; ++i) ep[i] = op(ep[i] * t); fp.x = pk2(ep[0], ep[1]); fp.y = pk2(ep[2], ep[3]); }
    if constexpr (S == 23) { fp.z = pk2(ep[4], ep[5]); fp.w = pk2(ep[6], ep[7]); _Pragma("unroll") for (int i = 0; i < 3; ++i) em[i] = op(em[i] * t2); }
    if constexpr (S == 24) { pt = mfma32(fp, selp, zero); _Pragma("unroll") for (int i = 3; i < 8; ++i) em[i] = op(em[i] * t2); fm = v4u{pk2(em[0], em[1]), pk2(em[2], em[3]), pk2(em[4], em[5]), pk2(em[6], em[7])}; }
    if constexpr (S == 25) { pt = mfma32(fm, selm, pt); v0.w = pk2(vt[6], vt[7]) | ones; v1.x = pk2(vt[8], vt[9]) | ones; v1.y = pk2(vt[10], vt[11]) | ones; v1.z = pk2(vt[12], vt[13]) | ones; v1.w = pk2(vt[14], vt[15]) | ones; }
    if constexpr (S == 27) fp = pack8(pt, 0);
    if constexpr (S == 28) { kv = mfma32(fp, v0, first ? zero : kv); fm = pack8(pt, 1); }
    if constexpr (S == 29) kv = mfma32(fm, v1, kv);
  }
  DI void tail(const f32x16& a, float rs, v4u wph, f32x16& kv) {
    f32x16 zero;
#pragma unroll
    for (int i = 0; i < 16; ++i) zero[i] = 0.f;
    pr = mfma32(wph, pack8(a, 0), zero);
    vb = v4u{pk2(op(a[8] * rs), op(a[9] * rs)), pk2(op(a[10] * rs), op(a[11] * rs)), pk2(op(a[12] * rs), op(a[13] * rs)), pk2(op(a[14] * rs), op(a[15] * rs))};
    vt = mfma32(vb, selp, zero);
    dual_softmax(pr, 0, rs * LOG2E, fp, fm);
    pt = mfma32(fp, selp, zero);
    pt = mfma32(fm, selm, pt);
    v0 = pack8(vt, 0) | ones; v1 = pack8(vt, 1) | ones;
    kv = mfma32(pack8(pt, 0), v0, kv);
    kv = mfma32(pack8(pt, 1), v1, kv);
  }
};

template <int LY, bool KV>
DI void layer(const Params& p, const Ctx& c, v4u (&W)[32], v2u& csf, const v4u wph ST_PARAM) {
  LAS unsigned char* L = c.L;
  const rsrc_t r = c.r;
  int lane = c.lane;
  PIN(lane);
  const int b = c.b, w = c.w, rb = b & 31, cg = b >> 5, hf = cg & 1, cq = cg >> 1;
  const int G = lane >> 4, q = (lane >> 2) & 3, pp = lane & 3, h = lane >> 5, n = lane & 31;
  constexpr int S0 = LY ? 20 : 10, S1 = LY ? 50 : 40;
  constexpr u32 C4 = LY ? 8 : 4;
  const u32 tagl = c.tag + LY;
  const v4u z4 = {0u, 0u, 0u, 0u};
  f32x16 zero;
#pragma unroll
  for (int i = 0; i < 16; ++i) zero[i] = 0.f;
  if (KV) {
    const v4u cs4 = {csf.x, csf.y, 0u, 0u};
    f32x16 acc0 = zero, acc1 = zero, kv;
    Post ps;
    Stat s0, s1;
    ps.init(n, h);
    const int head = 4 * cg + w;
    int so = B_YI + rb * 65536 + (head * 8 + h * 2 + (n >> 3)) * 256 + (n & 7) * 16, so1 = B_ZP + (head * 32 + rb) * 128 + h * 32;
    PIN(so); PIN(so1);
    so = n < 16 ? so : so1;
    so1 = so + (n < 16 ? 128 : 16);
    int so2 = so + (n < 16 ? 1024 : 64), so3 = so + (n < 16 ? 1152 : 80);
    PIN(so); PIN(so1); PIN(so2); PIN(so3);
    nap<D_XI - 2>();
    STV(LY ? 38 : 57, load_image(p, r, L, rb, w, lane, tagl));
    ST(S0);
    {
      constexpr int PD = 4;
      LAS unsigned char* xb = L + R2 + lane * 16;
      LAS unsigned char* sb = L + L_ST + n * 16;
      v4u x[PD];
      u32 seen;
      until(L_C1 + 20, C4);
#pragma unroll
      for (int i = 0; i < PD; ++i) x[i] = *(LAS v4u*)(xb + i * 1024);
      SB();
      sfor<0, 64>([&](auto I) __attribute__((always_inline)) {
        constexpr int i = decltype(I)::v;
        const v4u cur = x[i % PD];
        if constexpr (i < 32) acc0 = mfma32(W[i & 31], cur, acc0); else acc1 = mfma32(W[i & 31], cur, acc1);
        if constexpr (i == 23) seen = *(volatile LAS u32*)(L + L_C1 + 24);
        if constexpr (i == 27) { if (__builtin_amdgcn_readfirstlane(seen) < C4) until(L_C1 + 24, C4); FENCE(); }
        if constexpr (i + PD < 64) x[i % PD] = *(LAS v4u*)(xb + (i + PD) * 1024);
        if constexpr (i >= 1 && i < 8) s0.template step<i - 1>(sb);
        if constexpr (i == 10) acc0 = mfma32(cs4, v4u{s0.mf, s0.mf, 0u, 0u}, acc0);
        if constexpr (i >= 27 && i < 34) s1.template step<i - 27>(sb + 1024);
        if constexpr (i == 60) acc1 = mfma32(cs4, v4u{s1.mf, s1.mf, 0u, 0u}, acc1);
        if constexpr (i >= 34) ps.template step<i - 34>(acc0, s0.rs, wph, kv, true);
        SB();
      });
    }
    ST(S1 + 2);
    ps.tail(acc1, s1.rs, wph, kv);
    if (n <= 16) {
      const v4u tb = tag4(tagl);
#define TG(i) __builtin_amdgcn_perm(BC(u32, kv[i]), tb[(i) & 3], 0x07060500u)
      st16(r, so, v4u{TG(0), TG(1), TG(2), TG(3)}); st16(r, so1, v4u{TG(4), TG(5), TG(6), TG(7)});
      st16(r, so2, v4u{TG(8), TG(9), TG(10), TG(11)}); st16(r, so3, v4u{TG(12), TG(13), TG(14), TG(15)});
    }
    ST(S0 + 1);
    arrive(L_C1 + 16);
    if (w == 0) {
      nap<D_KVP>();
      v4u t[8];
      STV(58 + LY, (fetch<8, 2>(r, t, B_YI + G * 65536 + b * 256 + (lane & 15) * 16, 262144, tagl)));
      ST(S0 + 3);
      float s[4];
#pragma unroll
      for (int i = 0; i < 4; ++i)
        s[i] = op(op(op(BC(float, t[0][i]) + BC(float, t[1][i])) + op(BC(float, t[2][i]) + BC(float, t[3][i]))) + op(op(BC(float, t[4][i]) + BC(float, t[5][i])) + op(BC(float, t[6][i]) + BC(float, t[7][i]))));
      swap32(s[0], s[2]); swap32(s[1], s[3]);
      float a = op(s[0] + s[2]), bb = op(s[1] + s[3]), a2 = a, b2 = bb;
      swap16(a, a2); swap16(bb, b2);
      u32 d0 = pk2(op(a + a2), op(bb + b2)), d1 = d0;
      swap32u(d0, d1);
      const u32 e0 = dppu<0x128>(d0), e1 = dppu<0x128>(d1);
      if (lane < 8) st16(r, B_KVR + LY * 32768 + b * 128 + lane * 16, v4u{d0, d1, e0, e1});
      nap<D_KVR>();
      arrive(L_C1 + 8);
    } else if (w == 1 && (b & 7) == 0) {
      nap<D_KVP>();
      v4u t[4];
      fetch<4, 2>(r, t, B_ZP + (b >> 3) * 4096 + lane * 16, 1024, tagl);
      float s[4];
#pragma unroll
      for (int i = 0; i < 4; ++i) s[i] = op(op(BC(float, t[0][i]) + BC(float, t[1][i])) + op(BC(float, t[2][i]) + BC(float, t[3][i])));
      swap32(s[0], s[2]); swap32(s[1], s[3]);
      float a = op(s[0] + s[2]), bb = op(s[1] + s[3]), a2 = a, b2 = bb;
      swap16(a, a2); swap16(bb, b2);
      a = op(a + a2); bb = op(bb + b2);
      a = op(a + dppf<0x128>(a)); bb = op(bb + dppf<0x128>(bb));
      u32 d0 = pk2(a, bb), d1 = d0;
      swap32u(d0, d1);
      const u32 e0 = dppu<0xb1>(d0), e1 = dppu<0xb1>(d1);
      if (lane < 8 && !(lane & 1)) st16(r, B_ZR + LY * 4096 + (b >> 3) * 128 + lane * 8, v4u{d0, d1, e0, e1});
    }
  } else {
    const int hp = (w >> 1) & 1, kh = w & 1;
    nap<D_XI + D_XQ>();
    STV(LY ? 38 : 57, load_image(p, r, L, rb, w, lane, tagl));
    ST(S0);
    if (LY == 0) {
      dma_run<false, 16>(p.wqkv, ((w - 4) * 128 + ((lane >> 2) & 7)) * 3072 + cg * 128 + h * 64 + pp * 16, 8 * 3072, R1A + (w - 4) * 16384);
      const rsrc_t rw = __builtin_amdgcn_make_buffer_rsrc((void*)p.wo, 0, 512 * 1024, 0x00020000);
      const int wsrc = q * 1024 + cq * 256 + G * 64 + pp * 16;
#pragma unroll
      for (int i = 0; i < 16; ++i) W[16 + i] = __builtin_amdgcn_raw_buffer_load_b128(rw, wsrc + (256 + (w - 4) * 32) * 1024, ((i >> 3) * 32 + (i & 7)) * 4096, 0);
      VMW(16);
      arrive(L_C1 + 4);
      until<1>(L_C1 + 4, 4);
      ST(S1 + 0);
      LAS unsigned char* wb = L + R1A + kh * 32768 + hp * 512 + (4 * h + q) * 64 + 32 * (G & 1) + 8 * pp;
#pragma unroll
      for (int i = 0; i < 16; ++i) W[i] = trd2(wb + i * 2048, 1024);
      arrive(L_C1 + 12);
      until<1>(L_C1 + 12, 4);
      dma_run<false, 16>(p.wo, wsrc + (w - 4) * 65536, 4096, R1A + (w - 4) * 16384);
    }
    ST(S1 + 1);
    until<1>(L_C1 + 16, C4);
    ST(S1 + 2);
    if (LY == 0) csf = colsum<0, 16>(W);
    const v4u cs4 = {csf.x, csf.y, 0u, 0u};
    u32 mf; float rs;
    row_stats(L, kh ^ 1, lane, mf, rs);
    f32x16 acco = mfma32(cs4, v4u{mf, mf, 0u, 0u}, zero);
    row_stats(L, kh, lane, mf, rs);
    f32x16 acck = mfma32(cs4, v4u{mf, mf, 0u, 0u}, zero);
    gemm<16>(acco, W, L + R2 + (kh ^ 1) * 32768 + kh * 16384 + lane * 16);
    gemm<16>(acck, W, L + R2 + kh * 32768 + kh * 16384 + lane * 16);
    ST(S1 + 3);
#pragma unroll
    for (int i = 0; i < 4; ++i) *(LAS f32x4*)(L + L_EX + ((w - 4) * 4 + i) * 1024 + lane * 16) = f32x4{acco[4 * i], acco[4 * i + 1], acco[4 * i + 2], acco[4 * i + 3]};
    arrive(L_C1);
    until(L_C1, C4);
    ST(S1 + 5);
#pragma unroll
    for (int i = 0; i < 4; ++i) {
      const f32x4 t = *(LAS f32x4*)(L + L_EX + (((w - 4) ^ 1) * 4 + i) * 1024 + lane * 16);
      acck[4 * i] += t.x; acck[4 * i + 1] += t.y; acck[4 * i + 2] += t.z; acck[4 * i + 3] += t.w;
    }
    f32x16 pr = mfma32(n < 16 ? wph : z4, pack8(acck, 0), zero);
    pr = mfma32(n < 16 ? z4 : wph, pack8(acck, 1), pr);
    v4u ph[4], kr[4];
    dual_softmax(pr, 0, rs * LOG2E, ph[0], ph[1]);
    dual_softmax(pr, 8, rs * LOG2E, ph[2], ph[3]);
    PIN(ph[0]); PIN(ph[1]); PIN(ph[2]); PIN(ph[3]);
    ST(S0 + 1);
    until(L_C1 + 8, LY + 1);
    ST(S0 + 3);
    {
      const int hd = 4 * cg + 2 * hp;
      const int o0 = n < 16 ? B_KVR + LY * 32768 + hd * 1024 + h * 256 + n * 16 : B_ZR + LY * 4096 + hd * 128 + h * 16;
      const int hs = n < 16 ? 1024 : 128, ss = n < 16 ? 512 : 32;
      u32 spins = 0;
#pragma nounroll
      for (;; ++spins) {
        FENCE();
        u32 a = 0u;
#pragma unroll
        for (int i = 0; i < 4; ++i) kr[i] = ld16(r, o0 + (i >> 1) * hs + (i & 1) * ss);
#pragma unroll
        for (int i = 0; i < 4; ++i) a = max(max(a, max(kr[i].x, kr[i].y)), max(kr[i].z, kr[i].w));
        if (all_lanes(a != ~0u) || spins > SPIN_CAP) break;
        nap<D_RETRY>();
      }
      FENCE();
      STV(62 + LY, spins);
    }
    ST(S0 + 5);
    v4u of[2];
#pragma unroll
    for (int i = 0; i < 2; ++i) {
      f32x16 o = mfma32(kr[2 * i], ph[2 * i], zero);
      o = mfma32(kr[2 * i + 1], ph[2 * i + 1], o);
      const float inv = __builtin_amdgcn_rcpf(o[8] + 1e-6f);
      of[i] = v4u{pk2(o[0] * inv, o[1] * inv), pk2(o[2] * inv, o[3] * inv), pk2(o[4] * inv, o[5] * inv), pk2(o[6] * inv, o[7] * inv)};
    }
    const int fo = (rb * 2 + kh) * 32 + 4 * cg + 2 * hp;
    publish2(r, B_YI + rb * 65536 + (2 * cg + hp) * 4096 + kh * 2048 + lane * 16, B_OR + fo * 128 + lane * 4, of[0], of[1], tagl);
  }
  ST(S0 + 2);
  BAR();
  ST(S0 + 4);
  LAS unsigned char* ob = L + R2 + (hf ^ 1) * 32768 + lane * 16;
  v4u x0, x1;
  if (KV) {
    nap<D_OI>();
    u32 spins = 0;
    const int fo = (rb * 2 + hf) * 32 + 8 * w;
#pragma nounroll
    for (;; ++spins) {
      dma_run<true, 8, true>(p.ws, B_YI + rb * 65536 + w * 16384 + hf * 2048 + lane * 16, 4096, R2 + (hf ^ 1) * 32768 + 8 * w * 1024);
      v4u rec;
#pragma unroll
      for (int i = 0; i < 4; ++i) rec[i] = ld4(r, B_OR + fo * 128 + i * 256 + lane * 4);
      u32 a = 4u * tagl;
      LAS v4u* o0 = (LAS v4u*)(ob + 8 * w * 1024);
      VMW(8); a = sum4(o0[64], sum4(o0[0], a)); a = sum4(o0[192], sum4(o0[128], a));
      VMW(4); a = sum4(o0[320], sum4(o0[256], a)); a = sum4(o0[448], sum4(o0[384], a));
      FENCE();
      if (all_lanes(a == sum4(rec, 0u)) || spins > SPIN_CAP) break;
      nap<D_RETRY>();
    }
    STV(48 + LY, spins);
  } else {
    LAS unsigned char* xb = L + R2 + (hf * 32 + 8 * cq + 2 * (w - 4)) * 1024 + lane * 16;
    x0 = *(LAS v4u*)xb; x1 = *(LAS v4u*)(xb + 1024);
    if (LY == 0) {
      LAS unsigned char* tb = L + R2 + hf * 32768;
#pragma unroll
      for (int j = 0; j < 2; ++j) {
        arrive(L_C1 + 28);
        until(L_C1 + 28, 4 + 8 * j);
#pragma unroll
        for (int i = 0; i < 8; ++i) *(LAS v4u*)(tb + ((w - 4) * 8 + i) * 1024 + lane * 16) = W[16 + 8 * j + i];
        arrive(L_C1 + 28);
        until(L_C1 + 28, 8 + 8 * j);
#pragma unroll
        for (int i = 0; i < 8; ++i) W[16 + 8 * j + i] = trd2(tb + i * 4096 + h * 1024 + (w - 4) * 256 + q * 64 + 32 * (G & 1) + 8 * pp, 2048);
      }
    }
  }
  f32x16 acc;
  if (!KV) {
#pragma unroll
    for (int i = 0; i < 4; ++i) { acc[2 * i] = bflo(x0[i]); acc[2 * i + 1] = bfhi(x0[i]); acc[8 + 2 * i] = bflo(x1[i]); acc[9 + 2 * i] = bfhi(x1[i]); }
    PIN(acc);
  }
  BAR();
  ST(S0 + 6);
  if (!KV) {
    const int ct = w - 4, ks0 = 8 * cq + 2 * ct;
    LAS unsigned char* wb = L + R1A + h * 1024 + ct * 256 + q * 64 + 32 * (G & 1) + 8 * pp;
    {
      constexpr int PD = 4;
      v4u o[PD], a[PD];
#pragma unroll
      for (int i = 0; i < PD; ++i) o[i] = *(LAS v4u*)(ob + (16 + i) * 1024);
      SB();
#pragma unroll
      for (int i = 0; i < 32; ++i) {
        acc = mfma32(i < 16 ? W[16 + (i & 15)] : a[i % PD], o[i % PD], acc);
        if (i + PD < 32) o[i % PD] = *(LAS v4u*)(ob + ((16 + i + PD) & 31) * 1024);
        if (i + PD >= 16 && i + PD < 32) a[i % PD] = trd2(wb + (i + PD - 16) * 4096, 2048);
        SB();
      }
    }
    PIN(acc);
    ST(S0 + 7);
    if (LY == 0) {
      f32x2 s1 = {0.f, 0.f}, s2 = {0.f, 0.f};
#pragma unroll
      for (int i = 0; i < 8; ++i) { const f32x2 t = {acc[2 * i], acc[2 * i + 1]}; s1 += t; s2 += t * t; }
      float u1 = s1.x + s1.y, u2 = s2.x + s2.y;
      swap32(u1, u2);
      float t = u1 + u2, t2 = t;
      swap32(t, t2);
      const int hrb = rb * 2 + hf;
      const v4u tb = tag4(c.tag);
      if (lane < 32) __builtin_amdgcn_raw_buffer_store_b64(v2u{(BC(u32, t) & ~0xffu) | tb.x, (BC(u32, t2) & ~0xffu) | tb.y}, r, B_SP + (hrb * 16 + cq * 4 + ct) * 256 + n * 8, 0, SC1);
      const v4u y0 = *(LAS v4u*)(L + L_Y + ct * 2048 + lane * 16), y1 = *(LAS v4u*)(L + L_Y + ct * 2048 + 1024 + lane * 16);
      nap<D_SP>();
      v2u sp[8];
      u32 spins = 0;
#pragma nounroll
      for (;; ++spins) {
        FENCE();
        u32 a = 0u;
#pragma unroll
        for (int i = 0; i < 8; ++i) sp[i] = __builtin_amdgcn_raw_buffer_load_b64(r, B_SP + (hrb * 16 + 8 * h + i) * 256 + n * 8, 0, SC1);
#pragma unroll
        for (int i = 0; i < 8; ++i) { sp[i].x ^= tb.x; sp[i].y ^= tb.y; a |= sp[i].x | sp[i].y; }
        if (all_lanes((a & 0xffu) == 0u) || spins > SPIN_CAP) break;
        nap<D_RETRY>();
      }
      STV(37, spins);
      ST(S0 + 8);
      u1 = 0.f; u2 = 0.f;
#pragma unroll
      for (int i = 0; i < 8; ++i) { u1 += BC(float, sp[i].x); u2 += BC(float, sp[i].y); }
      swap32(u1, u2);
      t = u1 + u2; t2 = t;
      swap32(t, t2);
      const float m = t * (1.f / 512.f), rs = __builtin_amdgcn_rsqf(mx3(t2 * (1.f / 512.f) - m * m, 0.f, 0.f) + 1e-5f);
      gate_publish(acc, rs, -m * rs, y0, y1, r, rb, hf, ks0, lane, c.tag + 1u);
    } else {
      LAS unsigned char* eb = L + L_EX + ct * 4096;
#pragma unroll
      for (int i = 0; i < 4; ++i) *(LAS v2u*)(eb + n * 64 + ((i ^ ((n >> 1) & 3)) * 16) + h * 8) = v2u{pk2(acc[4 * i], acc[4 * i + 1]), pk2(acc[4 * i + 2], acc[4 * i + 3])};
      FENCE();
      const v4u a = *(LAS v4u*)(eb + (lane >> 2) * 64 + (((lane & 3) ^ ((lane >> 3) & 3)) * 16)), d = *(LAS v4u*)(eb + 1024 + (lane >> 2) * 64 + (((lane & 3) ^ ((lane >> 3) & 3)) * 16));
      const rsrc_t ro = __builtin_amdgcn_make_buffer_rsrc((void*)p.out, 0, 2 * MB, 0x00020000);
      const int oo = (rb * 64 + hf * 32 + (lane >> 2)) * 1024 + cq * 256 + ct * 64 + (lane & 3) * 16;
      asm volatile(".Lfk_end:");
      __builtin_amdgcn_raw_buffer_store_b128(a, ro, oo, 0, OUT_AUX);
      __builtin_amdgcn_raw_buffer_store_b128(d, ro, oo + 16384, 0, OUT_AUX);
    }
  }
  if (LY == 0) {
    BAR();
    ST(S0 + 9);

  }
}

DI void stage_kv(const Params& p, int stage, int cg, int w, int lane) {
  const int ty = w >> 1, g = ((lane >> 4) & 1) ^ (2 * ty);
  dma_run<false, 16>(p.wqkv, (stage * 256 + (w & 1) * 128 + (lane >> 3)) * 3072 + (1 + ty) * 1024 + cg * 128 + (((lane & 7) ^ (2 * g)) * 16), 8 * 3072, R1A + w * 16384);
}

template <bool KV>
DI void program(const Params& p, Ctx c ST_PARAM) {
  LAS unsigned char* L = c.L;
  const rsrc_t r = c.r;
  u32 tag;
  asm volatile("s_load_dword %0, %1, 0x0" : "=s"(tag) : "s"(p.ws));
  const int b = c.b, w = c.w, lane = c.lane, t8 = (w & 3) * 64 + lane;
  const int x8 = b & 7, hi = b >> 3, rb = b & 31, cg = b >> 5, hf = cg & 1, cq = cg >> 1, pair = x8 * 32 + hi;
  const int G = lane >> 4, q = (lane >> 2) & 3, pp = lane & 3, h = lane >> 5, n = lane & 31;
  const v4u z4 = {0u, 0u, 0u, 0u}, s4 = {~0u, ~0u, ~0u, ~0u};
  v4u W[32];
  v2u csf = {0u, 0u};
  v4u wph;
#define WPH_ISSUE() \
    u32 we[8]; { \
    const GAS u16* wp = (const GAS u16*)p.wphi + (KV ? 4 * cg + w : 4 * cg + (w & 2) + (n >> 4)) * 256 + (4 * h) * 16 + (n & 15); \
    _Pragma("unroll") for (int j = 0; j < 8; ++j) we[j] = wp[(8 * (j >> 2) + (j & 3)) * 16]; }
#define WPH_FINISH() { wph = v4u{we[0] | (we[1] << 16), we[2] | (we[3] << 16), we[4] | (we[5] << 16), we[6] | (we[7] << 16)}; PIN(wph); }
#define BAR_A() { asm volatile("s_waitcnt lgkmcnt(0)\n\ts_barrier\n\ts_add_u32 %0, %0, 1" : "+s"(tag) :: "memory", "scc"); c.tag = tag; }
  const int yh = 2 * ((t8 >> 5) + 8 * ((t8 >> 3) & 3)) + ((t8 >> 2) & 1);
  const int ypos = B_YI + yh * 32768 + pair * 128 + (lane & 3) * 16;
  if (!KV) {
    const u32 pfc = c.tag;
    const int ct = w - 4, ks0 = 8 * cq + 2 * ct;
    if (w == 4 && lane < 16) *(LAS u32*)(L + L_C0 + lane * 4) = 0u;
    if (w == 5 && n < 30) *(LAS v4u*)(L + L_U + h * 4608 + 4096 + n * 16) = z4;
    nap<D_H>();
    v4u hv[2]; u32 hn[2];
#pragma unroll
    for (int cc = 0; cc < 2; ++cc) {
      hv[cc] = *(const GAS v4u*)(p.h + pair * 4096 + cc * 2048 + t8 * 8);
      hn[cc] = *(const GAS u32*)(p.h + pair * 4096 + cc * 2048 + min(t8 * 8 + 8, 2040));
    }
    WPH_ISSUE();
    const u32 dd = *(const GAS u32*)(p.dv + 2 * pair);
    u32 pfa = 0, pfb = 0, pft = 0;
    if (TLB && w == 5 && lane < 8) {
      const unsigned char* a = lane == 0 ? p.ws + 2048 : lane == 1 ? p.ws + B_UT + x8 * 262144 + hi * 128 : lane == 2 ? p.ws + B_YI + pair * 128 : lane == 3 ? (const unsigned char*)p.out + (rb * 64 + hf * 32) * 1024 : lane == 4 ? (const unsigned char*)p.wo :
                               lane == 5 ? (const unsigned char*)p.wqkv : lane == 6 ? (const unsigned char*)p.wqkv + MB : (const unsigned char*)p.q + (rb * 64 + hf * 32) * 1024;
      pft = __hip_atomic_load((const GAS u32*)a, __ATOMIC_RELAXED, __HIP_MEMORY_SCOPE_AGENT);
    }
#define WARM_W() { const int line = hi * 256 + t8; \
      if (PFA & 1) pfa = *(const GAS u32*)((const GAS unsigned char*)p.wqkv + (line >> 4) * 3072 + 1024 + (line & 15) * 128); \
      if (PFA & 2) pfb = *(const GAS u32*)(hi < 16 ? (const GAS unsigned char*)p.wqkv + (line >> 3) * 3072 + (line & 7) * 128 : (const GAS unsigned char*)p.wo + (line - 4096) * 128); \
      FENCE(); }
    if (!(PFA & 4)) { FENCE(); nap<D_PF>(); WARM_W(); }
    BAR_A();
    ST(1);
#pragma unroll
    for (int cc = 0; cc < 2; ++cc) {
      LAS unsigned char* hb = L + L_H + cc * 8320 + t8 * 16 + 32;
      const v4u a = hv[cc];
      *(LAS v4u*)hb = a;
      *(LAS v4u*)(hb + 4160) = v4u{__builtin_amdgcn_alignbit(a.y, a.x, 16), __builtin_amdgcn_alignbit(a.z, a.y, 16), __builtin_amdgcn_alignbit(a.w, a.z, 16), __builtin_amdgcn_alignbit(hn[cc], a.w, 16)};
      if (t8 == 0) {
        *(LAS v4u*)(hb - 32) = z4; *(LAS v4u*)(hb - 16) = z4; *(LAS v4u*)(hb + 4160 - 32) = z4;
        *(LAS v4u*)(hb + 4160 - 16) = v4u{0u, 0u, 0u, a.x << 16};
      }
    }
    arrive(L_C0);
    WPH_FINISH();
    asm volatile("" ::"v"(pfc), "v"(dd));
    if (PFA & 4) WARM_W();
    ST(2);
    if (PRIO) __builtin_amdgcn_s_setprio(2);
    until(L_C0, 8);
    const int ch = (w >> 1) & 1, rho = w & 1, i16 = lane & 15;
    LAS unsigned char* ab = L + L_H + ch * 8320 + ((i16 & 1) ? 0 : 4160) + 2 * (((i16 + 1) & ~1) + 8 * G) + 256 * rho;
#pragma unroll
    for (int i = 0; i < 16; ++i) {
      LAS u32* ap = (LAS u32*)(ab + 512 * (i & 7) + 64 * (i >> 3));
      W[i] = v4u{ap[0], ap[1], ap[2], ap[3]};
    }
    FENCE();
    nap<D_UT>();
    ST(3);
    const int usrc = B_UT + pair * 8192 + t8 * 16, urec = B_UR + ((t8 >> 3) * 8 + x8) * 1024 + (hi * 8 + (lane & 7)) * 4;
    v4u u0 = ld16(r, usrc), u1 = ld16(r, usrc + 4096);
    u32 ur_ = ld4(r, urec);
    FENCE();
    arrive(L_C0 + 24);
#pragma unroll
    for (int i = 16; i < 32; ++i) {
      LAS u32* ap = (LAS u32*)(ab + 512 * (i & 7) + 64 * (i >> 3));
      W[i] = v4u{ap[0], ap[1], ap[2], ap[3]};
    }
    FENCE();
    {
      u32 spins = 0;
#pragma nounroll
      while (!all_lanes(sum4(u1, sum4(u0, tag)) == ur_) && spins <= SPIN_CAP) {
        ++spins;
        FENCE();
        u0 = ld16(r, usrc); u1 = ld16(r, usrc + 4096); ur_ = ld4(r, urec);
      }
      STV(35, spins);
    }
    asm volatile("" ::"v"(pfa), "v"(pfb), "v"(pft));
    ST(4);
    *(LAS v4u*)(L + L_U + 4080 - 16 * t8) = u0;
    *(LAS v4u*)(L + L_U + 4608 + 4080 - 16 * t8) = u1;
    LAS unsigned char* bb = L + L_U + ch * 4608 + 4064 - 3584 - 32 * i16 + 16 * G + 256 * rho;
    LAS unsigned char* pb = L + L_P + ct * 8192 + G * 256 + ((i16 ^ (8 * (G >> 1))) * 16);
    LAS unsigned char* pb2 = L + L_P + ct * 8192 + G * 256 + ((i16 ^ (8 * (G >> 1)) ^ 4) * 16);
    arrive(L_C0 + 36);
    until(L_C0 + 36, 4);
    ST(5);
    f32x4 acc[8];
#pragma unroll
    for (int i = 0; i < 8; ++i) acc[i] = f32x4{0.f, 0.f, 0.f, 0.f};
    v4u B[8];
#pragma unroll
    for (int i = 0; i < 8; ++i) B[i] = *(LAS v4u*)(bb + 512 * (7 - i));
    SB();
    const int oct = 4 * yh + (lane & 3);
    v4u ur[2];
    float y[2][8];
#pragma unroll
    for (int rr = 0; rr < 4; ++rr) {
#pragma unroll
      for (int m = 0; m < 8; ++m) {
#pragma unroll
        for (int nn = 0; nn + m < 8; ++nn) {
          acc[nn + m] = mfma16(W[8 * rr + nn], B[m], acc[nn + m]);
          if (rr == 3 && m >= 1 && nn == 0) *(LAS f32x4*)((((m - 1) & 2) ? pb2 : pb) + (m - 1) * 1024) = acc[m - 1];
          if (rr == 2 && m == 5 && (nn == 0 || nn == 1)) ur[nn] = *(LAS v4u*)(L + L_U + nn * 4608 + 4080 - 16 * oct);
          if (rr == 3 && m < 4 && nn >= 1 && nn <= 4) {
            const int e = 4 * m + nn - 1, i = e >> 3, k = e & 7;
            y[i][k] = ((k & 1) ? bflo(ur[i][3 - (k >> 1)]) : bfhi(ur[i][3 - (k >> 1)])) * (i ? bfhi(dd) : bflo(dd));
            PIN(y[i][k]);
          }
          SB();
        }
        if (rr < 3) B[m] = *(LAS v4u*)(bb + 512 * (7 - m) + 64 * (rr + 1));
        SB();
      }
    }
    *(LAS f32x4*)(pb2 + 7 * 1024) = acc[7];
    ST(6);
    arrive(L_C0 + 4);
    until(L_C0 + 4, 8);
    {
      const int o = oct & 1, j = (oct >> 1) & 15;
      v4u yo[2];
#pragma unroll
      for (int i = 0; i < 2; ++i) {
#pragma unroll
        for (int rh = 0; rh < 2; ++rh)
#pragma unroll
          for (int e = 0; e < 2; ++e) {
            const f32x4 t = *(LAS f32x4*)(L + L_P + ((i * 2 + rh) * 8 + (oct >> 5)) * 1024 + (2 * o + e) * 256 + ((j ^ (8 * o) ^ ((oct >> 4) & 4)) * 16));
            y[i][4 * e] += t.x; y[i][4 * e + 1] += t.y; y[i][4 * e + 2] += t.z; y[i][4 * e + 3] += t.w;
          }
        yo[i] = v4u{pk2(y[i][0], y[i][1]), pk2(y[i][2], y[i][3]), pk2(y[i][4], y[i][5]), pk2(y[i][6], y[i][7])};
      }
      publish2<64>(r, ypos, B_YR + pair * 1024 + t8 * 4, yo[0], yo[1], tag);
    }
    ST(7);
    until(L_C0 + 28, 4);
    f32x16 x;
    {
      LAS unsigned char* qb = L + L_QR + n * 1024;
      float s1 = 0.f, s2 = 0.f;
#pragma unroll
      for (int i = 0; i < 8; ++i) acc_stats(*(LAS v4u*)(qb + ((16 * ct + ((8 * h + i) ^ (n & 15))) * 16)), s1, s2);
      *(LAS f32x2*)(L + L_S0 + ct * 512 + n * 16 + h * 8) = f32x2{s1, s2};
#pragma unroll
      for (int i = 0; i < 4; ++i) {
        const v2u t = *(LAS v2u*)(qb + (((2 * ks0 + i) ^ (n & 15)) * 16) + h * 8);
        x[4 * i] = bflo(t.x); x[4 * i + 1] = bfhi(t.x); x[4 * i + 2] = bflo(t.y); x[4 * i + 3] = bfhi(t.y);
      }
    }
    arrive(L_C0 + 32);
    ST(29);
    nap<D_YI>();
    ST(8);
    const int ysrc = B_YI + (rb * 2 + hf) * 32768 + ks0 * 1024 + lane * 16;
    const int yrec = B_YR + (8 * ks0 + (lane >> 2)) * 1024 + ((rb & 7) * 32 + (rb >> 3) * 8 + hf * 4 + (lane & 3)) * 4;
    dma_run<true, 2>(p.ws, ysrc, 1024, L_H + ct * 2048);
    u32 yr = ld4(r, yrec);
    FENCE();
    until(L_C0 + 32, 4);
    float m, rs;
    row_moments(L + L_S0, lane, m, rs);
    PIN(x); PIN(m); PIN(rs);
    v4u y0, y1;
    u32 spins = 0;
#pragma nounroll
    for (;; ++spins) {
      DRAIN();
      LAS unsigned char* yb = L + L_H + ct * 2048 + (4 * h + q) * 64 + 32 * (G & 1) + 8 * pp;
      LAS unsigned char* yc = L + L_H + ct * 2048 + (lane >> 2) * 128 + (lane & 3) * 16;
      const v4u c0 = *(LAS v4u*)yc, c1 = *(LAS v4u*)(yc + 64);
      y0 = trd2(yb, 512); y1 = trd2(yb + 1024, 512);
      if (all_lanes(sum4(c1, sum4(c0, tag)) == yr) || spins > SPIN_CAP) break;
      asm volatile("s_waitcnt lgkmcnt(0)" ::: "memory");
      nap<D_RETRY>();
      dma_run<true, 2>(p.ws, ysrc, 1024, L_H + ct * 2048);
      yr = ld4(r, yrec);
    }
    STV(36, spins);
    ST(9);
    gate_publish(x, rs, m * rs, y0, y1, r, rb, hf, ks0, lane, tag);
    *(LAS v4u*)(L + L_Y + ct * 2048 + lane * 16) = y0; *(LAS v4u*)(L + L_Y + ct * 2048 + 1024 + lane * 16) = y1;
    if (w == 4 && lane < 8) *(LAS u32*)(L + L_C1 + lane * 4) = 0u;
    if (PRIO) __builtin_amdgcn_s_setprio(0);
  } else {
#pragma unroll
    for (int i = 0; i < 2; ++i) {
      const int lr = 8 * (2 * w + i) + (lane >> 3);
      dma16<false>(p.k, (64 * hi + lr) * 1024 + x8 * 128 + (((lane & 7) ^ ((2 * w + i + 1) & 7)) * 16), L_KT + 1024 + (2 * w + i) * 1024);
      dma16<false>(p.v, (64 * hi + lr) * 1024 + x8 * 128 + (((lane & 7) ^ (2 * w + i)) * 16), L_VT + (2 * w + i) * 1024);
    }
    if (lane < 32) {
      if (w == 0) {
        if (hi > 0) dma16<false>(p.k, (64 * hi - 4 + (lane >> 3)) * 1024 + x8 * 128 + (lane & 7) * 16, L_KT + 512);
        else *(LAS v4u*)(L + L_KT + 512 + lane * 16) = z4;
      }
      if (w == 1) dma16<false>(p.shk, x8 * 512 + lane * 16, L_C0 - 512);
    }
    const int oc = lane & 7, pl = 8 * w + (lane >> 3);
    const int upos = B_UT + (x8 * 32 + pl) * 8192 + (8 * hi + oc) * 16, urec = B_UR + b * 1024 + t8 * 4;
    if (TOUCH & 1) {
      st16(r, upos, s4); st16(r, upos + 4096, s4);
      __builtin_amdgcn_raw_buffer_store_b32(0u, r, urec, 0, SC1);
      VMW(3);
    } else DRAIN();
    BAR_A();
    ST(1);
    {
      const int ck = pl >> 2;
      const v4u sk = *(LAS v4u*)(L + L_C0 - 512 + pl * 16);
      LAS unsigned char* ka = L + L_KT + oc * 1024 + ((ck ^ oc) * 16) + (pl & 3) * 4;
      LAS unsigned char* kb = L + L_KT + (oc + 1) * 1024 + ((ck ^ ((oc + 1) & 7)) * 16) + (pl & 3) * 4;
      LAS unsigned char* va = L + L_VT + oc * 1024 + ((ck ^ oc) * 16) + (pl & 3) * 4;
      u32 kk[11], vv[8];
#pragma unroll
      for (int i = 0; i < 3; ++i) kk[i] = *(LAS u32*)(ka + (i + 5) * 128);
#pragma unroll
      for (int i = 0; i < 8; ++i) { kk[3 + i] = *(LAS u32*)(kb + i * 128); vv[i] = *(LAS u32*)(va + i * 128); }
      const f32x2 w0 = {bflo(sk.x), bflo(sk.z)}, w1 = {bfhi(sk.x), bfhi(sk.z)}, w2 = {bflo(sk.y), bflo(sk.w)}, w3 = {bfhi(sk.y), bfhi(sk.w)};
      f32x2 kf[11], u[8];
#pragma unroll
      for (int i = 0; i < 11; ++i) kf[i] = f32x2{bflo(kk[i]), bfhi(kk[i])};
#pragma unroll
      for (int i = 0; i < 8; ++i) u[i] = (kf[i] * w3 + kf[i + 1] * w2 + kf[i + 2] * w1 + kf[i + 3] * w0) * f32x2{bflo(vv[i]), bfhi(vv[i])};
      if (TOUCH & 1) DRAIN();
      publish2<4096>(r, upos, urec, v4u{pk2(u[7].x, u[6].x), pk2(u[5].x, u[4].x), pk2(u[3].x, u[2].x), pk2(u[1].x, u[0].x)},
                     v4u{pk2(u[7].y, u[6].y), pk2(u[5].y, u[4].y), pk2(u[3].y, u[2].y), pk2(u[1].y, u[0].y)}, tag);
    }
    arrive(L_C0);
    ST(2);
    nap<D_CL>();
    {
      const bool kvr = w < 2, on = lane < 8 && (kvr || (b & 7) == 0);
      if (on) st16(r, (kvr ? B_KVR + w * 32768 + b * 128 : B_ZR + (w - 2) * 4096 + (b >> 3) * 128) + lane * 16, s4);
      const u32 nt = ~tag;
      const v4u n4 = {nt, nt, nt, nt};
      if (TOUCH & 2) { st16(r, ypos, n4); st16(r, ypos + 64, n4); }
      if (TOUCH & 4) {
        st16(r, (G == 0 ? B_XR + rb * 16384 + (hf * 32 + 8 * cq + 2 * w) * 256 : G == 1 ? B_OR + ((rb * 2 + (w & 1)) * 32 + 4 * cg + (w & 2)) * 128 :
                 G == 2 ? B_SP + ((rb * 2 + hf) * 16 + cq * 4 + w) * 256 : B_YR + pair * 1024 + w * 256) + (lane & 15) * 16, n4);
        if (lane < 8) st16(r, B_ZP + ((4 * cg + w) * 32 + rb) * 128 + lane * 16, n4);
      }
    }
    ST(3);
    until<1>(L_C0 + TRIG, 4);
    ST(4);
    u32 pf = 0;
    if (PF) {
      if (w == 0 && (PFA & 3) != 3) { const int line = b * 64 + lane;
        pf = *(const GAS u32*)(line < 12288 ? (const GAS unsigned char*)p.wqkv + line * 128 : (const GAS unsigned char*)p.wo + (line - 12288) * 128); }
      if (w == 1) pf = *(const GAS u32*)((const GAS unsigned char*)p.q + (rb * 64 + hf * 32 + cq * 8 + (lane >> 3)) * 1024 + (lane & 7) * 128);
      FENCE();
    }
#pragma unroll
    for (int i = 0; i < 8; ++i)
      dma16<false>(p.q, (rb * 64 + hf * 32 + w * 8 + i) * 1024 + ((lane ^ ((w * 8 + i) & 15)) * 16), L_QR + (w * 8 + i) * 1024);
    if (PF && (w == 1 || (w == 0 && (PFA & 3) != 3))) VMW(9); else VMW(8);
    arrive(L_C0 + 4);
    stage_kv(p, 0, cg, w, lane);
    VMW(16);
    arrive(L_C0 + 28);
    {
      const rsrc_t rw = __builtin_amdgcn_make_buffer_rsrc((void*)p.wqkv, 0, 512 * 3072, 0x00020000);
      const int src = (256 + (w & 1) * 128 + (lane >> 3)) * 3072 + (1 + (w >> 1)) * 1024 + cg * 128 + (((lane & 7) ^ (2 * ((G & 1) ^ (w & 2)))) * 16);
#pragma unroll
      for (int i = 0; i < 16; ++i) W[16 + i] = __builtin_amdgcn_raw_buffer_load_b128(rw, src, i * 8 * 3072, 0);
      FENCE();
    }
    VMW(16);
    arrive(L_C0 + 12);
    WPH_ISSUE();
    FENCE();
    until<1>(L_C0 + 12, 4);
    ST(5);
    if (HOLD == 1) until<2>(L_C0 + 4, 8);
    if (HOLD == 2) until<2>(L_C0 + 32, 4);
    LAS unsigned char* wb = L + R1A + (G & 1) * 32768 + (4 * h + q) * 128 + ((w ^ (q >> 1) ^ (2 * (G & 1))) * 32) + 8 * pp;
#pragma unroll
    for (int i = 0; i < 16; ++i) W[i] = trd2(wb + i * 2048, 1024);
    arrive(L_C0 + 8);
    until<1>(L_C0 + 8, 4);
    ST(6);
#pragma unroll
    for (int i = 0; i < 16; ++i) *(LAS v4u*)(L + R1A + (w * 16 + i) * 1024 + lane * 16) = W[16 + i];
    arrive(L_C0 + 16);
    WPH_FINISH();
    asm volatile("" ::"v"(pf));
    until<1>(L_C0 + 16, 4);
    ST(7);
#pragma unroll
    for (int i = 0; i < 16; ++i) W[16 + i] = trd2(wb + i * 2048, 1024);
    until<1>(L_C0 + 32, 4);
    csf = colsum<0, 32>(W);
    ST(9);
  }
  BAR();
  ST(28);
  layer<0, KV>(p, c, W, csf, wph ST_ARG);
  if (KV && b == 0 && w == 0 && lane == 0) __builtin_amdgcn_raw_buffer_store_b32(tag + 1u, r, WS_EPOCH, 0, SC1);
  layer<1, KV>(p, c, W, csf, wph ST_ARG);
}

__global__ void __launch_bounds__(512, 2) fused_kernel(Params p) {
  extern __shared__ __attribute__((aligned(16))) unsigned char smem[];
  LAS unsigned char* L = (LAS unsigned char*)smem;
  const int tid = threadIdx.x, lane = tid & 63, w = __builtin_amdgcn_readfirstlane(tid >> 6), b = blockIdx.x;
  const rsrc_t r = __builtin_amdgcn_make_buffer_rsrc((void*)p.ws, 0, WS_BYTES, 0x00020000);
  ST(0);
  STC(32);
  u32 pfc = 0;
  {
    u64 pc;
    int len;
    asm volatile("s_getpc_b64 %0\n.Lfk_begin:\n\ts_mov_b32 %1, .Lfk_end-.Lfk_begin" : "=s"(pc), "=s"(len));
    if (w == 4 && (lane * 32 + (b >> 3)) * 128 < len) {
      u32 t;
      asm volatile("global_load_dword %0, %1, %2" : "=v"(t) : "v"((lane * 32 + (b >> 3)) * 128), "s"(pc) : "memory");
      pfc = t;
    }
  }
  asm volatile("" ::"s"(p.q), "s"(p.k), "s"(p.v), "s"(p.shk), "s"(p.h), "s"(p.ws));
  const Ctx c = {L, r, pfc, b, w, lane};
  if (w >= 4) program<false>(p, c ST_ARG);
  else program<true>(p, c ST_ARG);
  ST(30);
  STC(33);
  ST_FLUSH();
}

extern "C" void kernel_launch(void* const* d_in, const int* in_sizes, int n_in, void* d_out, int out_size, void* d_ws, size_t ws_size,
                              hipStream_t stream) {
  Params p;
  p.q = (const u16*)d_in[0]; p.k = (const u16*)d_in[1]; p.v = (const u16*)d_in[2]; p.shk = (const u16*)d_in[3];
  p.h = (const u16*)d_in[4]; p.dv = (const u16*)d_in[5]; p.wqkv = (const u16*)d_in[7]; p.wphi = (const u16*)d_in[8];
  p.wo = (const u16*)d_in[9]; p.out = (u16*)d_out; p.ws = (unsigned char*)d_ws + (ws_size - WS_BYTES);
  (void)hipFuncSetAttribute((const void*)fused_kernel, hipFuncAttributeMaxDynamicSharedMemorySize, LDS_BYTES);
  hipLaunchKernelGGL(fused_kernel, dim3(256), dim3(512), LDS_BYTES, stream, p);
}
```

```cpp
#define STAMP
#define PROBE 1
#define STMASK ((1ull<<1)|1ull)
#include <hip/hip_runtime.h>
#include <hip/hip_bf16.h>

typedef unsigned short u16;
typedef unsigned u32;
typedef unsigned long long u64;
typedef u32 v4u __attribute__((ext_vector_type(4)));
typedef u32 v2u __attribute__((ext_vector_type(2)));
typedef short v4s __attribute__((ext_vector_type(4)));
typedef float f32x2 __attribute__((ext_vector_type(2)));
typedef float f32x4 __attribute__((ext_vector_type(4)));
typedef float f32x16 __attribute__((ext_vector_type(16)));
typedef __bf16 v8bf __attribute__((ext_vector_type(8)));
typedef __bf16 v2bf __attribute__((ext_vector_type(2)));
#define LAS __attribute__((address_space(3)))
#define GAS __attribute__((address_space(1)))
#define DI __device__ __forceinline__
template <class T, class F> DI T bc(F x) { return __builtin_bit_cast(T, x); }
#define BC(T, x) bc<T>(x)
typedef __amdgpu_buffer_rsrc_t rsrc_t;

constexpr int MB = 1 << 20;
constexpr int WS_EPOCH = 0;
constexpr int B_KVR = 4096;
constexpr int B_ZR = B_KVR + 64 * 1024;
constexpr int B_FL = 128 * 1024;
constexpr int B_ZP = 256 * 1024;
constexpr int B_SP = 512 * 1024;
constexpr int B_UT = 1 * MB;
constexpr int B_YI = 3 * MB;
constexpr int B_XI = 5 * MB;
constexpr int B_OI = 9 * MB;
constexpr int B_XR = 11 * MB;
constexpr int B_OR = B_XR + 512 * 1024;
constexpr int B_KVP = 13 * MB;
constexpr int B_STAMP = 25 * MB;
constexpr int WS_BYTES = 26 * MB;

constexpr int R1A = 0, R1B = 65536, R2 = 98304, LDS_BYTES = 163840;
constexpr int L_H = R2, L_U = R2 + 16640, L_P = R2 + 25856, L_S0 = R2 + 59136, L_C0 = R2 + 65536 - 64;
constexpr int L_KT = L_P, L_VT = L_P + 8704;
constexpr int L_QR = R1B;
constexpr int L_ST = R1B, L_EX = R1B + 4096, L_C1 = R1B + 20480, L_Y = R1B + 24576;

constexpr u32 SPIN_CAP = 1u << 14;
constexpr int SC1 = 16;
constexpr float LOG2E = 1.4426950408889634f;
constexpr u32 ONES = 0x3f803f80u;

#define D_UT 0
#define D_YI 0
#define D_KVP 16
#define D_OI 10
#define D_KVR 12
#define D_SP 8
#define D_XI 10
#define PF_Q 1
#define PF_W 0
#define D_H 0
#define C_EARLY 1
#define D_CL 8
#define Q_EARLY 0
#define D_F1 0
#define PRIO 0
#define D_RETRY 2

struct Params {
  const u16 *q, *k, *v, *shk, *h, *dv, *wqkv, *wphi, *wo;
  u16* out;
  unsigned char* ws;
};

DI float bflo(u32 x) { return BC(float, x << 16); }
DI float bfhi(u32 x) { return BC(float, x & 0xffff0000u); }
DI u32 pk2(float a, float b) { f32x2 p = {a, b}; return BC(u32, __builtin_convertvector(p, v2bf)); }
DI float op(float r) { asm("" : "+v"(r)); return r; }
DI float mx3(float a, float b, float c) { return __builtin_fmaxf(__builtin_fmaxf(a, b), c); }
DI f32x16 mfma32(v4u a, v4u b, f32x16 c) { return __builtin_amdgcn_mfma_f32_32x32x16_bf16(BC(v8bf, a), BC(v8bf, b), c, 0, 0, 0); }
DI f32x4 mfma16(v4u a, v4u b, f32x4 c) { return __builtin_amdgcn_mfma_f32_16x16x32_bf16(BC(v8bf, a), BC(v8bf, b), c, 0, 0, 0); }
DI float dot2(u32 a, u32 b, float c) { return __builtin_amdgcn_fdot2_f32_bf16(BC(v2bf, a), BC(v2bf, b), c, false); }
DI v2u trd(LAS unsigned char* p) { return BC(v2u, __builtin_amdgcn_ds_read_tr16_b64_v4i16((LAS v4s*)p)); }
DI v4u trd2(LAS unsigned char* p, int second) { v2u a = trd(p), b = trd(p + second); return v4u{a.x, a.y, b.x, b.y}; }
DI v4u pack8(const f32x16& x, int s) { return v4u{pk2(x[8 * s], x[8 * s + 1]), pk2(x[8 * s + 2], x[8 * s + 3]), pk2(x[8 * s + 4], x[8 * s + 5]), pk2(x[8 * s + 6], x[8 * s + 7])}; }
DI void swap32(float& a, float& b) { auto r = __builtin_amdgcn_permlane32_swap(BC(u32, a), BC(u32, b), false, false); a = BC(float, r[0]); b = BC(float, r[1]); }
DI void swap16(float& a, float& b) { auto r = __builtin_amdgcn_permlane16_swap(BC(u32, a), BC(u32, b), false, false); a = BC(float, r[0]); b = BC(float, r[1]); }
DI void swap32u(u32& a, u32& b) { auto r = __builtin_amdgcn_permlane32_swap(a, b, false, false); a = r[0]; b = r[1]; }
template <int CTRL> DI u32 dppu(u32 x) { return (u32)__builtin_amdgcn_update_dpp(0, (int)x, CTRL, 0xf, 0xf, true); }
template <int CTRL> DI float dppf(float x) { return BC(float, dppu<CTRL>(BC(u32, x))); }
DI bool all_lanes(bool ok) { return __builtin_amdgcn_ballot_w64(!ok) == 0ull; }
#define BAR() asm volatile("s_waitcnt lgkmcnt(0)\n\ts_barrier" ::: "memory")
#define DRAIN() asm volatile("s_waitcnt vmcnt(0)" ::: "memory")
#define VMW(n) asm volatile("s_waitcnt vmcnt(%0)" ::"n"(n) : "memory")
#define PIN(x) asm volatile("" : "+v"(x))
#define SB() __builtin_amdgcn_sched_barrier(0)
#define FENCE() asm volatile("" ::: "memory")
template <int I> struct IC { static constexpr int v = I; };
template <int I, int N, class F> DI void sfor(F&& f) { if constexpr (I < N) { f(IC<I>{}); sfor<I + 1, N>(f); } }
template <int N> DI void nap() { if (N > 0) __builtin_amdgcn_s_sleep(N); }

template <bool COH> DI void dma16(const void* base, u32 voff, u32 ldst) {
  u32 keep;
  ldst = __builtin_amdgcn_readfirstlane(ldst);
  if (COH) asm volatile("s_mov_b32 %0, m0\n\ts_mov_b32 m0, %3\n\ts_nop 0\n\tglobal_load_lds_dwordx4 %1, %2 sc1\n\ts_mov_b32 m0, %0" : "=&s"(keep) : "v"(voff), "s"(base), "s"(ldst) : "memory");
  else asm volatile("s_mov_b32 %0, m0\n\ts_mov_b32 m0, %3\n\ts_nop 0\n\tglobal_load_lds_dwordx4 %1, %2\n\ts_mov_b32 m0, %0" : "=&s"(keep) : "v"(voff), "s"(base), "s"(ldst) : "memory");
}
template <bool COH, int N> DI void dma_run(const void* base, u32 voff, u32 ss, u32 ldst) {
  u64 a = (u64)base;
  u32 keep;
  ldst = __builtin_amdgcn_readfirstlane(ldst);
  asm volatile("s_mov_b32 %0, m0\n\ts_mov_b32 m0, %1" : "=&s"(keep) : "s"(ldst));
#pragma unroll
  for (int i = 0; i < N; ++i) {
    if (COH) asm volatile("s_nop 0\n\tglobal_load_lds_dwordx4 %0, %1 sc1\n\ts_add_u32 m0, m0, 0x400" ::"v"(voff), "s"(a) : "memory", "scc");
    else asm volatile("s_nop 0\n\tglobal_load_lds_dwordx4 %0, %1\n\ts_add_u32 m0, m0, 0x400" ::"v"(voff), "s"(a) : "memory", "scc");
    a += ss;
  }
  asm volatile("s_mov_b32 m0, %0" ::"s"(keep));
}
DI v4u ld16(rsrc_t r, int off) { return __builtin_amdgcn_raw_buffer_load_b128(r, off, 0, SC1); }
DI u32 ld4(rsrc_t r, int off) { return __builtin_amdgcn_raw_buffer_load_b32(r, off, 0, SC1); }
DI u32 sum4(v4u x, u32 a) { return (a + x.x + x.y) + x.z + x.w; }
DI void st16(rsrc_t r, int off, v4u v) { __builtin_amdgcn_raw_buffer_store_b128(v, r, off, 0, SC1); }

DI void publish2(rsrc_t r, int off, int roff, v4u a, v4u b, u32 tag) {
  st16(r, off, a); st16(r, off + 1024, b);
  __builtin_amdgcn_raw_buffer_store_b32(sum4(b, sum4(a, tag)), r, roff, 0, SC1);
}

DI void arrive(u32 ctr) {
  asm volatile("s_mov_b64 exec, 1\n\tds_add_u32 %0, %1\n\ts_mov_b64 exec, -1" ::"v"(ctr), "v"(1u) : "memory");
}
template <int NAP = 0> DI void until(u32 ctr, u32 target) {
  u32 t, u;
  if (NAP) asm volatile("s_branch 2f\n1:\n\ts_sleep %4\n2:\n\tds_read_b32 %0, %2\n\ts_waitcnt lgkmcnt(0)\n\tv_readfirstlane_b32 %1, %0\n\ts_cmp_lt_u32 %1, %3\n\ts_cbranch_scc1 1b" : "=&v"(t), "=&s"(u) : "v"(ctr), "s"(target), "n"(NAP) : "memory", "scc");
  else asm volatile("1:\n\tds_read_b32 %0, %2\n\ts_waitcnt lgkmcnt(0)\n\tv_readfirstlane_b32 %1, %0\n\ts_cmp_lt_u32 %1, %3\n\ts_cbranch_scc1 1b" : "=&v"(t), "=&s"(u) : "v"(ctr), "s"(target) : "memory", "scc");
}

template <int N, int MODE> DI u32 fetch(rsrc_t r, v4u (&v)[N], int off, int stride, u32 tag) {
  u32 spins = 0;
#pragma nounroll
  for (;; ++spins) {
    FENCE();
#pragma unroll
    for (int i = 0; i < N; ++i) v[i] = ld16(r, off + i * stride);
    u32 a = 0u;
#pragma unroll
    for (int i = 0; i < N; ++i) {
      if (MODE == 0) a |= (v[i].y ^ tag) | (v[i].w ^ tag);
      if (MODE == 1) a = max(max(a, max(v[i].x, v[i].y)), max(v[i].z, v[i].w));
      if (MODE == 2) { v[i] ^= tag; a |= (v[i].x | v[i].y) | (v[i].z | v[i].w); }
    }
    const bool ok = MODE == 0 ? a == 0u : MODE == 1 ? a != ~0u : (a & 0xffu) == 0u;
    if (all_lanes(ok) || spins > SPIN_CAP) break;
    nap<D_RETRY>();
  }
  FENCE();
  return spins;
}

#define CLK() __builtin_amdgcn_s_memrealtime()
#define ST(i) do { if (!((STMASK >> (i)) & 1ull)) break; const u32 t_ = (u32)CLK(); asm volatile("s_nop 4\n\tv_writelane_b32 %0, %1, %2" : "+v"(stampv) : "s"(t_), "n"(i)); } while (0)
#define STC(i) do { const u32 t_ = (u32)__builtin_amdgcn_s_memtime(); asm volatile("s_nop 4\n\tv_writelane_b32 %0, %1, %2" : "+v"(stampv) : "s"(t_), "n"(i)); } while (0)
#define STV(i, v) do { const u32 t_ = __builtin_amdgcn_readfirstlane((v) + 1); asm volatile("s_nop 4\n\tv_writelane_b32 %0, %1, %2" : "+v"(stampv) : "s"(t_), "n"(i)); } while (0)
#define ST_PARAM , u32& stampv
#define ST_ARG , stampv
#define ST_FLUSH() do { const u32 d_ = __builtin_amdgcn_readlane(stampv, PROBE) - __builtin_amdgcn_readlane(stampv, 0); const u32 e_ = (u32)CLK(); \
    if (w >= 4 && d_ < 20000u) while ((u32)CLK() - e_ < d_) __builtin_amdgcn_s_sleep(1); } while (0)

DI void dual_softmax(const f32x16& x, int o, float c, v4u& plus, v4u& minus) {
  float mx = mx3(mx3(x[o], x[o + 1], x[o + 2]), mx3(x[o + 3], x[o + 4], x[o + 5]), mx3(x[o + 6], x[o + 7], x[o]));
  float nm = mx3(mx3(-x[o], -x[o + 1], -x[o + 2]), mx3(-x[o + 3], -x[o + 4], -x[o + 5]), mx3(-x[o + 6], -x[o + 7], -x[o]));
  swap32(mx, nm);
  float t = mx3(mx, nm, nm) * -c, t2 = t;
  swap32(t, t2);
  float ep[8], em[8];
#pragma unroll
  for (int i = 0; i < 8; ++i) { ep[i] = op(__builtin_fmaf(x[o + i], c, t)); em[i] = op(__builtin_fmaf(x[o + i], -c, t2)); }
#pragma unroll
  for (int i = 0; i < 8; ++i) { ep[i] = __builtin_amdgcn_exp2f(ep[i]); em[i] = __builtin_amdgcn_exp2f(em[i]); }
  float sp = op(op(op(ep[0] + ep[1]) + op(ep[2] + ep[3])) + op(op(ep[4] + ep[5]) + op(ep[6] + ep[7])));
  float sm = op(op(op(em[0] + em[1]) + op(em[2] + em[3])) + op(op(em[4] + em[5]) + op(em[6] + em[7])));
  swap32(sp, sm);
  float ip = __builtin_amdgcn_rcpf(sp + sm), im = ip;
  swap32(ip, im);
  plus = v4u{pk2(op(ep[0] * ip), op(ep[1] * ip)), pk2(op(ep[2] * ip), op(ep[3] * ip)), pk2(op(ep[4] * ip), op(ep[5] * ip)), pk2(op(ep[6] * ip), op(ep[7] * ip))};
  minus = v4u{pk2(op(em[0] * im), op(em[1] * im)), pk2(op(em[2] * im), op(em[3] * im)), pk2(op(em[4] * im), op(em[5] * im)), pk2(op(em[6] * im), op(em[7] * im))};
}

struct Ctx {
  LAS unsigned char* L;
  rsrc_t r;
  u32 tag;
  int b, w, lane;
};

template <int N> DI void gemm(f32x16& acc, const v4u (&W)[32], LAS unsigned char* xb) {
  constexpr int PD = 4;
  v4u x[PD];
#pragma unroll
  for (int i = 0; i < PD; ++i) x[i] = *(LAS v4u*)(xb + i * 1024);
  SB();
#pragma unroll
  for (int i = 0; i < N; ++i) {
    const v4u cur = x[i % PD];
    acc = mfma32(W[i], cur, acc);
    if (i + PD < N) x[i % PD] = *(LAS v4u*)(xb + (i + PD) * 1024);
    SB();
  }
  PIN(acc);
}
DI void row_moments(LAS unsigned char* st, int lane, float& m, float& rs) {
  LAS f32x4* s = (LAS f32x4*)(st + (lane & 31) * 16);
  const f32x4 a = s[0], b = s[32], c = s[64], d = s[96];
  m = op(op(op(a.x + a.z) + op(b.x + b.z)) + op(op(c.x + c.z) + op(d.x + d.z))) * (-1.f / 512.f);
  const float e2 = op(op(op(a.y + a.w) + op(b.y + b.w)) + op(op(c.y + c.w) + op(d.y + d.w))) * (1.f / 512.f);
  rs = __builtin_amdgcn_rsqf(mx3(__builtin_fmaf(-m, m, op(e2)), 0.f, 0.f) + 1e-5f);
}
DI void row_stats(LAS unsigned char* L, int T, int lane, u32& mf, float& rs) {
  float m;
  row_moments(L + L_ST + T * 2048, lane, m, rs);
  const float hi = bflo(pk2(m, m));
  mf = pk2(hi, op(m - hi));
}
DI void acc_stats(v4u x, float& s1, float& s2) {
  s1 = dot2(x.x, ONES, s1); s2 = dot2(x.x, x.x, s2); s1 = dot2(x.y, ONES, s1); s2 = dot2(x.y, x.y, s2);
  s1 = dot2(x.z, ONES, s1); s2 = dot2(x.z, x.z, s2); s1 = dot2(x.w, ONES, s1); s2 = dot2(x.w, x.w, s2);
}
template <int N0, int N1> DI v2u colsum(const v4u (&W)[32]) {
  float cs = 0.f, cs2 = 0.f;
#pragma unroll
  for (int i = N0; i < N1; ++i) { cs = dot2(W[i].x, ONES, cs); cs2 = dot2(W[i].y, ONES, cs2); cs = dot2(W[i].z, ONES, cs); cs2 = dot2(W[i].w, ONES, cs2); }
  cs += cs2;
  const u32 hh = pk2(cs, cs);
  const float lo = cs - bflo(hh);
  v2u o = {hh, pk2(lo, lo)};
  PIN(o);
  return o;
}
DI void stage64(const void* base, int off, int stride, int w4, int lane) {
  dma_run<false, 16>(base, off + (lane >> 2) * stride + (lane & 3) * 16, 16 * stride, R1A + w4 * 16384);
}
DI u32 load_image(const Params& p, rsrc_t r, LAS unsigned char* L, int LY, int rb, int w, int lane, u32 tag) {
  const int f0 = (w & 1) * 32 + (w >> 1) * 8;
  float a1, a2, b1, b2;
  u32 spins = 0;
  nap<D_XI>();
#pragma nounroll
  for (;; ++spins) {
    dma_run<true, 8>(p.ws, B_XI + LY * 2 * MB + rb * 65536 + f0 * 1024 + lane * 16, 1024, R2 + f0 * 1024);
    v4u rec;
#pragma unroll
    for (int i = 0; i < 4; ++i) rec[i] = ld4(r, B_XR + LY * 262144 + rb * 8192 + f0 * 128 + i * 256 + lane * 4);
    a1 = a2 = b1 = b2 = 0.f;
    u32 cs = 4u * tag;
    LAS v4u* x0 = (LAS v4u*)(L + R2 + f0 * 1024 + lane * 16);
    v4u s, t;
    VMW(10); s = x0[0]; t = x0[64]; acc_stats(s, a1, a2); acc_stats(t, b1, b2); cs = sum4(t, sum4(s, cs));
    VMW(8); s = x0[128]; t = x0[192]; acc_stats(s, a1, a2); acc_stats(t, b1, b2); cs = sum4(t, sum4(s, cs));
    VMW(6); s = x0[256]; t = x0[320]; acc_stats(s, a1, a2); acc_stats(t, b1, b2); cs = sum4(t, sum4(s, cs));
    VMW(4); s = x0[384]; t = x0[448]; acc_stats(s, a1, a2); acc_stats(t, b1, b2); cs = sum4(t, sum4(s, cs));
    FENCE();
    if (all_lanes(cs == sum4(rec, 0u)) || spins > SPIN_CAP) break;
    nap<D_RETRY>();
  }
  *(LAS f32x2*)(L + L_ST + (w & 1) * 2048 + (w >> 1) * 512 + (lane & 31) * 16 + (lane >> 5) * 8) = f32x2{a1 + b1, a2 + b2};
  return spins;
}
DI void gate_publish(f32x16& acc, float rs, float mr, v4u y0, v4u y1, rsrc_t r, int LY, int rb, int f, int lane, u32 tag) {
#pragma unroll
  for (int i = 0; i < 4; ++i) {
    acc[2 * i] = __builtin_fmaf(__builtin_fmaf(acc[2 * i], rs, mr), bflo(y0[i]), acc[2 * i]);
    acc[2 * i + 1] = __builtin_fmaf(__builtin_fmaf(acc[2 * i + 1], rs, mr), bfhi(y0[i]), acc[2 * i + 1]);
    acc[8 + 2 * i] = __builtin_fmaf(__builtin_fmaf(acc[8 + 2 * i], rs, mr), bflo(y1[i]), acc[8 + 2 * i]);
    acc[9 + 2 * i] = __builtin_fmaf(__builtin_fmaf(acc[9 + 2 * i], rs, mr), bfhi(y1[i]), acc[9 + 2 * i]);
  }
  publish2(r, B_XI + LY * 2 * MB + rb * 65536 + f * 1024 + lane * 16, B_XR + LY * 262144 + rb * 8192 + f * 128 + lane * 4, pack8(acc, 0), pack8(acc, 1), tag);
}

struct Post {
  v4u selp, selm, kb, vb, fp, fm, v0, v1;
  u32 ones;
  f32x16 pr, vt, pt;
  float vs[8], ep[8], em[8], mx, nm, t, t2, cc, s0, s1, s2, s3;
  DI void init(int n, int h) {
    const int n16 = n & 15, j = 4 * (n16 >> 3) + (n16 & 3);
    const u32 one = (((n16 >> 2) & 1) == h) ? (0x3f80u << (16 * (j & 1))) : 0u;
    const u32 op_ = n < 16 ? one : 0u, om = n < 16 ? 0u : one;
    selp = v4u{(j >> 1) == 0 ? op_ : 0u, (j >> 1) == 1 ? op_ : 0u, (j >> 1) == 2 ? op_ : 0u, (j >> 1) == 3 ? op_ : 0u};
    selm = v4u{(j >> 1) == 0 ? om : 0u, (j >> 1) == 1 ? om : 0u, (j >> 1) == 2 ? om : 0u, (j >> 1) == 3 ? om : 0u};
    ones = n < 16 ? 0u : ONES;
    PIN(selp); PIN(selm); PIN(ones);
  }
  template <int S> DI void step(const f32x16& a, float rs, v4u wph, f32x16& kv, bool first) {
    f32x16 zero;
#pragma unroll
    for (int i = 0; i < 16; ++i) zero[i] = 0.f;
    if constexpr (S == 0) { kb = pack8(a, 0); cc = rs * LOG2E; }
    if constexpr (S == 1) { pr = mfma32(wph, kb, zero); _Pragma("unroll") for (int i = 0; i < 8; ++i) vs[i] = op(a[8 + i] * rs); }
    if constexpr (S == 2) vb = v4u{pk2(vs[0], vs[1]), pk2(vs[2], vs[3]), pk2(vs[4], vs[5]), pk2(vs[6], vs[7])};
    if constexpr (S == 3) {
      vt = mfma32(vb, selp, zero);
      mx = mx3(mx3(pr[0], pr[1], pr[2]), mx3(pr[3], pr[4], pr[5]), mx3(pr[6], pr[7], pr[0]));
      nm = mx3(mx3(-pr[0], -pr[1], -pr[2]), mx3(-pr[3], -pr[4], -pr[5]), mx3(-pr[6], -pr[7], -pr[0]));
    }
    if constexpr (S == 4) { swap32(mx, nm); t = mx3(mx, nm, nm) * -cc; t2 = t; }
    if constexpr (S == 5) { swap32(t, t2); v0.x = pk2(vt[0], vt[1]) | ones; }
    if constexpr (S == 6) _Pragma("unroll") for (int i = 0; i < 5; ++i) ep[i] = op(__builtin_fmaf(pr[i], cc, t));
    if constexpr (S == 7) { _Pragma("unroll") for (int i = 5; i < 8; ++i) ep[i] = op(__builtin_fmaf(pr[i], cc, t)); _Pragma("unroll") for (int i = 0; i < 2; ++i) em[i] = op(__builtin_fmaf(pr[i], -cc, t2)); }
    if constexpr (S == 8) _Pragma("unroll") for (int i = 2; i < 7; ++i) em[i] = op(__builtin_fmaf(pr[i], -cc, t2));
    if constexpr (S == 9) { em[7] = op(__builtin_fmaf(pr[7], -cc, t2)); ep[0] = __builtin_amdgcn_exp2f(ep[0]); ep[1] = __builtin_amdgcn_exp2f(ep[1]); }
    if constexpr (S == 10) { ep[2] = __builtin_amdgcn_exp2f(ep[2]); ep[3] = __builtin_amdgcn_exp2f(ep[3]); s0 = op(ep[0] + ep[1]); }
    if constexpr (S == 11) { ep[4] = __builtin_amdgcn_exp2f(ep[4]); ep[5] = __builtin_amdgcn_exp2f(ep[5]); s1 = op(ep[2] + ep[3]); }
    if constexpr (S == 12) { ep[6] = __builtin_amdgcn_exp2f(ep[6]); ep[7] = __builtin_amdgcn_exp2f(ep[7]); s0 = op(s0 + s1); }
    if constexpr (S == 13) { em[0] = __builtin_amdgcn_exp2f(em[0]); em[1] = __builtin_amdgcn_exp2f(em[1]); s1 = op(ep[4] + ep[5]); }
    if constexpr (S == 14) { em[2] = __builtin_amdgcn_exp2f(em[2]); em[3] = __builtin_amdgcn_exp2f(em[3]); s2 = op(ep[6] + ep[7]); }
    if constexpr (S == 15) { em[4] = __builtin_amdgcn_exp2f(em[4]); em[5] = __builtin_amdgcn_exp2f(em[5]); s1 = op(s1 + s2); }
    if constexpr (S == 16) { em[6] = __builtin_amdgcn_exp2f(em[6]); em[7] = __builtin_amdgcn_exp2f(em[7]); s0 = op(s0 + s1); }
    if constexpr (S == 17) { s1 = op(em[0] + em[1]); s2 = op(em[2] + em[3]); s3 = op(em[4] + em[5]); t = op(em[6] + em[7]); s1 = op(s1 + s2); }
    if constexpr (S == 18) { s3 = op(s3 + t); v0.y = pk2(vt[2], vt[3]) | ones; s1 = op(s1 + s3); }
    if constexpr (S == 19) { swap32(s0, s1); t = __builtin_amdgcn_rcpf(s0 + s1); }
    if constexpr (S == 20) { t2 = t; v0.z = pk2(vt[4], vt[5]) | ones; swap32(t, t2); }
    if constexpr (S == 21) _Pragma("unroll") for (int i = 0; i < 5; ++i) ep[i] = op(ep[i] * t);
    if constexpr (S == 22) { _Pragma("unroll") for (int i = 5; i < 8; ++i) ep[i] = op(ep[i] * t); fp.x = pk2(ep[0], ep[1]); fp.y = pk2(ep[2], ep[3]); }
    if constexpr (S == 23) { fp.z = pk2(ep[4], ep[5]); fp.w = pk2(ep[6], ep[7]); _Pragma("unroll") for (int i = 0; i < 3; ++i) em[i] = op(em[i] * t2); }
    if constexpr (S == 24) { pt = mfma32(fp, selp, zero); _Pragma("unroll") for (int i = 3; i < 8; ++i) em[i] = op(em[i] * t2); fm = v4u{pk2(em[0], em[1]), pk2(em[2], em[3]), pk2(em[4], em[5]), pk2(em[6], em[7])}; }
    if constexpr (S == 25) { pt = mfma32(fm, selm, pt); v0.w = pk2(vt[6], vt[7]) | ones; v1.x = pk2(vt[8], vt[9]) | ones; v1.y = pk2(vt[10], vt[11]) | ones; v1.z = pk2(vt[12], vt[13]) | ones; v1.w = pk2(vt[14], vt[15]) | ones; }
    if constexpr (S == 27) fp = pack8(pt, 0);
    if constexpr (S == 28) { kv = mfma32(fp, v0, first ? zero : kv); fm = pack8(pt, 1); }
    if constexpr (S == 29) kv = mfma32(fm, v1, kv);
  }
};

template <int LY, bool KV>
DI void layer(const Params& p, const Ctx& c, v4u (&W)[32], v2u& csf, const v4u wph ST_PARAM) {
  LAS unsigned char* L = c.L;
  const rsrc_t r = c.r;
  int lane = c.lane;
  PIN(lane);
  const int b = c.b, w = c.w, rb = b & 31, cg = b >> 5, hf = cg & 1, cq = cg >> 1;
  const int G = lane >> 4, q = (lane >> 2) & 3, pp = lane & 3, h = lane >> 5, n = lane & 31;
  constexpr int S0 = LY ? 20 : 10, S1 = LY ? 50 : 40;
  constexpr u32 C4 = LY ? 8 : 4;
  const u32 tagl = c.tag + LY;
  const v4u z4 = {0u, 0u, 0u, 0u};
  f32x16 zero;
#pragma unroll
  for (int i = 0; i < 16; ++i) zero[i] = 0.f;
  if (KV) {
    const v4u cs4 = {csf.x, csf.y, 0u, 0u};
    f32x16 acc0 = zero, acc1, kv;
    Post ps;
    u32 mf0, mf1; float rs0, rs1;
    {
      constexpr int PD = 4;
      LAS unsigned char* xb = L + R2 + lane * 16;
      v4u x[PD];
#pragma unroll
      for (int i = 0; i < PD; ++i) x[i] = *(LAS v4u*)(xb + i * 1024);
      SB();
      sfor<0, 64>([&](auto I) __attribute__((always_inline)) {
        constexpr int i = decltype(I)::v;
        const v4u cur = x[i % PD];
        if constexpr (i < 32) acc0 = mfma32(W[i & 31], cur, acc0); else acc1 = mfma32(W[i & 31], cur, acc1);
        if constexpr (i + PD < 64) x[i % PD] = *(LAS v4u*)(xb + (i + PD) * 1024);
        if constexpr (i == 1) { row_stats(L, 0, lane, mf0, rs0); PIN(mf0); PIN(rs0); }
        if constexpr (i == 8) { row_stats(L, 1, lane, mf1, rs1); PIN(mf1); PIN(rs1); }
        if constexpr (i == 15) ps.init(n, h);
        if constexpr (i == 24) acc0 = mfma32(cs4, v4u{mf0, mf0, 0u, 0u}, acc0);
        if constexpr (i == 28) acc1 = mfma32(cs4, v4u{mf1, mf1, 0u, 0u}, zero);
        if constexpr (i >= 33) ps.template step<i - 33>(acc0, rs0, wph, kv, true);
        if constexpr (i < 1 || i >= 22 || i == 7 || i == 14) SB();
      });
    }
    ST(S1 + 2);
    arrive(L_C1 + 16);
    sfor<0, 31>([&](auto I) __attribute__((always_inline)) { ps.template step<decltype(I)::v>(acc1, rs1, wph, kv, false); });
    if (n <= 16) {
      const int head = 4 * cg + w;
      const int o0 = n < 16 ? B_KVP + ((head * 8 + h * 2 + (n >> 3)) * 32 + rb) * 256 + (n & 7) * 16 : B_ZP + (head * 32 + rb) * 128 + h * 32;
      const u32 tb = tagl & 0xffu;
#pragma unroll
      for (int qq = 0; qq < 4; ++qq)
        st16(r, o0 + (n < 16 ? (qq >> 1) * 32768 + (qq & 1) * 128 : (qq >> 1) * 64 + (qq & 1) * 16),
             v4u{(BC(u32, kv[4 * qq]) & ~0xffu) | tb, (BC(u32, kv[4 * qq + 1]) & ~0xffu) | tb, (BC(u32, kv[4 * qq + 2]) & ~0xffu) | tb, (BC(u32, kv[4 * qq + 3]) & ~0xffu) | tb});
    }
    ST(S0 + 1);
    if (w == 0) {
      nap<D_KVP>();
      v4u t[8];
      STV(58 + LY, (fetch<8, 2>(r, t, B_KVP + b * 8192 + lane * 16, 1024, tagl & 0xffu)));
      ST(S0 + 3);
      float s[4];
#pragma unroll
      for (int i = 0; i < 4; ++i)
        s[i] = op(op(op(BC(float, t[0][i]) + BC(float, t[1][i])) + op(BC(float, t[2][i]) + BC(float, t[3][i]))) + op(op(BC(float, t[4][i]) + BC(float, t[5][i])) + op(BC(float, t[6][i]) + BC(float, t[7][i]))));
      swap32(s[0], s[2]); swap32(s[1], s[3]);
      float a = op(s[0] + s[2]), bb = op(s[1] + s[3]), a2 = a, b2 = bb;
      swap16(a, a2); swap16(bb, b2);
      u32 d0 = pk2(op(a + a2), op(bb + b2)), d1 = d0;
      swap32u(d0, d1);
      const u32 e0 = dppu<0x128>(d0), e1 = dppu<0x128>(d1);
      if (lane < 8) st16(r, B_KVR + LY * 32768 + b * 128 + lane * 16, v4u{d0, d1, e0, e1});
      arrive(L_C1 + 8);
    } else if (w == 1 && (b & 7) == 0) {
      nap<D_KVP>();
      v4u t[4];
      fetch<4, 2>(r, t, B_ZP + (b >> 3) * 4096 + lane * 16, 1024, tagl & 0xffu);
      float s[4];
#pragma unroll
      for (int i = 0; i < 4; ++i) s[i] = op(op(BC(float, t[0][i]) + BC(float, t[1][i])) + op(BC(float, t[2][i]) + BC(float, t[3][i])));
      swap32(s[0], s[2]); swap32(s[1], s[3]);
      float a = op(s[0] + s[2]), bb = op(s[1] + s[3]), a2 = a, b2 = bb;
      swap16(a, a2); swap16(bb, b2);
      a = op(a + a2); bb = op(bb + b2);
      a = op(a + dppf<0x128>(a)); bb = op(bb + dppf<0x128>(bb));
      u32 d0 = pk2(a, bb), d1 = d0;
      swap32u(d0, d1);
      const u32 e0 = dppu<0xb1>(d0), e1 = dppu<0xb1>(d1);
      if (lane < 8 && !(lane & 1)) st16(r, B_ZR + LY * 4096 + (b >> 3) * 128 + lane * 8, v4u{d0, d1, e0, e1});
    }
  } else {
    const int hp = (w >> 1) & 1, kh = w & 1;
    LAS unsigned char* wb = L + R1A + (w - 4) * 16384 + (4 * h + q) * 64 + 32 * (G & 1) + 8 * pp;
    if (LY == 0) {
      stage64(p.wqkv, kh * 256 * 3072 + cg * 128 + hp * 64, 3072, w - 4, lane);
      const rsrc_t rw = __builtin_amdgcn_make_buffer_rsrc((void*)p.wo, 0, 512 * 1024, 0x00020000);
#pragma unroll
      for (int i = 0; i < 16; ++i) W[16 + i] = __builtin_amdgcn_raw_buffer_load_b128(rw, (256 + (lane >> 2)) * 1024 + cq * 256 + (w - 4) * 64 + (lane & 3) * 16, i * 16384, 0);
      VMW(16);
      ST(S1 + 0);
#pragma unroll
      for (int i = 0; i < 16; ++i) W[i] = trd2(wb + i * 1024, 512);
      asm volatile("s_waitcnt lgkmcnt(0)" ::: "memory");
      stage64(p.wo, cq * 256 + (w - 4) * 64, 1024, w - 4, lane);
    }
    ST(S1 + 1);
    until<1>(L_C1 + 16, C4);
    ST(S1 + 2);
    if (LY == 0) csf = colsum<0, 16>(W);
    const v4u cs4 = {csf.x, csf.y, 0u, 0u};
    u32 mf; float rs;
    row_stats(L, kh ^ 1, lane, mf, rs);
    f32x16 acco = mfma32(cs4, v4u{mf, mf, 0u, 0u}, zero);
    row_stats(L, kh, lane, mf, rs);
    f32x16 acck = mfma32(cs4, v4u{mf, mf, 0u, 0u}, zero);
    gemm<16>(acco, W, L + R2 + (kh ^ 1) * 32768 + kh * 16384 + lane * 16);
    gemm<16>(acck, W, L + R2 + kh * 32768 + kh * 16384 + lane * 16);
    ST(S1 + 3);
#pragma unroll
    for (int i = 0; i < 4; ++i) *(LAS f32x4*)(L + L_EX + ((w - 4) * 4 + i) * 1024 + lane * 16) = f32x4{acco[4 * i], acco[4 * i + 1], acco[4 * i + 2], acco[4 * i + 3]};
    arrive(L_C1);
    until(L_C1, C4);
    ST(S1 + 5);
#pragma unroll
    for (int i = 0; i < 4; ++i) {
      const f32x4 t = *(LAS f32x4*)(L + L_EX + (((w - 4) ^ 1) * 4 + i) * 1024 + lane * 16);
      acck[4 * i] += t.x; acck[4 * i + 1] += t.y; acck[4 * i + 2] += t.z; acck[4 * i + 3] += t.w;
    }
    f32x16 pr = mfma32(n < 16 ? wph : z4, pack8(acck, 0), zero);
    pr = mfma32(n < 16 ? z4 : wph, pack8(acck, 1), pr);
    v4u ph[4], kr[4];
    dual_softmax(pr, 0, rs * LOG2E, ph[0], ph[1]);
    dual_softmax(pr, 8, rs * LOG2E, ph[2], ph[3]);
    PIN(ph[0]); PIN(ph[1]); PIN(ph[2]); PIN(ph[3]);
    ST(S0 + 1);
    until<1>(L_C1 + 8, LY + 1);
    ST(S0 + 3);
    nap<D_KVR>();
    {
      const int hd = 4 * cg + 2 * hp;
      const int o0 = n < 16 ? B_KVR + LY * 32768 + hd * 1024 + h * 256 + n * 16 : B_ZR + LY * 4096 + hd * 128 + h * 16;
      const int hs = n < 16 ? 1024 : 128, ss = n < 16 ? 512 : 32;
      u32 spins = 0;
#pragma nounroll
      for (;; ++spins) {
        FENCE();
        u32 a = 0u;
#pragma unroll
        for (int i = 0; i < 4; ++i) kr[i] = ld16(r, o0 + (i >> 1) * hs + (i & 1) * ss);
#pragma unroll
        for (int i = 0; i < 4; ++i) a = max(max(a, max(kr[i].x, kr[i].y)), max(kr[i].z, kr[i].w));
        if (all_lanes(a != ~0u) || spins > SPIN_CAP) break;
        nap<D_RETRY>();
      }
      FENCE();
      STV(62 + LY, spins);
    }
    ST(S0 + 5);
    v4u of[2];
#pragma unroll
    for (int i = 0; i < 2; ++i) {
      f32x16 o = mfma32(kr[2 * i], ph[2 * i], zero);
      o = mfma32(kr[2 * i + 1], ph[2 * i + 1], o);
      const float inv = __builtin_amdgcn_rcpf(o[8] + 1e-6f);
      of[i] = v4u{pk2(o[0] * inv, o[1] * inv), pk2(o[2] * inv, o[3] * inv), pk2(o[4] * inv, o[5] * inv), pk2(o[6] * inv, o[7] * inv)};
    }
    const int fo = (rb * 2 + kh) * 32 + 4 * cg + 2 * hp;
    publish2(r, B_OI + fo * 1024 + lane * 16, B_OR + fo * 128 + lane * 4, of[0], of[1], tagl);
  }
  ST(S0 + 2);
  BAR();
  ST(S0 + 4);
  LAS unsigned char* ob = L + R2 + (hf ^ 1) * 32768 + lane * 16;
  {
    if (!KV && LY == 0) {
      LAS unsigned char* eb = L + L_EX + (w - 4) * 4096;
#pragma unroll
      for (int j = 0; j < 4; ++j) {
#pragma unroll
        for (int i = 0; i < 4; ++i) *(LAS v4u*)(eb + i * 1024 + lane * 16) = W[16 + 4 * j + i];
        FENCE();
#pragma unroll
        for (int i = 0; i < 4; ++i) W[16 + 4 * j + i] = trd2(eb + i * 1024 + (4 * h + q) * 64 + 32 * (G & 1) + 8 * pp, 512);
        FENCE();
      }
    } else nap<D_OI>();
    u32 spins = 0;
    const int fo = (rb * 2 + hf) * 32 + 4 * w;
#pragma nounroll
    for (;; ++spins) {
      dma_run<true, 4>(p.ws, B_OI + fo * 1024 + lane * 16, 1024, R2 + (hf ^ 1) * 32768 + 4 * w * 1024);
      const u32 r0 = ld4(r, B_OR + fo * 128 + lane * 4), r1 = ld4(r, B_OR + fo * 128 + 256 + lane * 4);
      VMW(2);
      u32 a = 2u * tagl;
#pragma unroll
      for (int i = 0; i < 4; ++i) a = sum4(*(LAS v4u*)(ob + (4 * w + i) * 1024), a);
      FENCE();
      if (all_lanes(a == r0 + r1) || spins > SPIN_CAP) break;
      nap<D_RETRY>();
    }
    STV(48 + LY, spins);
  }
  BAR();
  ST(S0 + 6);
  if (!KV) {
    const int ct = w - 4, ks0 = 8 * cq + 2 * ct;
    LAS unsigned char* xb = L + R2 + (hf * 32 + ks0) * 1024 + lane * 16;
    LAS unsigned char* wb = L + R1A + ct * 16384 + (4 * h + q) * 64 + 32 * (G & 1) + 8 * pp;
    f32x16 acc = zero;
    v4u x0, x1;
    {
      constexpr int PD = 4;
      v4u o[PD], a[PD];
#pragma unroll
      for (int i = 0; i < PD; ++i) o[i] = *(LAS v4u*)(ob + (16 + i) * 1024);
      SB();
#pragma unroll
      for (int i = 0; i < 32; ++i) {
        acc = mfma32(i < 16 ? W[16 + (i & 15)] : a[i % PD], o[i % PD], acc);
        if (i + PD < 32) o[i % PD] = *(LAS v4u*)(ob + ((16 + i + PD) & 31) * 1024);
        if (i + PD >= 16 && i + PD < 32) a[i % PD] = trd2(wb + (i + PD - 16) * 1024, 512);
        if (i == 2) x0 = *(LAS v4u*)xb;
        if (i == 4) x1 = *(LAS v4u*)(xb + 1024);
        SB();
      }
    }
#pragma unroll
    for (int i = 0; i < 4; ++i) { acc[2 * i] += bflo(x0[i]); acc[2 * i + 1] += bfhi(x0[i]); acc[8 + 2 * i] += bflo(x1[i]); acc[9 + 2 * i] += bfhi(x1[i]); }
    ST(S0 + 7);
    if (LY == 0) {
      f32x2 s1 = {0.f, 0.f}, s2 = {0.f, 0.f};
#pragma unroll
      for (int i = 0; i < 8; ++i) { const f32x2 t = {acc[2 * i], acc[2 * i + 1]}; s1 += t; s2 += t * t; }
      float u1 = s1.x + s1.y, u2 = s2.x + s2.y;
      swap32(u1, u2);
      float t = u1 + u2, t2 = t;
      swap32(t, t2);
      const int hrb = rb * 2 + hf;
      if (lane < 32) st16(r, B_SP + (hrb * 16 + cq * 4 + ct) * 512 + n * 16, v4u{BC(u32, t), c.tag, BC(u32, t2), c.tag});
      const v4u y0 = *(LAS v4u*)(L + L_Y + ct * 2048 + lane * 16), y1 = *(LAS v4u*)(L + L_Y + ct * 2048 + 1024 + lane * 16);
      nap<D_SP>();
      v4u sp[8];
      STV(37, (fetch<8, 0>(r, sp, B_SP + (hrb * 16 + 8 * h) * 512 + n * 16, 512, c.tag)));
      ST(S0 + 8);
      u1 = 0.f; u2 = 0.f;
#pragma unroll
      for (int i = 0; i < 8; ++i) { u1 += BC(float, sp[i].x); u2 += BC(float, sp[i].z); }
      swap32(u1, u2);
      t = u1 + u2; t2 = t;
      swap32(t, t2);
      const float m = t * (1.f / 512.f), rs = __builtin_amdgcn_rsqf(mx3(t2 * (1.f / 512.f) - m * m, 0.f, 0.f) + 1e-5f);
      gate_publish(acc, rs, -m * rs, y0, y1, r, 1, rb, hf * 32 + ks0, lane, c.tag);
    } else {
      v4u a = pack8(acc, 0), d = pack8(acc, 1);
      u32 a0 = a.x, a1 = a.y, a2 = a.z, a3 = a.w, d0 = d.x, d1 = d.y, d2 = d.z, d3 = d.w;
      swap32u(a0, a2); swap32u(a1, a3); swap32u(d0, d2); swap32u(d1, d3);
      GAS unsigned char* o = (GAS unsigned char*)p.out + (rb * 64 + hf * 32 + n) * 1024 + cq * 256 + ct * 64 + h * 16;
      *(GAS v4u*)o = v4u{a0, a1, a2, a3};
      *(GAS v4u*)(o + 32) = v4u{d0, d1, d2, d3};
    }
  }
  if (LY == 0) {
    BAR();
    ST(S0 + 9);
    STV(38, load_image(p, r, L, 1, rb, w, lane, c.tag));
    BAR();
    ST(20);
  }
}

DI void stage_kv(const Params& p, int stage, int cg, int w, int lane) {
  const int ty = w >> 1, g = ((lane >> 4) & 1) ^ (2 * ty);
  dma_run<false, 16>(p.wqkv, (stage * 256 + (w & 1) * 128 + (lane >> 3)) * 3072 + (1 + ty) * 1024 + cg * 128 + (((lane & 7) ^ (2 * g)) * 16), 8 * 3072, R1A + w * 16384);
}

template <bool KV>
DI void program(const Params& p, Ctx c ST_PARAM) {
  LAS unsigned char* L = c.L;
  const rsrc_t r = c.r;
  u32 tag;
  asm volatile("s_load_dword %0, %1, 0x0" : "=s"(tag) : "s"(p.ws));
  const int b = c.b, w = c.w, lane = c.lane, t8 = (w & 3) * 64 + lane;
  const int x8 = b & 7, hi = b >> 3, rb = b & 31, cg = b >> 5, hf = cg & 1, cq = cg >> 1, pair = x8 * 32 + hi;
  const int G = lane >> 4, q = (lane >> 2) & 3, pp = lane & 3, h = lane >> 5, n = lane & 31;
  const v4u z4 = {0u, 0u, 0u, 0u}, s4 = {~0u, ~0u, ~0u, ~0u};
  v4u W[32];
  v2u csf = {0u, 0u};
  v4u wph;
#define LOAD_WPH() { \
    const int head = KV ? 4 * cg + w : 4 * cg + (w & 2) + (n >> 4); \
    const GAS u16* wp = (const GAS u16*)p.wphi + head * 256 + (4 * h) * 16 + (n & 15); \
    u32 e[8]; \
    _Pragma("unroll") for (int j = 0; j < 8; ++j) e[j] = wp[(8 * (j >> 2) + (j & 3)) * 16]; \
    wph = v4u{e[0] | (e[1] << 16), e[2] | (e[3] << 16), e[4] | (e[5] << 16), e[6] | (e[7] << 16)}; \
    PIN(wph); }
#define LOAD_WPH_ISSUE() \
    u32 we[8]; { \
    const GAS u16* wp = (const GAS u16*)p.wphi + (4 * cg + (w & 2) + (n >> 4)) * 256 + (4 * h) * 16 + (n & 15); \
    _Pragma("unroll") for (int j = 0; j < 8; ++j) we[j] = wp[(8 * (j >> 2) + (j & 3)) * 16]; }
#define LOAD_WPH_FINISH() { wph = v4u{we[0] | (we[1] << 16), we[2] | (we[3] << 16), we[4] | (we[5] << 16), we[6] | (we[7] << 16)}; PIN(wph); }
#define PREFETCH_KV() { const int line = hi * 256 + t8; pf = *(const GAS u32*)((const GAS unsigned char*)p.wqkv + (line >> 4) * 3072 + 1024 + (line & 15) * 128); }
  const int ypos = B_YI + ((w & 3) * 16 + (lane >> 3)) * 32768 + pair * 128 + (lane & 7) * 16;
  if (!KV) {
    const int ct = w - 4, ks0 = 8 * cq + 2 * ct;
    if (w == 4 && lane < 16) *(LAS u32*)(L + L_C0 + lane * 4) = 0u;
    if (w == 5 && n < 30) *(LAS v4u*)(L + L_U + h * 4608 + 4096 + n * 16) = z4;
    nap<D_H>();
    v4u hv[2]; u32 hn[2];
#pragma unroll
    for (int cc = 0; cc < 2; ++cc) {
      hv[cc] = *(const GAS v4u*)(p.h + pair * 4096 + cc * 2048 + t8 * 8);
      hn[cc] = *(const GAS u32*)(p.h + pair * 4096 + cc * 2048 + min(t8 * 8 + 8, 2040));
    }
#define Q_ROWS() _Pragma("unroll") for (int i = 0; i < 8; ++i) \
      dma16<false>(p.q, (rb * 64 + hf * 32 + ct * 8 + i) * 1024 + ((lane ^ ((ct * 8 + i) & 15)) * 16), L_QR + (ct * 8 + i) * 1024);
    LOAD_WPH_ISSUE();
    const u32 dd = *(const GAS u32*)(p.dv + 2 * pair);
    asm volatile("s_waitcnt lgkmcnt(0)\n\ts_barrier\n\ts_add_u32 %0, %0, 1" : "+s"(tag) :: "memory", "scc");
    c.tag = tag;
    ST(1);
#pragma unroll
    for (int cc = 0; cc < 2; ++cc) {
      LAS unsigned char* hb = L + L_H + cc * 8320 + t8 * 16 + 32;
      const v4u a = hv[cc];
      *(LAS v4u*)hb = a;
      *(LAS v4u*)(hb + 4160) = v4u{__builtin_amdgcn_alignbit(a.y, a.x, 16), __builtin_amdgcn_alignbit(a.z, a.y, 16), __builtin_amdgcn_alignbit(a.w, a.z, 16), __builtin_amdgcn_alignbit(hn[cc], a.w, 16)};
      if (t8 == 0) {
        *(LAS v4u*)(hb - 32) = z4; *(LAS v4u*)(hb - 16) = z4; *(LAS v4u*)(hb + 4160 - 32) = z4;
        *(LAS v4u*)(hb + 4160 - 16) = v4u{0u, 0u, 0u, a.x << 16};
      }
    }
    arrive(L_C0);
    LOAD_WPH_FINISH();
    ST(2);
    const int f1 = B_FL + ((8 * ct + (lane & 7)) * 8 + x8) * 128;
    v4u fl[1], u[2];
    nap<D_F1>();
    fl[0] = ld16(r, f1);
    FENCE();
    until(L_C0, 4);
    const int ch = (w >> 1) & 1, rho = w & 1, i16 = lane & 15;
    LAS unsigned char* ab = L + L_H + ch * 8320 + ((i16 & 1) ? 0 : 4160) + 2 * (((i16 + 1) & ~1) + 8 * G) + 256 * rho;
#pragma unroll
    for (int i = 0; i < 16; ++i) {
      LAS u32* ap = (LAS u32*)(ab + 512 * (i & 7) + 64 * (i >> 3));
      W[i] = v4u{ap[0], ap[1], ap[2], ap[3]};
    }
    FENCE();
    if (!all_lanes(fl[0].y == tag && fl[0].w == tag)) STV(34, (fetch<1, 0>(r, fl, f1, 0, tag)));
    FENCE();
    ST(3);
    nap<D_UT>();
#pragma unroll
    for (int i = 0; i < 2; ++i) u[i] = ld16(r, B_UT + pair * 8192 + ct * 2048 + lane * 16 + i * 1024);
    FENCE();
#pragma unroll
    for (int i = 16; i < 32; ++i) {
      LAS u32* ap = (LAS u32*)(ab + 512 * (i & 7) + 64 * (i >> 3));
      W[i] = v4u{ap[0], ap[1], ap[2], ap[3]};
    }
    FENCE();
    if (!all_lanes(max(max(max(u[0].x, u[0].y), max(u[0].z, u[0].w)), max(max(u[1].x, u[1].y), max(u[1].z, u[1].w))) != ~0u))
      STV(35, (fetch<2, 1>(r, u, B_UT + pair * 8192 + ct * 2048 + lane * 16, 1024, 0u)));
    ST(4);
#pragma unroll
    for (int i = 0; i < 2; ++i) {
      *(LAS v2u*)(L + L_U + 4088 - 8 * (ct * 128 + i * 64 + lane)) = v2u{u[i].x, u[i].y};
      *(LAS v2u*)(L + L_U + 4608 + 4088 - 8 * (ct * 128 + i * 64 + lane)) = v2u{u[i].z, u[i].w};
    }
    LAS unsigned char* bb = L + L_U + ch * 4608 + 4064 - 3584 - 32 * i16 + 16 * G + 256 * rho;
    LAS unsigned char* pb = L + L_P + ct * 8192 + G * 256 + ((i16 ^ (8 * (G >> 1)) ^ (4 * ch)) * 16);
    if (PRIO) __builtin_amdgcn_s_setprio(2);
    BAR();
    ST(5);
    f32x4 acc[8];
#pragma unroll
    for (int i = 0; i < 8; ++i) acc[i] = f32x4{0.f, 0.f, 0.f, 0.f};
    v4u B[8];
#pragma unroll
    for (int i = 0; i < 8; ++i) B[i] = *(LAS v4u*)(bb + 512 * (7 - i));
    SB();
    const int cc2 = (lane >> 2) & 1;
    const float dc = cc2 ? bfhi(dd) : bflo(dd);
    v4u ur[2];
    float y[2][8];
#pragma unroll
    for (int rr = 0; rr < 4; ++rr) {
#pragma unroll
      for (int m = 0; m < 8; ++m) {
#pragma unroll
        for (int nn = 0; nn + m < 8; ++nn) {
          acc[nn + m] = mfma16(W[8 * rr + nn], B[m], acc[nn + m]);
          if (rr == 3 && m >= 1 && nn == 0) *(LAS f32x4*)(pb + (m - 1) * 1024) = acc[m - 1];
          if (rr == 2 && m == 5 && (nn == 0 || nn == 1)) ur[nn] = *(LAS v4u*)(L + L_U + cc2 * 4608 + 4080 - 16 * (ct * 64 + nn * 32 + 4 * (lane >> 3) + (lane & 3)));
          if (rr == 3 && m < 4 && nn >= 1 && nn <= 4) {
            const int e = 4 * m + nn - 1, i = e >> 3, k = e & 7;
            y[i][k] = ((k & 1) ? bflo(ur[i][3 - (k >> 1)]) : bfhi(ur[i][3 - (k >> 1)])) * dc;
            PIN(y[i][k]);
          }
          SB();
        }
        if (rr < 3) B[m] = *(LAS v4u*)(bb + 512 * (7 - m) + 64 * (rr + 1));
        SB();
      }
    }
    *(LAS f32x4*)(pb + 7 * 1024) = acc[7];
    ST(6);
    arrive(L_C0 + 4);
    until(L_C0 + 4, 8);
    {
      const int o = lane & 1, j = 2 * (lane >> 3) + ((lane >> 1) & 1);
#pragma unroll
      for (int i = 0; i < 2; ++i) {
#pragma unroll
        for (int rh = 0; rh < 2; ++rh)
#pragma unroll
          for (int e = 0; e < 2; ++e) {
            const f32x4 t = *(LAS f32x4*)(L + L_P + ((cc2 * 2 + rh) * 8 + 2 * ct + i) * 1024 + (2 * o + e) * 256 + ((j ^ (8 * o) ^ (4 * cc2)) * 16));
            y[i][4 * e] += t.x; y[i][4 * e + 1] += t.y; y[i][4 * e + 2] += t.z; y[i][4 * e + 3] += t.w;
          }
        st16(r, ypos + i * 8 * 32768, v4u{pk2(y[i][0], y[i][1]), pk2(y[i][2], y[i][3]), pk2(y[i][4], y[i][5]), pk2(y[i][6], y[i][7])});
      }
    }
    ST(7);
    until(L_C0 + 28, 4);
    f32x16 x;
    {
      LAS unsigned char* qb = L + L_QR + n * 1024;
      float s1 = 0.f, s2 = 0.f;
#pragma unroll
      for (int i = 0; i < 8; ++i) acc_stats(*(LAS v4u*)(qb + ((16 * ct + ((8 * h + i) ^ (n & 15))) * 16)), s1, s2);
      *(LAS f32x2*)(L + L_S0 + ct * 512 + n * 16 + h * 8) = f32x2{s1, s2};
#pragma unroll
      for (int i = 0; i < 4; ++i) {
        const v2u t = *(LAS v2u*)(qb + (((2 * ks0 + i) ^ (n & 15)) * 16) + h * 8);
        x[4 * i] = bflo(t.x); x[4 * i + 1] = bfhi(t.x); x[4 * i + 2] = bflo(t.y); x[4 * i + 3] = bfhi(t.y);
      }
    }
    arrive(L_C0 + 32);
    ST(29);
    nap<D_YI>();
    until(L_C0 + 20, 4);
    ST(8);
    const int ysrc = B_YI + (rb * 2 + hf) * 32768 + ks0 * 1024 + lane * 16;
    dma_run<true, 2>(p.ws, ysrc, 1024, L_H + ct * 2048);
    until(L_C0 + 32, 4);
    float m, rs;
    row_moments(L + L_S0, lane, m, rs);
    PIN(x); PIN(m); PIN(rs);
    v4u y0, y1;
    u32 spins = 0;
#pragma nounroll
    for (;; ++spins) {
      DRAIN();
      LAS unsigned char* yb = L + L_H + ct * 2048 + (4 * h + q) * 64 + 32 * (G & 1) + 8 * pp;
      y0 = trd2(yb, 512); y1 = trd2(yb + 1024, 512);
      float s1 = 0.f;
#pragma unroll
      for (int i = 0; i < 4; ++i) { s1 = dot2(y0[i], y0[i], s1); s1 = dot2(y1[i], y1[i], s1); }
      if (all_lanes(s1 == s1) || spins > SPIN_CAP) break;
      nap<D_RETRY>();
      dma_run<true, 2>(p.ws, ysrc, 1024, L_H + ct * 2048);
    }
    STV(36, spins);
    ST(9);
    gate_publish(x, rs, m * rs, y0, y1, r, 0, rb, hf * 32 + ks0, lane, tag);
    *(LAS v4u*)(L + L_Y + ct * 2048 + lane * 16) = y0; *(LAS v4u*)(L + L_Y + ct * 2048 + 1024 + lane * 16) = y1;
    if (w == 4 && lane < 8) *(LAS u32*)(L + L_C1 + lane * 4) = 0u;
    if (PRIO) __builtin_amdgcn_s_setprio(0);
  } else {
#pragma unroll
    for (int i = 0; i < 2; ++i) {
      const int lr = 8 * (2 * w + i) + (lane >> 3);
      dma16<false>(p.k, (64 * hi + lr) * 1024 + x8 * 128 + (((lane & 7) ^ (((lr + 4) >> 2) & 7)) * 16), L_KT + 512 + (2 * w + i) * 1024);
      dma16<false>(p.v, (64 * hi + lr) * 1024 + x8 * 128 + (((lane & 7) ^ ((lr >> 2) & 7)) * 16), L_VT + (2 * w + i) * 1024);
    }
    if (lane < 32) {
      if (w == 0) {
        if (hi > 0) dma16<false>(p.k, (64 * hi - 4 + (lane >> 3)) * 1024 + x8 * 128 + (lane & 7) * 16, L_KT);
        else *(LAS v4u*)(L + L_KT + lane * 16) = z4;
      }
      if (w == 1) dma16<false>(p.shk, x8 * 512 + lane * 16, L_C0 - 512);
    }
    const int rq = lane & 15, upos = B_UT + (x8 * 32 + 4 * w + G) * 8192 + (16 * hi + rq) * 16;
    st16(r, upos, s4); st16(r, upos + 16 * 8192, s4);
    DRAIN();
    asm volatile("s_waitcnt lgkmcnt(0)\n\ts_barrier\n\ts_add_u32 %0, %0, 1" : "+s"(tag) :: "memory", "scc");
    c.tag = tag;
    if (w == 3 && lane == 0) st16(r, B_FL + b * 128, v4u{tag, tag, tag, tag});
    ST(1);
#pragma unroll
    for (int it = 0; it < 2; ++it) {
      const int ck = w + 4 * it;
      const v4u sk = *(LAS v4u*)(L + L_C0 - 512 + (4 * ck + G) * 16);
      LAS unsigned char* ka = L + L_KT + (4 * rq) * 128 + ((ck ^ (rq & 7)) * 16) + G * 4;
      LAS unsigned char* kb = L + L_KT + (4 * rq + 4) * 128 + ((ck ^ ((rq + 1) & 7)) * 16) + G * 4;
      LAS unsigned char* va = L + L_VT + (4 * rq) * 128 + ((ck ^ (rq & 7)) * 16) + G * 4;
      u32 kk[7], vv[4];
#pragma unroll
      for (int i = 0; i < 3; ++i) kk[i] = *(LAS u32*)(ka + (i + 1) * 128);
#pragma unroll
      for (int i = 0; i < 4; ++i) { kk[3 + i] = *(LAS u32*)(kb + i * 128); vv[i] = *(LAS u32*)(va + i * 128); }
      const f32x2 w0 = {bflo(sk.x), bflo(sk.z)}, w1 = {bfhi(sk.x), bfhi(sk.z)}, w2 = {bflo(sk.y), bflo(sk.w)}, w3 = {bfhi(sk.y), bfhi(sk.w)};
      f32x2 kf[7], u[4];
#pragma unroll
      for (int i = 0; i < 7; ++i) kf[i] = f32x2{bflo(kk[i]), bfhi(kk[i])};
#pragma unroll
      for (int i = 0; i < 4; ++i) u[i] = (kf[i] * w3 + kf[i + 1] * w2 + kf[i + 2] * w1 + kf[i + 3] * w0) * f32x2{bflo(vv[i]), bfhi(vv[i])};
      st16(r, upos + it * 16 * 8192, v4u{pk2(u[3].x, u[2].x), pk2(u[1].x, u[0].x), pk2(u[3].y, u[2].y), pk2(u[1].y, u[0].y)});
    }
    ST(2);
#define CLEARS() { \
      st16(r, ypos, s4); st16(r, ypos + 8 * 32768, s4); \
      const bool kvr = w < 2, on = lane < 8 && (kvr || (b & 7) == 0); \
      if (on) st16(r, (kvr ? B_KVR + w * 32768 + b * 128 : B_ZR + (w - 2) * 4096 + (b >> 3) * 128) + lane * 16, s4); \
      FENCE(); }
#define CLEARED() { \
      arrive(L_C0 + 4); \
      arrive(L_C0 + 24); \
      if (w == 3) { \
        until(L_C0 + 24, 4); \
        if (lane == 0) st16(r, B_FL + 32768 + b * 128, v4u{tag, tag, tag, tag}); \
      } }
    LOAD_WPH();
    nap<D_CL>();
    CLEARS();
    u32 pf = 0;
    if (PF_W == 1) PREFETCH_KV();
    ST(3);
    BAR();
    ST(4);
    DRAIN();
    CLEARED();
    {
      const int ct = w;
      Q_ROWS();
    }
    if (PF_W == 2) PREFETCH_KV();
    stage_kv(p, 0, cg, w, lane);
    VMW(PF_W == 2 ? 17 : 16);
    arrive(L_C0 + 28);
    {
      const rsrc_t rw = __builtin_amdgcn_make_buffer_rsrc((void*)p.wqkv, 0, 512 * 3072, 0x00020000);
      const int src = (256 + (w & 1) * 128 + (lane >> 3)) * 3072 + (1 + (w >> 1)) * 1024 + cg * 128 + (((lane & 7) ^ (2 * ((G & 1) ^ (w & 2)))) * 16);
#pragma unroll
      for (int i = 0; i < 16; ++i) W[16 + i] = __builtin_amdgcn_raw_buffer_load_b128(rw, src, i * 8 * 3072, 0);
      FENCE();
    }
    VMW(16);
    arrive(L_C0 + 12);
    {
      v4u fl[1];
      const int pr = 64 * cq + 16 * w + (lane & 15);
      STV(39, (fetch<1, 0>(r, fl, B_FL + 32768 + ((pr & 31) * 8 + (pr >> 5)) * 128, 0, tag)));
      arrive(L_C0 + 20);
    }
    asm volatile("" ::"v"(pf));
    until<1>(L_C0 + 12, 4);
    ST(5);
    LAS unsigned char* wb = L + R1A + (G & 1) * 32768 + (4 * h + q) * 128 + ((w ^ (q >> 1) ^ (2 * (G & 1))) * 32) + 8 * pp;
#pragma unroll
    for (int i = 0; i < 16; ++i) W[i] = trd2(wb + i * 2048, 1024);
    arrive(L_C0 + 8);
    until<1>(L_C0 + 8, 4);
    ST(6);
#pragma unroll
    for (int i = 0; i < 16; ++i) *(LAS v4u*)(L + R1A + (w * 16 + i) * 1024 + lane * 16) = W[16 + i];
    arrive(L_C0 + 16);
    until<1>(L_C0 + 16, 4);
    ST(7);
#pragma unroll
    for (int i = 0; i < 16; ++i) W[16 + i] = trd2(wb + i * 2048, 1024);
    until<1>(L_C0 + 32, 4);
    csf = colsum<0, 32>(W);
    ST(9);
  }
  BAR();
  ST(28);
  STV(57, load_image(p, r, L, 0, rb, w, lane, tag));
  BAR();
  ST(10);
  layer<0, KV>(p, c, W, csf, wph ST_ARG);
  if (KV && b == 0 && w == 0 && lane == 0) __builtin_amdgcn_raw_buffer_store_b32(tag + 1u, r, WS_EPOCH, 0, SC1);
  layer<1, KV>(p, c, W, csf, wph ST_ARG);
}

__global__ void __launch_bounds__(512, 2) fused_kernel(Params p) {
  extern __shared__ __attribute__((aligned(16))) unsigned char smem[];
  LAS unsigned char* L = (LAS unsigned char*)smem;
  const int tid = threadIdx.x, lane = tid & 63, w = __builtin_amdgcn_readfirstlane(tid >> 6), b = blockIdx.x;
  const rsrc_t r = __builtin_amdgcn_make_buffer_rsrc((void*)p.ws, 0, WS_BYTES, 0x00020000);
  u32 stampv = 0;
  ST(0);
  STC(32);
  asm volatile("" ::"s"(p.q), "s"(p.k), "s"(p.v), "s"(p.shk), "s"(p.h), "s"(p.ws));
  const Ctx c = {L, r, 0u, b, w, lane};
  if (w >= 4) program<false>(p, c ST_ARG);
  else program<true>(p, c ST_ARG);
  ST(30);
  STC(33);
  ST_FLUSH();
}

extern "C" void kernel_launch(void* const* d_in, const int* in_sizes, int n_in, void* d_out, int out_size, void* d_ws, size_t ws_size,
                              hipStream_t stream) {
  Params p;
  p.q = (const u16*)d_in[0]; p.k = (const u16*)d_in[1]; p.v = (const u16*)d_in[2]; p.shk = (const u16*)d_in[3];
  p.h = (const u16*)d_in[4]; p.dv = (const u16*)d_in[5]; p.wqkv = (const u16*)d_in[7]; p.wphi = (const u16*)d_in[8];
  p.wo = (const u16*)d_in[9]; p.out = (u16*)d_out; p.ws = (unsigned char*)d_ws + (ws_size - WS_BYTES);
  (void)hipFuncSetAttribute((const void*)fused_kernel, hipFuncAttributeMaxDynamicSharedMemorySize, LDS_BYTES);
  hipLaunchKernelGGL(fused_kernel, dim3(256), dim3(512), LDS_BYTES, stream, p);
}
```

```cpp
#include <hip/hip_runtime.h>
#include <hip/hip_bf16.h>

typedef unsigned short u16;
typedef unsigned u32;
typedef unsigned long long u64;
typedef u32 v4u __attribute__((ext_vector_type(4)));
typedef u32 v2u __attribute__((ext_vector_type(2)));
typedef short v4s __attribute__((ext_vector_type(4)));
typedef float f32x2 __attribute__((ext_vector_type(2)));
typedef float f32x4 __attribute__((ext_vector_type(4)));
typedef float f32x16 __attribute__((ext_vector_type(16)));
typedef __bf16 v8bf __attribute__((ext_vector_type(8)));
typedef __bf16 v2bf __attribute__((ext_vector_type(2)));
#define LAS __attribute__((address_space(3)))
#define GAS __attribute__((address_space(1)))
#define DI __device__ __forceinline__
template <class T, class F> DI T bc(F x) { return __builtin_bit_cast(T, x); }
#define BC(T, x) bc<T>(x)
typedef __amdgpu_buffer_rsrc_t rsrc_t;

constexpr int MB = 1 << 20;
constexpr int WS_EPOCH = 0;
constexpr int B_KVR = 4096;
constexpr int B_ZR = B_KVR + 64 * 1024;
constexpr int B_ZP = 256 * 1024;
constexpr int B_SP = 384 * 1024;
constexpr int B_OR = 640 * 1024;
constexpr int B_UR = 896 * 1024;
constexpr int B_YR = 1152 * 1024;
constexpr int B_XR = 1408 * 1024;
constexpr int B_UT = 2 * MB;
constexpr int B_YI = 4 * MB;
constexpr int B_STAMP = 6 * MB;
constexpr int WS_BYTES = 8 * MB;

constexpr int R1A = 0, R1B = 65536, R2 = 98304, LDS_BYTES = 163840;
constexpr int L_H = R2, L_U = R2 + 16640, L_P = R2 + 25856, L_S0 = R2 + 59136, L_C0 = R2 + 65536 - 64;
constexpr int L_KT = L_P, L_VT = L_P + 9216;
constexpr int L_QR = R1B;
constexpr int L_ST = R1B, L_EX = R1B + 4096, L_C1 = R1B + 20480, L_Y = R1B + 24576;

constexpr u32 SPIN_CAP = 1u << 14;
constexpr int SC1 = 16;
constexpr float LOG2E = 1.4426950408889634f;
constexpr u32 ONES = 0x3f803f80u;

#define D_UT 8
#define D_YI 16
#define D_KVP 18
#define D_OI 18
#define D_KVR 16
#define D_SP 14
#define D_XI 14
#define D_XQ 2
#define Q_LATE
#define OUT_AUX 16
#define WO2 1
#define WO1 1
#define CS_EARLY 0
#define D_X0 2
#define D_H 3
#define D_CL 8
#define PRIO 1
#define HOLD 1
#define TRIG 24
#define PE 0
#define PT 7
#define QL 0
#define D_RETRY 0

struct Params {
  const u16 *q, *k, *v, *shk, *h, *dv, *wqkv, *wphi, *wo;
  u16* out;
  unsigned char* ws;
};

DI float bflo(u32 x) { return BC(float, x << 16); }
DI float bfhi(u32 x) { return BC(float, x & 0xffff0000u); }
DI u32 pk2(float a, float b) { f32x2 p = {a, b}; return BC(u32, __builtin_convertvector(p, v2bf)); }
DI float op(float r) { asm("" : "+v"(r)); return r; }
DI float mx3(float a, float b, float c) { return __builtin_fmaxf(__builtin_fmaxf(a, b), c); }
DI f32x16 mfma32(v4u a, v4u b, f32x16 c) { return __builtin_amdgcn_mfma_f32_32x32x16_bf16(BC(v8bf, a), BC(v8bf, b), c, 0, 0, 0); }
DI f32x4 mfma16(v4u a, v4u b, f32x4 c) { return __builtin_amdgcn_mfma_f32_16x16x32_bf16(BC(v8bf, a), BC(v8bf, b), c, 0, 0, 0); }
DI float dot2(u32 a, u32 b, float c) { return __builtin_amdgcn_fdot2_f32_bf16(BC(v2bf, a), BC(v2bf, b), c, false); }
DI v2u trd(LAS unsigned char* p) { return BC(v2u, __builtin_amdgcn_ds_read_tr16_b64_v4i16((LAS v4s*)p)); }
DI v4u trd2(LAS unsigned char* p, int second) { v2u a = trd(p), b = trd(p + second); return v4u{a.x, a.y, b.x, b.y}; }
DI v4u pack8(const f32x16& x, int s) { return v4u{pk2(x[8 * s], x[8 * s + 1]), pk2(x[8 * s + 2], x[8 * s + 3]), pk2(x[8 * s + 4], x[8 * s + 5]), pk2(x[8 * s + 6], x[8 * s + 7])}; }
DI void swap32(float& a, float& b) { auto r = __builtin_amdgcn_permlane32_swap(BC(u32, a), BC(u32, b), false, false); a = BC(float, r[0]); b = BC(float, r[1]); }
DI void swap16(float& a, float& b) { auto r = __builtin_amdgcn_permlane16_swap(BC(u32, a), BC(u32, b), false, false); a = BC(float, r[0]); b = BC(float, r[1]); }
DI void swap32u(u32& a, u32& b) { auto r = __builtin_amdgcn_permlane32_swap(a, b, false, false); a = r[0]; b = r[1]; }
template <int CTRL> DI u32 dppu(u32 x) { return (u32)__builtin_amdgcn_update_dpp(0, (int)x, CTRL, 0xf, 0xf, true); }
template <int CTRL> DI float dppf(float x) { return BC(float, dppu<CTRL>(BC(u32, x))); }
DI bool all_lanes(bool ok) { return __builtin_amdgcn_ballot_w64(!ok) == 0ull; }
#define BAR() asm volatile("s_waitcnt lgkmcnt(0)\n\ts_barrier" ::: "memory")
#define DRAIN() asm volatile("s_waitcnt vmcnt(0)" ::: "memory")
#define VMW(n) asm volatile("s_waitcnt vmcnt(%0)" ::"n"(n) : "memory")
#define PIN(x) asm volatile("" : "+v"(x))
#define SB() __builtin_amdgcn_sched_barrier(0)
#define FENCE() asm volatile("" ::: "memory")
template <int I> struct IC { static constexpr int v = I; };
template <int I, int N, class F> DI void sfor(F&& f) { if constexpr (I < N) { f(IC<I>{}); sfor<I + 1, N>(f); } }
template <int N> DI void nap() { if (N > 0) __builtin_amdgcn_s_sleep(N); }

DI v4u srd(const void* base, u32 bytes) { const u64 a = (u64)base; return v4u{(u32)a, (u32)(a >> 32) & 0xffffu, bytes, 0x00020000u}; }
template <bool COH, int N, int SS, bool PAIRS = false> DI void dma_run(v4u d, u32 voff, u32 ldst) {
  u32 keep, so;
  ldst = __builtin_amdgcn_readfirstlane(ldst);
#define DMA_BODY(C) \
  if (PAIRS) asm volatile("s_nop 4\n\ts_mov_b32 %0, m0\n\ts_mov_b32 m0, %4\n\ts_mov_b32 %1, 0\n\t.rept %5\n\tbuffer_load_dwordx4 %2, %3, %1 offen" C " lds\n\t" \
                          "buffer_load_dwordx4 %2, %3, %1 offen offset:1024" C " lds\n\ts_add_u32 m0, m0, 0x800\n\ts_add_u32 %1, %1, %6\n\t.endr\n\ts_mov_b32 m0, %0" \
                          : "=&s"(keep), "=&s"(so) : "v"(voff), "s"(d), "s"(ldst), "n"(N / 2), "n"(SS) : "memory", "scc"); \
  else asm volatile("s_nop 4\n\ts_mov_b32 %0, m0\n\ts_mov_b32 m0, %4\n\ts_mov_b32 %1, 0\n\t.rept %5\n\tbuffer_load_dwordx4 %2, %3, %1 offen" C " lds\n\t" \
                    "s_add_u32 m0, m0, 0x400\n\ts_add_u32 %1, %1, %6\n\t.endr\n\ts_mov_b32 m0, %0" \
                    : "=&s"(keep), "=&s"(so) : "v"(voff), "s"(d), "s"(ldst), "n"(N), "n"(SS) : "memory", "scc");
  if (COH) { DMA_BODY(" sc1") } else { DMA_BODY("") }
}
DI v4u ld16(rsrc_t r, int off) { return __builtin_amdgcn_raw_buffer_load_b128(r, off, 0, SC1); }
DI u32 ld4(rsrc_t r, int off) { return __builtin_amdgcn_raw_buffer_load_b32(r, off, 0, SC1); }
DI u32 sum4(v4u x, u32 a) { return (a + x.x + x.y) + x.z + x.w; }
DI void st16(rsrc_t r, int off, v4u v) { __builtin_amdgcn_raw_buffer_store_b128(v, r, off, 0, SC1); }

template <int STEP = 1024> DI void publish2(rsrc_t r, int off, int roff, v4u a, v4u b, u32 tag) {
  st16(r, off, a); st16(r, off + STEP, b);
  __builtin_amdgcn_raw_buffer_store_b32(sum4(b, sum4(a, tag)), r, roff, 0, SC1);
}

DI void arrive(u32 ctr) {
  asm volatile("s_mov_b64 exec, 1\n\tds_add_u32 %0, %1\n\ts_mov_b64 exec, -1" ::"v"(ctr), "v"(1u) : "memory");
}
template <int NAP = 0> DI void until(u32 ctr, u32 target) {
  u32 t, u;
  if (NAP) asm volatile("s_branch 2f\n1:\n\ts_sleep %4\n2:\n\tds_read_b32 %0, %2\n\ts_waitcnt lgkmcnt(0)\n\tv_readfirstlane_b32 %1, %0\n\ts_cmp_lt_u32 %1, %3\n\ts_cbranch_scc1 1b" : "=&v"(t), "=&s"(u) : "v"(ctr), "s"(target), "n"(NAP) : "memory", "scc");
  else asm volatile("1:\n\tds_read_b32 %0, %2\n\ts_waitcnt lgkmcnt(0)\n\tv_readfirstlane_b32 %1, %0\n\ts_cmp_lt_u32 %1, %3\n\ts_cbranch_scc1 1b" : "=&v"(t), "=&s"(u) : "v"(ctr), "s"(target) : "memory", "scc");
}

DI v4u tag4(u32 t) { return v4u{t & 0xffu, (t + 64u) & 0xffu, (t + 128u) & 0xffu, (t + 192u) & 0xffu}; }
template <int N, int MODE> DI u32 fetch(rsrc_t r, v4u (&v)[N], int off, int stride, u32 tag) {
  u32 spins = 0;
#pragma nounroll
  for (;; ++spins) {
    FENCE();
#pragma unroll
    for (int i = 0; i < N; ++i) v[i] = ld16(r, off + i * stride);
    u32 a = 0u;
#pragma unroll
    for (int i = 0; i < N; ++i) {
      if (MODE == 0) a |= (v[i].y ^ tag) | (v[i].w ^ tag);
      if (MODE == 1) a = max(max(a, max(v[i].x, v[i].y)), max(v[i].z, v[i].w));
      if (MODE == 2) { v[i] ^= tag4(tag); a |= (v[i].x | v[i].y) | (v[i].z | v[i].w); }
    }
    const bool ok = MODE == 0 ? a == 0u : MODE == 1 ? a != ~0u : (a & 0xffu) == 0u;
    if (all_lanes(ok) || spins > SPIN_CAP) break;
    nap<D_RETRY>();
  }
  FENCE();
  return spins;
}

#define STV(i, v) (void)(v)
#define STC(i)
#define ST_PARAM
#define ST_ARG
#define ST_FLUSH()
#define ST(i)

DI void dual_softmax(const f32x16& x, int o, float c, v4u& plus, v4u& minus) {
  float mx = mx3(mx3(x[o], x[o + 1], x[o + 2]), mx3(x[o + 3], x[o + 4], x[o + 5]), mx3(x[o + 6], x[o + 7], x[o]));
  float nm = mx3(mx3(-x[o], -x[o + 1], -x[o + 2]), mx3(-x[o + 3], -x[o + 4], -x[o + 5]), mx3(-x[o + 6], -x[o + 7], -x[o]));
  swap32(mx, nm);
  float t = mx3(mx, nm, nm) * -c, t2 = t;
  swap32(t, t2);
  f32x2 ep[4], em[4];
  const f32x2 c2 = {c, c}, tp = {t, t}, tm = {t2, t2};
#pragma unroll
  for (int i = 0; i < 4; ++i) { const f32x2 v = {x[o + 2 * i], x[o + 2 * i + 1]}; ep[i] = __builtin_elementwise_fma(v, c2, tp); em[i] = __builtin_elementwise_fma(-v, c2, tm); }
#pragma unroll
  for (int i = 0; i < 4; ++i) {
    ep[i] = f32x2{__builtin_amdgcn_exp2f(ep[i].x), __builtin_amdgcn_exp2f(ep[i].y)};
    em[i] = f32x2{__builtin_amdgcn_exp2f(em[i].x), __builtin_amdgcn_exp2f(em[i].y)};
  }
  const f32x2 sp2 = (ep[0] + ep[1]) + (ep[2] + ep[3]), sm2 = (em[0] + em[1]) + (em[2] + em[3]);
  float sp = op(sp2.x + sp2.y), sm = op(sm2.x + sm2.y);
  swap32(sp, sm);
  float ip = __builtin_amdgcn_rcpf(sp + sm), im = ip;
  swap32(ip, im);
#pragma unroll
  for (int i = 0; i < 4; ++i) {
    plus[i] = BC(u32, __builtin_convertvector(ep[i] * ip, v2bf));
    minus[i] = BC(u32, __builtin_convertvector(em[i] * im, v2bf));
  }
}

struct Ctx {
  LAS unsigned char* L;
  rsrc_t r;
  u32 tag;
  int b, w, lane;
};

template <int N> DI void gemm(f32x16& acc, const v4u (&W)[32], LAS unsigned char* xb) {
  constexpr int PD = 4;
  v4u x[PD];
#pragma unroll
  for (int i = 0; i < PD; ++i) x[i] = *(LAS v4u*)(xb + i * 1024);
  SB();
#pragma unroll
  for (int i = 0; i < N; ++i) {
    const v4u cur = x[i % PD];
    acc = mfma32(W[i], cur, acc);
    if (i + PD < N) x[i % PD] = *(LAS v4u*)(xb + (i + PD) * 1024);
    SB();
  }
  PIN(acc);
}
DI void row_moments(LAS unsigned char* st, int lane, float& m, float& rs) {
  LAS f32x4* s = (LAS f32x4*)(st + (lane & 31) * 16);
  const f32x4 a = s[0], b = s[32], c = s[64], d = s[96];
  m = op(op(op(a.x + a.z) + op(b.x + b.z)) + op(op(c.x + c.z) + op(d.x + d.z))) * (-1.f / 512.f);
  const float e2 = op(op(op(a.y + a.w) + op(b.y + b.w)) + op(op(c.y + c.w) + op(d.y + d.w))) * (1.f / 512.f);
  rs = __builtin_amdgcn_rsqf(mx3(__builtin_fmaf(-m, m, op(e2)), 0.f, 0.f) + 1e-5f);
}
DI void row_stats(LAS unsigned char* L, int T, int lane, u32& mf, float& rs) {
  LAS f32x4* s = (LAS f32x4*)(L + L_ST + T * 1024 + (lane & 31) * 16);
  const f32x4 a = s[0], b = s[32];
  const float m = op(op(a.x + a.y) + op(a.z + a.w)) * (-1.f / 512.f);
  const float e2 = op(op(b.x + b.y) + op(b.z + b.w)) * (1.f / 512.f);
  rs = __builtin_amdgcn_rsqf(mx3(__builtin_fmaf(-m, m, op(e2)), 0.f, 0.f) + 1e-5f);
  const float hi = bflo(pk2(m, m));
  mf = pk2(hi, op(m - hi));
}
struct Stat {
  f32x4 a, b;
  float m, e2, rs, hi;
  u32 mf;
  template <int S> DI void step(LAS unsigned char* s) {
    if constexpr (S == 0) { a = *(LAS f32x4*)s; b = *(LAS f32x4*)(s + 512); }
    if constexpr (S == 3) { m = op(a.x + a.y); hi = op(a.z + a.w); e2 = op(b.x + b.y); rs = op(b.z + b.w); }
    if constexpr (S == 4) { m = op(op(m + hi) * (-1.f / 512.f)); e2 = op(op(e2 + rs) * (1.f / 512.f)); }
    if constexpr (S == 5) { e2 = op(mx3(__builtin_fmaf(-m, m, e2), 0.f, 0.f) + 1e-5f); hi = op(bflo(pk2(m, m))); }
    if constexpr (S == 6) { rs = __builtin_amdgcn_rsqf(e2); mf = pk2(hi, op(m - hi)); PIN(rs); PIN(mf); }
  }
};
DI void acc_stats(v4u x, float& s1, float& s2) {
  s1 = dot2(x.x, ONES, s1); s2 = dot2(x.x, x.x, s2); s1 = dot2(x.y, ONES, s1); s2 = dot2(x.y, x.y, s2);
  s1 = dot2(x.z, ONES, s1); s2 = dot2(x.z, x.z, s2); s1 = dot2(x.w, ONES, s1); s2 = dot2(x.w, x.w, s2);
}
template <int N0, int N1> DI void cs_acc(const v4u (&W)[32], float& cs, float& cs2) {
#pragma unroll
  for (int i = N0; i < N1; ++i) { cs = dot2(W[i].x, ONES, cs); cs2 = dot2(W[i].y, ONES, cs2); cs = dot2(W[i].z, ONES, cs); cs2 = dot2(W[i].w, ONES, cs2); }
}
DI v2u cs_pack(float cs) {
  const u32 hh = pk2(cs, cs);
  const float lo = cs - bflo(hh);
  v2u o = {hh, pk2(lo, lo)};
  PIN(o);
  return o;
}
template <int N0, int N1> DI v2u colsum(const v4u (&W)[32]) {
  float cs = 0.f, cs2 = 0.f;
  cs_acc<N0, N1>(W, cs, cs2);
  return cs_pack(cs + cs2);
}
DI u32 load_image(const Params& p, rsrc_t r, LAS unsigned char* L, int rb, int w, int lane, u32 tag) {
  const int f0 = w * 8;
  u32 spins = 0;
  v2u rec[4];
#pragma nounroll
  for (;; ++spins) {
    dma_run<true, 8, 131072, true>(srd(p.ws, WS_BYTES), B_UT + (w & 3) * 524288 + (rb * 2 + (w >> 2)) * 2048 + lane * 16, R2 + f0 * 1024);
#pragma unroll
    for (int i = 0; i < 4; ++i) rec[i] = __builtin_amdgcn_raw_buffer_load_b64(r, B_XR + rb * 16384 + f0 * 256 + i * 512 + lane * 8, 0, SC1);
    u32 cs = 4u * tag;
    LAS v4u* x0 = (LAS v4u*)(L + R2 + f0 * 1024 + lane * 16);
    VMW(8); cs = sum4(x0[64], sum4(x0[0], cs)); cs = sum4(x0[192], sum4(x0[128], cs));
    VMW(4); cs = sum4(x0[320], sum4(x0[256], cs)); cs = sum4(x0[448], sum4(x0[384], cs));
    FENCE();
    if (all_lanes(cs + (rec[0].x + rec[1].x + rec[2].x + rec[3].x) == rec[0].y + rec[1].y + rec[2].y + rec[3].y) || spins > SPIN_CAP) break;
    nap<D_RETRY>();
  }
  *(LAS float*)(L + L_ST + (w >> 2) * 1024 + lane * 16 + (w & 3) * 4) = op(BC(float, rec[0].x) + BC(float, rec[1].x)) + op(BC(float, rec[2].x) + BC(float, rec[3].x));
  arrive(L_C1 + 20 + (w >> 2) * 4);
  return spins;
}
DI void gate_publish(f32x16& acc, float rs, float mr, v4u y0, v4u y1, rsrc_t r, int rb, int hf, int ks0, int lane, u32 tag) {
#pragma unroll
  for (int i = 0; i < 4; ++i) {
    acc[2 * i] = __builtin_fmaf(__builtin_fmaf(acc[2 * i], rs, mr), bflo(y0[i]), acc[2 * i]);
    acc[2 * i + 1] = __builtin_fmaf(__builtin_fmaf(acc[2 * i + 1], rs, mr), bfhi(y0[i]), acc[2 * i + 1]);
    acc[8 + 2 * i] = __builtin_fmaf(__builtin_fmaf(acc[8 + 2 * i], rs, mr), bflo(y1[i]), acc[8 + 2 * i]);
    acc[9 + 2 * i] = __builtin_fmaf(__builtin_fmaf(acc[9 + 2 * i], rs, mr), bfhi(y1[i]), acc[9 + 2 * i]);
  }
  const v4u a = pack8(acc, 0), b = pack8(acc, 1);
  const int off = B_UT + ks0 * 65536 + (rb * 2 + hf) * 2048 + lane * 16;
  st16(r, off, a); st16(r, off + 1024, b);
  FENCE();
  float s1 = 0.f, s2 = 0.f, t1 = 0.f, t2 = 0.f;
  acc_stats(a, s1, s2); acc_stats(b, t1, t2);
  s1 += t1; s2 += t2;
  swap32(s1, s2);
  const u32 st = BC(u32, s1 + s2);
  __builtin_amdgcn_raw_buffer_store_b64(v2u{st, sum4(b, sum4(a, tag + st))}, r, B_XR + rb * 16384 + (hf * 32 + ks0) * 256 + lane * 8, 0, SC1);
}

struct Post {
  v4u selp, selm, kb, vb, fp, fm, v0, v1;
  u32 ones;
  f32x16 pr, vt, pt;
  float vs[8], ep[8], em[8], mx, nm, t, t2, cc, s0, s1, s2, s3;
  DI void init(int n, int h) {
    const int n16 = n & 15, j = 4 * (n16 >> 3) + (n16 & 3);
    const u32 one = (((n16 >> 2) & 1) == h) ? (0x3f80u << (16 * (j & 1))) : 0u;
    const u32 op_ = n < 16 ? one : 0u, om = n < 16 ? 0u : one;
    selp = v4u{(j >> 1) == 0 ? op_ : 0u, (j >> 1) == 1 ? op_ : 0u, (j >> 1) == 2 ? op_ : 0u, (j >> 1) == 3 ? op_ : 0u};
    selm = v4u{(j >> 1) == 0 ? om : 0u, (j >> 1) == 1 ? om : 0u, (j >> 1) == 2 ? om : 0u, (j >> 1) == 3 ? om : 0u};
    ones = n < 16 ? 0u : ONES;
    PIN(selp); PIN(selm); PIN(ones);
  }
  template <int S> DI void step(const f32x16& a, float rs, v4u wph, f32x16& kv, bool first) {
    f32x16 zero;
#pragma unroll
    for (int i = 0; i < 16; ++i) zero[i] = 0.f;
    if constexpr (S == 0) { kb = pack8(a, 0); cc = rs * LOG2E; }
    if constexpr (S == 1) { pr = mfma32(wph, kb, zero); _Pragma("unroll") for (int i = 0; i < 8; ++i) vs[i] = op(a[8 + i] * rs); }
    if constexpr (S == 2) vb = v4u{pk2(vs[0], vs[1]), pk2(vs[2], vs[3]), pk2(vs[4], vs[5]), pk2(vs[6], vs[7])};
    if constexpr (S == 3) {
      PIN(pr);
      vt = mfma32(vb, selp, zero);
      mx = mx3(mx3(pr[0], pr[1], pr[2]), mx3(pr[3], pr[4], pr[5]), mx3(pr[6], pr[7], pr[0]));
      nm = mx3(mx3(-pr[0], -pr[1], -pr[2]), mx3(-pr[3], -pr[4], -pr[5]), mx3(-pr[6], -pr[7], -pr[0]));
    }
    if constexpr (S == 4) { swap32(mx, nm); t = mx3(mx, nm, nm) * -cc; t2 = t; }
    if constexpr (S == 5) { swap32(t, t2); v0.x = pk2(vt[0], vt[1]) | ones; }
    if constexpr (S == 6) _Pragma("unroll") for (int i = 0; i < 5; ++i) ep[i] = op(__builtin_fmaf(pr[i], cc, t));
    if constexpr (S == 7) { _Pragma("unroll") for (int i = 5; i < 8; ++i) ep[i] = op(__builtin_fmaf(pr[i], cc, t)); _Pragma("unroll") for (int i = 0; i < 2; ++i) em[i] = op(__builtin_fmaf(pr[i], -cc, t2)); }
    if constexpr (S == 8) _Pragma("unroll") for (int i = 2; i < 7; ++i) em[i] = op(__builtin_fmaf(pr[i], -cc, t2));
    if constexpr (S == 9) { em[7] = op(__builtin_fmaf(pr[7], -cc, t2)); ep[0] = __builtin_amdgcn_exp2f(ep[0]); ep[1] = __builtin_amdgcn_exp2f(ep[1]); }
    if constexpr (S == 10) { ep[2] = __builtin_amdgcn_exp2f(ep[2]); ep[3] = __builtin_amdgcn_exp2f(ep[3]); s0 = op(ep[0] + ep[1]); }
    if constexpr (S == 11) { ep[4] = __builtin_amdgcn_exp2f(ep[4]); ep[5] = __builtin_amdgcn_exp2f(ep[5]); s1 = op(ep[2] + ep[3]); }
    if constexpr (S == 12) { ep[6] = __builtin_amdgcn_exp2f(ep[6]); ep[7] = __builtin_amdgcn_exp2f(ep[7]); s0 = op(s0 + s1); }
    if constexpr (S == 13) { em[0] = __builtin_amdgcn_exp2f(em[0]); em[1] = __builtin_amdgcn_exp2f(em[1]); s1 = op(ep[4] + ep[5]); }
    if constexpr (S == 14) { em[2] = __builtin_amdgcn_exp2f(em[2]); em[3] = __builtin_amdgcn_exp2f(em[3]); s2 = op(ep[6] + ep[7]); }
    if constexpr (S == 15) { em[4] = __builtin_amdgcn_exp2f(em[4]); em[5] = __builtin_amdgcn_exp2f(em[5]); s1 = op(s1 + s2); }
    if constexpr (S == 16) { em[6] = __builtin_amdgcn_exp2f(em[6]); em[7] = __builtin_amdgcn_exp2f(em[7]); s0 = op(s0 + s1); }
    if constexpr (S == 17) { s1 = op(em[0] + em[1]); s2 = op(em[2] + em[3]); s3 = op(em[4] + em[5]); t = op(em[6] + em[7]); s1 = op(s1 + s2); }
    if constexpr (S == 18) { s3 = op(s3 + t); v0.y = pk2(vt[2], vt[3]) | ones; s1 = op(s1 + s3); }
    if constexpr (S == 19) { swap32(s0, s1); t = __builtin_amdgcn_rcpf(s0 + s1); }
    if constexpr (S == 20) { t2 = t; v0.z = pk2(vt[4], vt[5]) | ones; swap32(t, t2); }
    if constexpr (S == 21) _Pragma("unroll") for (int i = 0; i < 5; ++i) ep[i] = op(ep[i] * t);
    if constexpr (S == 22) { _Pragma("unroll") for (int i = 5; i < 8; ++i) ep[i] = op(ep[i] * t); fp.x = pk2(ep[0], ep[1]); fp.y = pk2(ep[2], ep[3]); }
    if constexpr (S == 23) { fp.z = pk2(ep[4], ep[5]); fp.w = pk2(ep[6], ep[7]); _Pragma("unroll") for (int i = 0; i < 3; ++i) em[i] = op(em[i] * t2); }
    if constexpr (S == 24) { pt = mfma32(fp, selp, zero); _Pragma("unroll") for (int i = 3; i < 8; ++i) em[i] = op(em[i] * t2); fm = v4u{pk2(em[0], em[1]), pk2(em[2], em[3]), pk2(em[4], em[5]), pk2(em[6], em[7])}; }
    if constexpr (S == 25) { pt = mfma32(fm, selm, pt); v0.w = pk2(vt[6], vt[7]) | ones; v1.x = pk2(vt[8], vt[9]) | ones; v1.y = pk2(vt[10], vt[11]) | ones; v1.z = pk2(vt[12], vt[13]) | ones; v1.w = pk2(vt[14], vt[15]) | ones; }
    if constexpr (S == 27) fp = pack8(pt, 0);
    if constexpr (S == 28) { kv = mfma32(fp, v0, first ? zero : kv); fm = pack8(pt, 1); }
    if constexpr (S == 29) kv = mfma32(fm, v1, kv);
  }
  DI void tail(const f32x16& a, float rs, v4u wph, f32x16& kv) {
    f32x16 zero;
#pragma unroll
    for (int i = 0; i < 16; ++i) zero[i] = 0.f;
    pr = mfma32(wph, pack8(a, 0), zero);
    vb = v4u{pk2(op(a[8] * rs), op(a[9] * rs)), pk2(op(a[10] * rs), op(a[11] * rs)), pk2(op(a[12] * rs), op(a[13] * rs)), pk2(op(a[14] * rs), op(a[15] * rs))};
    vt = mfma32(vb, selp, zero);
    dual_softmax(pr, 0, rs * LOG2E, fp, fm);
    pt = mfma32(fp, selp, zero);
    pt = mfma32(fm, selm, pt);
    v0 = pack8(vt, 0) | ones; v1 = pack8(vt, 1) | ones;
    kv = mfma32(pack8(pt, 0), v0, kv);
    kv = mfma32(pack8(pt, 1), v1, kv);
  }
};

template <int LY, bool KV>
DI void layer(const Params& p, const Ctx& c, v4u (&W)[32], v2u& csf, const v4u wph ST_PARAM) {
  LAS unsigned char* L = c.L;
  const rsrc_t r = c.r;
  int lane = c.lane;
  PIN(lane);
  const int b = c.b, w = c.w, rb = b & 31, cg = b >> 5, hf = cg & 1, cq = cg >> 1;
  const int G = lane >> 4, q = (lane >> 2) & 3, pp = lane & 3, h = lane >> 5, n = lane & 31;
  constexpr int S0 = LY ? 20 : 10, S1 = LY ? 50 : 40;
  constexpr u32 C4 = LY ? 8 : 4;
  const u32 tagl = c.tag + LY;
  const v4u z4 = {0u, 0u, 0u, 0u};
  f32x16 zero;
#pragma unroll
  for (int i = 0; i < 16; ++i) zero[i] = 0.f;
  v4u x0, x1;
  if (KV) {
    const v4u cs4 = {csf.x, csf.y, 0u, 0u};
    f32x16 acc0 = zero, acc1 = zero, kv;
    Post ps;
    Stat s0, s1;
    ps.init(n, h);
    const int head = 4 * cg + w;
    int so = B_YI + rb * 65536 + (head * 8 + h * 2 + (n >> 3)) * 256 + (n & 7) * 16, so1 = B_ZP + (head * 32 + rb) * 128 + h * 32;
    PIN(so); PIN(so1);
    so = n < 16 ? so : so1;
    so1 = so + (n < 16 ? 128 : 16);
    int so2 = so + (n < 16 ? 1024 : 64), so3 = so + (n < 16 ? 1152 : 80);
    PIN(so); PIN(so1); PIN(so2); PIN(so3);
    nap<LY ? D_XI - 2 : D_X0>();
    STV(LY ? 38 : 57, load_image(p, r, L, rb, w, lane, tagl));
    ST(S0);
    {
      constexpr int PD = 4;
      LAS unsigned char* xb = L + R2 + lane * 16;
      LAS unsigned char* sb = L + L_ST + n * 16;
      v4u x[PD];
      u32 seen;
      until(L_C1 + 20, C4);
#pragma unroll
      for (int i = 0; i < PD; ++i) x[i] = *(LAS v4u*)(xb + i * 1024);
      SB();
      sfor<0, 64>([&](auto I) __attribute__((always_inline)) {
        constexpr int i = decltype(I)::v;
        const v4u cur = x[i % PD];
        if constexpr (i < 32) acc0 = mfma32(W[i & 31], cur, acc0); else acc1 = mfma32(W[i & 31], cur, acc1);
        if constexpr (i == 23) seen = *(volatile LAS u32*)(L + L_C1 + 24);
        if constexpr (i == 27) { if (__builtin_amdgcn_readfirstlane(seen) < C4) until(L_C1 + 24, C4); FENCE(); }
        if constexpr (i + PD < 64) x[i % PD] = *(LAS v4u*)(xb + (i + PD) * 1024);
        if constexpr (i >= 1 && i < 8) s0.template step<i - 1>(sb);
        if constexpr (i == 10) acc0 = mfma32(cs4, v4u{s0.mf, s0.mf, 0u, 0u}, acc0);
        if constexpr (i >= 27 && i < 34) s1.template step<i - 27>(sb + 1024);
        if constexpr (i == 60) acc1 = mfma32(cs4, v4u{s1.mf, s1.mf, 0u, 0u}, acc1);
        if constexpr (i >= 34) ps.template step<i - 34>(acc0, s0.rs, wph, kv, true);
        SB();
      });
    }
    ST(S1 + 2);
    ps.tail(acc1, s1.rs, wph, kv);
    if (n <= 16) {
      const v4u tb = tag4(tagl);
#define TG(i) __builtin_amdgcn_perm(BC(u32, kv[i]), tb[(i) & 3], 0x07060500u)
      st16(r, so, v4u{TG(0), TG(1), TG(2), TG(3)}); st16(r, so1, v4u{TG(4), TG(5), TG(6), TG(7)});
      st16(r, so2, v4u{TG(8), TG(9), TG(10), TG(11)}); st16(r, so3, v4u{TG(12), TG(13), TG(14), TG(15)});
    }
    ST(S0 + 1);
    arrive(L_C1 + 16);
    if (w == 0) {
      nap<D_KVP>();
      v4u t[8];
      STV(58 + LY, (fetch<8, 2>(r, t, B_YI + G * 65536 + b * 256 + (lane & 15) * 16, 262144, tagl)));
      ST(S0 + 3);
      float s[4];
#pragma unroll
      for (int i = 0; i < 4; ++i)
        s[i] = op(op(op(BC(float, t[0][i]) + BC(float, t[1][i])) + op(BC(float, t[2][i]) + BC(float, t[3][i]))) + op(op(BC(float, t[4][i]) + BC(float, t[5][i])) + op(BC(float, t[6][i]) + BC(float, t[7][i]))));
      swap32(s[0], s[2]); swap32(s[1], s[3]);
      float a = op(s[0] + s[2]), bb = op(s[1] + s[3]), a2 = a, b2 = bb;
      swap16(a, a2); swap16(bb, b2);
      u32 d0 = pk2(op(a + a2), op(bb + b2)), d1 = d0;
      swap32u(d0, d1);
      const u32 e0 = dppu<0x128>(d0), e1 = dppu<0x128>(d1);
      if (lane < 8) st16(r, B_KVR + LY * 32768 + b * 128 + lane * 16, v4u{d0, d1, e0, e1});
      if (LY == 0) arrive(L_C1 + 28);
      nap<D_KVR>();
      arrive(L_C1 + 8);
    } else if (w == 1 && (b & 7) == 0) {
      nap<D_KVP>();
      v4u t[4];
      fetch<4, 2>(r, t, B_ZP + (b >> 3) * 4096 + lane * 16, 1024, tagl);
      float s[4];
#pragma unroll
      for (int i = 0; i < 4; ++i) s[i] = op(op(BC(float, t[0][i]) + BC(float, t[1][i])) + op(BC(float, t[2][i]) + BC(float, t[3][i])));
      swap32(s[0], s[2]); swap32(s[1], s[3]);
      float a = op(s[0] + s[2]), bb = op(s[1] + s[3]), a2 = a, b2 = bb;
      swap16(a, a2); swap16(bb, b2);
      a = op(a + a2); bb = op(bb + b2);
      a = op(a + dppf<0x128>(a)); bb = op(bb + dppf<0x128>(bb));
      u32 d0 = pk2(a, bb), d1 = d0;
      swap32u(d0, d1);
      const u32 e0 = dppu<0xb1>(d0), e1 = dppu<0xb1>(d1);
      if (lane < 8 && !(lane & 1)) st16(r, B_ZR + LY * 4096 + (b >> 3) * 128 + lane * 8, v4u{d0, d1, e0, e1});
    }
    if (LY == 0 && w >= 2) {
      if (WO1 == 1) {
        until<1>(L_C1 + 12, 4);
        until(L_C1 + 16, 4);
        dma_run<false, 32, 4096>(srd(p.wo, 512 * 1024), q * 1024 + cq * 256 + G * 64 + pp * 16 + (w - 2) * 131072, R1A + (w - 2) * 32768);
      }
      if (WO1 == 2) {
        until<1>(L_C1 + 12, 4);
        until(L_C1 + 16, 4);
        dma_run<false, 16, 4096>(srd(p.wo, 512 * 1024), (128 + q) * 1024 + cq * 256 + G * 64 + pp * 16 + (w - 2) * 65536, R1A + 32768 + (w - 2) * 16384);
      }
      if (WO2 == 0) until<1>(L_C1 + 28, 1);
      until<1>(L_C1, 4);
      dma_run<false, 32, 4096>(srd(p.wo, 512 * 1024), (256 + q) * 1024 + cq * 256 + G * 64 + pp * 16 + (w - 2) * 131072, R2 + (w - 2) * 32768);
      DRAIN();
    }
  } else {
    const int hp = (w >> 1) & 1, kh = w & 1;
    nap<D_XI + D_XQ>();
    STV(LY ? 38 : 57, load_image(p, r, L, rb, w, lane, tagl));
    ST(S0);
    if (LY == 0) {
      until(L_C1 + 20, 4);
      dma_run<false, 16, 8 * 3072>(srd(p.wqkv, 512 * 3072), ((w - 4) * 128 + ((lane >> 2) & 7)) * 3072 + cg * 128 + h * 64 + pp * 16, R1A + (w - 4) * 16384);
      u32 pf = 0;
      if (lane < 32) pf = *(const GAS u32*)((const GAS unsigned char*)p.wo + ((b >> 3) * 128 + (w - 4) * 32 + lane) * 128);
      VMW(1);
      arrive(L_C1 + 4);
      until<1>(L_C1 + 4, 4);
      ST(S1 + 0);
      LAS unsigned char* wb = L + R1A + kh * 32768 + hp * 512 + (4 * h + q) * 64 + 32 * (G & 1) + 8 * pp;
#pragma unroll
      for (int i = 0; i < 16; ++i) W[i] = trd2(wb + i * 2048, 1024);
      arrive(L_C1 + 12);
      asm volatile("" ::"v"(pf));
      if (WO1 == 0) {
        until<1>(L_C1 + 12, 4);
        dma_run<false, 16, 4096>(srd(p.wo, 512 * 1024), q * 1024 + cq * 256 + G * 64 + pp * 16 + (w - 4) * 65536, R1A + (w - 4) * 16384);
      }
      if (WO1 == 2) {
        until<1>(L_C1 + 12, 4);
        dma_run<false, 8, 4096>(srd(p.wo, 512 * 1024), q * 1024 + cq * 256 + G * 64 + pp * 16 + (w - 4) * 32768, R1A + (w - 4) * 8192);
      }
      if (CS_EARLY == 1) csf = colsum<0, 16>(W);
    }
    ST(S1 + 1);
    until<1>(L_C1 + 16, C4);
    ST(S1 + 2);
    if (LY == 0 && CS_EARLY == 0) csf = colsum<0, 16>(W);
    const v4u cs4 = {csf.x, csf.y, 0u, 0u};
    u32 mf; float rs;
    row_stats(L, kh ^ 1, lane, mf, rs);
    f32x16 acco = mfma32(cs4, v4u{mf, mf, 0u, 0u}, zero);
    row_stats(L, kh, lane, mf, rs);
    f32x16 acck = mfma32(cs4, v4u{mf, mf, 0u, 0u}, zero);
    gemm<16>(acco, W, L + R2 + (kh ^ 1) * 32768 + kh * 16384 + lane * 16);
    gemm<16>(acck, W, L + R2 + kh * 32768 + kh * 16384 + lane * 16);
    ST(S1 + 3);
#pragma unroll
    for (int i = 0; i < 4; ++i) *(LAS f32x4*)(L + L_EX + ((w - 4) * 4 + i) * 1024 + lane * 16) = f32x4{acco[4 * i], acco[4 * i + 1], acco[4 * i + 2], acco[4 * i + 3]};
    {
      LAS unsigned char* xb = L + R2 + (hf * 32 + 8 * cq + 2 * (w - 4)) * 1024 + lane * 16;
      x0 = *(LAS v4u*)xb; x1 = *(LAS v4u*)(xb + 1024);
    }
    arrive(L_C1);
    until(L_C1, C4);
    ST(S1 + 5);
#pragma unroll
    for (int i = 0; i < 4; ++i) {
      const f32x4 t = *(LAS f32x4*)(L + L_EX + (((w - 4) ^ 1) * 4 + i) * 1024 + lane * 16);
      acck[4 * i] += t.x; acck[4 * i + 1] += t.y; acck[4 * i + 2] += t.z; acck[4 * i + 3] += t.w;
    }
    f32x16 pr = mfma32(n < 16 ? wph : z4, pack8(acck, 0), zero);
    pr = mfma32(n < 16 ? z4 : wph, pack8(acck, 1), pr);
    v4u ph[4], kr[4];
    dual_softmax(pr, 0, rs * LOG2E, ph[0], ph[1]);
    dual_softmax(pr, 8, rs * LOG2E, ph[2], ph[3]);
    PIN(ph[0]); PIN(ph[1]); PIN(ph[2]); PIN(ph[3]);
    ST(S0 + 1);
    until(L_C1 + 8, LY + 1);
    ST(S0 + 3);
    {
      const int hd = 4 * cg + 2 * hp;
      const int o0 = n < 16 ? B_KVR + LY * 32768 + hd * 1024 + h * 256 + n * 16 : B_ZR + LY * 4096 + hd * 128 + h * 16;
      const int hs = n < 16 ? 1024 : 128, ss = n < 16 ? 512 : 32;
      u32 spins = 0;
#pragma nounroll
      for (;; ++spins) {
        FENCE();
        u32 a = 0u;
#pragma unroll
        for (int i = 0; i < 4; ++i) kr[i] = ld16(r, o0 + (i >> 1) * hs + (i & 1) * ss);
#pragma unroll
        for (int i = 0; i < 4; ++i) a = max(max(a, max(kr[i].x, kr[i].y)), max(kr[i].z, kr[i].w));
        if (all_lanes(a != ~0u) || spins > SPIN_CAP) break;
        nap<D_RETRY>();
      }
      FENCE();
      STV(62 + LY, spins);
    }
    ST(S0 + 5);
    v4u of[2];
#pragma unroll
    for (int i = 0; i < 2; ++i) {
      f32x16 o = mfma32(kr[2 * i], ph[2 * i], zero);
      o = mfma32(kr[2 * i + 1], ph[2 * i + 1], o);
      const float inv = __builtin_amdgcn_rcpf(o[8] + 1e-6f);
      of[i] = v4u{pk2(o[0] * inv, o[1] * inv), pk2(o[2] * inv, o[3] * inv), pk2(o[4] * inv, o[5] * inv), pk2(o[6] * inv, o[7] * inv)};
    }
    const int fo = (rb * 2 + kh) * 32 + 4 * cg + 2 * hp;
    publish2(r, B_YI + rb * 65536 + (2 * cg + hp) * 4096 + kh * 2048 + lane * 16, B_OR + fo * 128 + lane * 4, of[0], of[1], tagl);
  }
  ST(S0 + 2);
  BAR();
  ST(S0 + 4);
  LAS unsigned char* ob = L + R2 + (hf ^ 1) * 32768 + lane * 16;
  if (KV) {
    nap<D_OI>();
    if (LY == 0) until(L_C1 + 28, 5);
    u32 spins = 0;
    const int fo = (rb * 2 + hf) * 32 + 8 * w;
#pragma nounroll
    for (;; ++spins) {
      dma_run<true, 8, 4096, true>(srd(p.ws, WS_BYTES), B_YI + rb * 65536 + w * 16384 + hf * 2048 + lane * 16, R2 + (hf ^ 1) * 32768 + 8 * w * 1024);
      v4u rec;
#pragma unroll
      for (int i = 0; i < 4; ++i) rec[i] = ld4(r, B_OR + fo * 128 + i * 256 + lane * 4);
      u32 a = 4u * tagl;
      LAS v4u* o0 = (LAS v4u*)(ob + 8 * w * 1024);
      VMW(8); a = sum4(o0[64], sum4(o0[0], a)); a = sum4(o0[192], sum4(o0[128], a));
      VMW(4); a = sum4(o0[320], sum4(o0[256], a)); a = sum4(o0[448], sum4(o0[384], a));
      FENCE();
      if (all_lanes(a == sum4(rec, 0u)) || spins > SPIN_CAP) break;
      nap<D_RETRY>();
    }
    STV(48 + LY, spins);
  } else if (LY == 0) {
#pragma unroll
    for (int i = 0; i < 16; ++i) W[16 + i] = trd2(L + R2 + i * 4096 + h * 1024 + (w - 4) * 256 + q * 64 + 32 * (G & 1) + 8 * pp, 2048);
    arrive(L_C1 + 28);
  }
  f32x16 acc;
  if (!KV) {
#pragma unroll
    for (int i = 0; i < 4; ++i) { acc[2 * i] = bflo(x0[i]); acc[2 * i + 1] = bfhi(x0[i]); acc[8 + 2 * i] = bflo(x1[i]); acc[9 + 2 * i] = bfhi(x1[i]); }
    PIN(acc);
  }
  BAR();
  ST(S0 + 6);
  if (!KV) {
    const int ct = w - 4, ks0 = 8 * cq + 2 * ct;
    LAS unsigned char* wb = L + R1A + h * 1024 + ct * 256 + q * 64 + 32 * (G & 1) + 8 * pp;
    {
      constexpr int PD = 4;
      v4u o[PD], a[PD];
#pragma unroll
      for (int i = 0; i < PD; ++i) o[i] = *(LAS v4u*)(ob + (16 + i) * 1024);
      SB();
#pragma unroll
      for (int i = 0; i < 32; ++i) {
        acc = mfma32(i < 16 ? W[16 + (i & 15)] : a[i % PD], o[i % PD], acc);
        if (i + PD < 32) o[i % PD] = *(LAS v4u*)(ob + ((16 + i + PD) & 31) * 1024);
        if (i + PD >= 16 && i + PD < 32) a[i % PD] = trd2(wb + (i + PD - 16) * 4096, 2048);
        SB();
      }
    }
    PIN(acc);
    ST(S0 + 7);
    if (LY == 0) {
      f32x2 s1 = {0.f, 0.f}, s2 = {0.f, 0.f};
#pragma unroll
      for (int i = 0; i < 8; ++i) { const f32x2 t = {acc[2 * i], acc[2 * i + 1]}; s1 += t; s2 += t * t; }
      float u1 = s1.x + s1.y, u2 = s2.x + s2.y;
      swap32(u1, u2);
      float t = u1 + u2, t2 = t;
      swap32(t, t2);
      const int hrb = rb * 2 + hf;
      const v4u tb = tag4(c.tag);
      if (lane < 32) __builtin_amdgcn_raw_buffer_store_b64(v2u{(BC(u32, t) & ~0xffu) | tb.x, (BC(u32, t2) & ~0xffu) | tb.y}, r, B_SP + (hrb * 16 + cq * 4 + ct) * 256 + n * 8, 0, SC1);
      const v4u y0 = *(LAS v4u*)(L + L_Y + ct * 2048 + lane * 16), y1 = *(LAS v4u*)(L + L_Y + ct * 2048 + 1024 + lane * 16);
      nap<D_SP>();
      v2u sp[8];
      u32 spins = 0;
#pragma nounroll
      for (;; ++spins) {
        FENCE();
        u32 a = 0u;
#pragma unroll
        for (int i = 0; i < 8; ++i) sp[i] = __builtin_amdgcn_raw_buffer_load_b64(r, B_SP + (hrb * 16 + 8 * h + i) * 256 + n * 8, 0, SC1);
#pragma unroll
        for (int i = 0; i < 8; ++i) { sp[i].x ^= tb.x; sp[i].y ^= tb.y; a |= sp[i].x | sp[i].y; }
        if (all_lanes((a & 0xffu) == 0u) || spins > SPIN_CAP) break;
        nap<D_RETRY>();
      }
      STV(37, spins);
      ST(S0 + 8);
      u1 = 0.f; u2 = 0.f;
#pragma unroll
      for (int i = 0; i < 8; ++i) { u1 += BC(float, sp[i].x); u2 += BC(float, sp[i].y); }
      swap32(u1, u2);
      t = u1 + u2; t2 = t;
      swap32(t, t2);
      const float m = t * (1.f / 512.f), rs = __builtin_amdgcn_rsqf(mx3(t2 * (1.f / 512.f) - m * m, 0.f, 0.f) + 1e-5f);
      gate_publish(acc, rs, -m * rs, y0, y1, r, rb, hf, ks0, lane, c.tag + 1u);
    } else {
      LAS unsigned char* eb = L + L_EX + ct * 4096;
#pragma unroll
      for (int i = 0; i < 4; ++i) *(LAS v2u*)(eb + n * 64 + ((i ^ ((n >> 1) & 3)) * 16) + h * 8) = v2u{pk2(acc[4 * i], acc[4 * i + 1]), pk2(acc[4 * i + 2], acc[4 * i + 3])};
      FENCE();
      const v4u a = *(LAS v4u*)(eb + (lane >> 2) * 64 + (((lane & 3) ^ ((lane >> 3) & 3)) * 16)), d = *(LAS v4u*)(eb + 1024 + (lane >> 2) * 64 + (((lane & 3) ^ ((lane >> 3) & 3)) * 16));
      const rsrc_t ro = __builtin_amdgcn_make_buffer_rsrc((void*)p.out, 0, 2 * MB, 0x00020000);
      const int oo = (rb * 64 + hf * 32 + (lane >> 2)) * 1024 + cq * 256 + ct * 64 + (lane & 3) * 16;
      asm volatile(".Lfk_end:");
      __builtin_amdgcn_raw_buffer_store_b128(a, ro, oo, 0, OUT_AUX);
      __builtin_amdgcn_raw_buffer_store_b128(d, ro, oo + 16384, 0, OUT_AUX);
    }
  }
  if (LY == 0) {
    BAR();
    ST(S0 + 9);

  }
}

DI void stage_kv(const Params& p, int stage, int cg, int w, int lane) {
  const int ty = w >> 1, g = ((lane >> 4) & 1) ^ (2 * ty);
  dma_run<false, 16, 8 * 3072>(srd(p.wqkv, 512 * 3072), (stage * 256 + (w & 1) * 128 + (lane >> 3)) * 3072 + (1 + ty) * 1024 + cg * 128 + (((lane & 7) ^ (2 * g)) * 16), R1A + w * 16384);
}

template <bool KV>
DI void program(const Params& p, Ctx c, v4u a0, u32 a4 ST_PARAM) {
  LAS unsigned char* L = c.L;
  const rsrc_t r = c.r;
  u32 tag;
  const int b = c.b, w = c.w, lane = c.lane, t8 = (w & 3) * 64 + lane;
  const int x8 = b & 7, hi = b >> 3, rb = b & 31, cg = b >> 5, hf = cg & 1, cq = cg >> 1, pair = x8 * 32 + hi;
  const int G = lane >> 4, q = (lane >> 2) & 3, pp = lane & 3, h = lane >> 5, n = lane & 31;
  const v4u z4 = {0u, 0u, 0u, 0u}, s4 = {~0u, ~0u, ~0u, ~0u};
  v4u W[32];
  v2u csf = {0u, 0u};
  float cs0 = 0.f, cs1 = 0.f;
  v4u wph;
#define WPH_ISSUE() \
    u32 we[8]; { \
    const GAS u16* wp = (const GAS u16*)p.wphi + (KV ? 4 * cg + w : 4 * cg + (w & 2) + (n >> 4)) * 256 + (4 * h) * 16 + (n & 15); \
    _Pragma("unroll") for (int j = 0; j < 8; ++j) we[j] = wp[(8 * (j >> 2) + (j & 3)) * 16]; }
#define WPH_FINISH() { wph = v4u{we[0] | (we[1] << 16), we[2] | (we[3] << 16), we[4] | (we[5] << 16), we[6] | (we[7] << 16)}; PIN(wph); }
#define WARM(M, WV, D) { \
    if (((M) & 5) && (w & 3) == (WV) && (lane < 32 ? ((M) & 1) : ((M) & 4))) \
      D = *(const GAS u32*)(lane < 48 ? (const GAS unsigned char*)p.wqkv + (lane < 32 ? (2 * b + (lane >> 4)) * 3072 + 1024 + (lane & 15) * 128 : (2 * b + ((lane >> 3) & 1)) * 3072 + (lane & 7) * 128) \
                                      : (const GAS unsigned char*)p.wo + (b * 16 + lane - 48) * 128); \
    if (((M) & 2) && (w & 3) == ((WV) ^ 1)) D = *(const GAS u32*)((const GAS unsigned char*)p.q + (rb * 64 + hf * 32 + cq * 8 + (lane >> 3)) * 1024 + (lane & 7) * 128); \
    FENCE(); }
  const int yh = 2 * ((t8 >> 5) + 8 * ((t8 >> 3) & 3)) + ((t8 >> 2) & 1);
  const int ypos = B_YI + yh * 32768 + pair * 128 + (lane & 3) * 16;
  if (!KV) {
    const int ct = w - 4, ks0 = 8 * cq + 2 * ct;
    if (w == 4 && lane < 16) *(LAS u32*)(L + L_C0 + lane * 4) = 0u;
    if (w == 6 && hi == 0 && lane < 32) *(LAS v4u*)(L + L_KT + 512 + lane * 16) = z4;
    if (w == 5 && n < 30) *(LAS v4u*)(L + L_U + h * 4608 + 4096 + n * 16) = z4;
    nap<D_H>();
    v4u hv[2]; u32 hn[2];
#pragma unroll
    for (int cc = 0; cc < 2; ++cc) {
      hv[cc] = *(const GAS v4u*)(p.h + pair * 4096 + cc * 2048 + t8 * 8);
      hn[cc] = *(const GAS u32*)(p.h + pair * 4096 + cc * 2048 + min(t8 * 8 + 8, 2040));
    }
    WPH_ISSUE();
    const u32 dd = *(const GAS u32*)(p.dv + 2 * pair);
    u32 pe = 0;
    WARM(PE, 3, pe);
    BAR();
    ST(1);
#pragma unroll
    for (int cc = 0; cc < 2; ++cc) {
      LAS unsigned char* hb = L + L_H + cc * 8320 + t8 * 16 + 32;
      const v4u a = hv[cc];
      *(LAS v4u*)hb = a;
      *(LAS v4u*)(hb + 4160) = v4u{__builtin_amdgcn_alignbit(a.y, a.x, 16), __builtin_amdgcn_alignbit(a.z, a.y, 16), __builtin_amdgcn_alignbit(a.w, a.z, 16), __builtin_amdgcn_alignbit(hn[cc], a.w, 16)};
      if (t8 == 0) {
        *(LAS v4u*)(hb - 32) = z4; *(LAS v4u*)(hb - 16) = z4; *(LAS v4u*)(hb + 4160 - 32) = z4;
        *(LAS v4u*)(hb + 4160 - 16) = v4u{0u, 0u, 0u, a.x << 16};
      }
    }
    arrive(L_C0);
    WPH_FINISH();
    asm volatile("" ::"v"(dd));
    ST(2);
    if (PRIO) __builtin_amdgcn_s_setprio(2);
    until(L_C0, 8);
    tag = __builtin_amdgcn_readfirstlane(*(LAS u32*)(L + L_C0 - 528));
    c.tag = tag;
    const int ch = (w >> 1) & 1, rho = w & 1, i16 = lane & 15;
    LAS unsigned char* ab = L + L_H + ch * 8320 + ((i16 & 1) ? 0 : 4160) + 2 * (((i16 + 1) & ~1) + 8 * G) + 256 * rho;
#pragma unroll
    for (int i = 0; i < 16; ++i) {
      LAS u32* ap = (LAS u32*)(ab + 512 * (i & 7) + 64 * (i >> 3));
      W[i] = v4u{ap[0], ap[1], ap[2], ap[3]};
    }
    FENCE();
    nap<D_UT>();
    ST(3);
    const int usrc = B_UT + pair * 8192 + t8 * 16, urec = B_UR + ((t8 >> 3) * 8 + x8) * 1024 + (hi * 8 + (lane & 7)) * 4;
    v4u u0 = ld16(r, usrc), u1 = ld16(r, usrc + 4096);
    u32 ur_ = ld4(r, urec);
    FENCE();
    arrive(L_C0 + 24);
#pragma unroll
    for (int i = 16; i < 32; ++i) {
      LAS u32* ap = (LAS u32*)(ab + 512 * (i & 7) + 64 * (i >> 3));
      W[i] = v4u{ap[0], ap[1], ap[2], ap[3]};
    }
    FENCE();
    {
      u32 spins = 0;
#pragma nounroll
      while (!all_lanes(sum4(u1, sum4(u0, tag)) == ur_) && spins <= SPIN_CAP) {
        ++spins;
        FENCE();
        u0 = ld16(r, usrc); u1 = ld16(r, usrc + 4096); ur_ = ld4(r, urec);
      }
      STV(35, spins);
    }
    if (TRIG == 40) arrive(L_C0 + 40);
    asm volatile("" ::"v"(pe));
    ST(4);
    *(LAS v4u*)(L + L_U + 4080 - 16 * t8) = u0;
    *(LAS v4u*)(L + L_U + 4608 + 4080 - 16 * t8) = u1;
    LAS unsigned char* bb = L + L_U + ch * 4608 + 4064 - 3584 - 32 * i16 + 16 * G + 256 * rho;
    LAS unsigned char* pb = L + L_P + ct * 8192 + G * 256 + ((i16 ^ (8 * (G >> 1))) * 16);
    LAS unsigned char* pb2 = L + L_P + ct * 8192 + G * 256 + ((i16 ^ (8 * (G >> 1)) ^ 4) * 16);
    arrive(L_C0 + 36);
    until(L_C0 + 36, 4);
    ST(5);
    f32x4 acc[8];
#pragma unroll
    for (int i = 0; i < 8; ++i) acc[i] = f32x4{0.f, 0.f, 0.f, 0.f};
    v4u B[8];
#pragma unroll
    for (int i = 0; i < 8; ++i) B[i] = *(LAS v4u*)(bb + 512 * (7 - i));
    SB();
    const int oct = 4 * yh + (lane & 3);
    v4u ur[2];
    float y[2][8];
#pragma unroll
    for (int rr = 0; rr < 4; ++rr) {
#pragma unroll
      for (int m = 0; m < 8; ++m) {
#pragma unroll
        for (int nn = 0; nn + m < 8; ++nn) {
          acc[nn + m] = mfma16(W[8 * rr + nn], B[m], acc[nn + m]);
          if (rr == 3 && m >= 1 && nn == 0) *(LAS f32x4*)((((m - 1) & 2) ? pb2 : pb) + (m - 1) * 1024) = acc[m - 1];
          if (rr == 2 && m == 5 && (nn == 0 || nn == 1)) ur[nn] = *(LAS v4u*)(L + L_U + nn * 4608 + 4080 - 16 * oct);
          if (rr == 3 && m < 4 && nn >= 1 && nn <= 4) {
            const int e = 4 * m + nn - 1, i = e >> 3, k = e & 7;
            y[i][k] = ((k & 1) ? bflo(ur[i][3 - (k >> 1)]) : bfhi(ur[i][3 - (k >> 1)])) * (i ? bfhi(dd) : bflo(dd));
            PIN(y[i][k]);
          }
          SB();
        }
        if (rr < 3) B[m] = *(LAS v4u*)(bb + 512 * (7 - m) + 64 * (rr + 1));
        SB();
      }
    }
    *(LAS f32x4*)(pb2 + 7 * 1024) = acc[7];
    ST(6);
    arrive(L_C0 + 4);
    until(L_C0 + 4, 8);
    {
      const int o = oct & 1, j = (oct >> 1) & 15;
      v4u yo[2];
#pragma unroll
      for (int i = 0; i < 2; ++i) {
#pragma unroll
        for (int rh = 0; rh < 2; ++rh)
#pragma unroll
          for (int e = 0; e < 2; ++e) {
            const f32x4 t = *(LAS f32x4*)(L + L_P + ((i * 2 + rh) * 8 + (oct >> 5)) * 1024 + (2 * o + e) * 256 + ((j ^ (8 * o) ^ ((oct >> 4) & 4)) * 16));
            y[i][4 * e] += t.x; y[i][4 * e + 1] += t.y; y[i][4 * e + 2] += t.z; y[i][4 * e + 3] += t.w;
          }
        yo[i] = v4u{pk2(y[i][0], y[i][1]), pk2(y[i][2], y[i][3]), pk2(y[i][4], y[i][5]), pk2(y[i][6], y[i][7])};
      }
      publish2<64>(r, ypos, B_YR + pair * 1024 + t8 * 4, yo[0], yo[1], tag);
    }
    ST(7);
    until(L_C0 + 28, 4);
    f32x16 x;
    {
      LAS unsigned char* qb = L + L_QR + n * 1024;
      float s1 = 0.f, s2 = 0.f;
#pragma unroll
      for (int i = 0; i < 8; ++i) acc_stats(*(LAS v4u*)(qb + ((16 * ct + ((8 * h + i) ^ (n & 15))) * 16)), s1, s2);
      *(LAS f32x2*)(L + L_S0 + ct * 512 + n * 16 + h * 8) = f32x2{s1, s2};
#pragma unroll
      for (int i = 0; i < 4; ++i) {
        const v2u t = *(LAS v2u*)(qb + (((2 * ks0 + i) ^ (n & 15)) * 16) + h * 8);
        x[4 * i] = bflo(t.x); x[4 * i + 1] = bfhi(t.x); x[4 * i + 2] = bflo(t.y); x[4 * i + 3] = bfhi(t.y);
      }
    }
    arrive(L_C0 + 32);
    ST(29);
    nap<D_YI>();
    ST(8);
    const int ysrc = B_YI + (rb * 2 + hf) * 32768 + ks0 * 1024 + lane * 16;
    const int yrec = B_YR + (8 * ks0 + (lane >> 2)) * 1024 + ((rb & 7) * 32 + (rb >> 3) * 8 + hf * 4 + (lane & 3)) * 4;
    dma_run<true, 2, 1024>(srd(p.ws, WS_BYTES), ysrc, L_H + ct * 2048);
    u32 yr = ld4(r, yrec);
    FENCE();
    until(L_C0 + 32, 4);
    float m, rs;
    row_moments(L + L_S0, lane, m, rs);
    PIN(x); PIN(m); PIN(rs);
    v4u y0, y1;
    u32 spins = 0;
#pragma nounroll
    for (;; ++spins) {
      DRAIN();
      LAS unsigned char* yb = L + L_H + ct * 2048 + (4 * h + q) * 64 + 32 * (G & 1) + 8 * pp;
      LAS unsigned char* yc = L + L_H + ct * 2048 + (lane >> 2) * 128 + (lane & 3) * 16;
      const v4u c0 = *(LAS v4u*)yc, c1 = *(LAS v4u*)(yc + 64);
      y0 = trd2(yb, 512); y1 = trd2(yb + 1024, 512);
      if (all_lanes(sum4(c1, sum4(c0, tag)) == yr) || spins > SPIN_CAP) break;
      asm volatile("s_waitcnt lgkmcnt(0)" ::: "memory");
      nap<D_RETRY>();
      dma_run<true, 2, 1024>(srd(p.ws, WS_BYTES), ysrc, L_H + ct * 2048);
      yr = ld4(r, yrec);
    }
    STV(36, spins);
    ST(9);
    gate_publish(x, rs, m * rs, y0, y1, r, rb, hf, ks0, lane, tag);
    *(LAS v4u*)(L + L_Y + ct * 2048 + lane * 16) = y0; *(LAS v4u*)(L + L_Y + ct * 2048 + 1024 + lane * 16) = y1;
    if (w == 4 && lane < 8) *(LAS u32*)(L + L_C1 + lane * 4) = 0u;
    if (PRIO) __builtin_amdgcn_s_setprio(0);
  } else {
    {
      u32 keep;
      asm volatile("s_nop 4\n\ts_load_dword %1, %10, 0x0\n\ts_mov_b32 %0, m0\n\ts_mov_b32 m0, %11\n\ts_nop 0\n\tbuffer_load_dwordx4 %2, %7, 0 offen lds\n\t"
                   "s_add_u32 m0, m0, 0x2000\n\ts_nop 0\n\tbuffer_load_dwordx4 %3, %8, 0 offen lds\n\ts_add_u32 m0, m0, 0x400\n\ts_nop 0\n\t"
                   "buffer_load_dwordx4 %5, %8, 0 offen lds\n\ts_sub_u32 m0, m0, 0x2000\n\ts_nop 0\n\tbuffer_load_dwordx4 %4, %7, 0 offen lds\n\t"
                   "s_mov_b32 m0, %12\n\ts_nop 0\n\tbuffer_load_dwordx4 %6, %9, 0 offen lds\n\ts_mov_b32 m0, %0\n\ts_waitcnt vmcnt(0) lgkmcnt(0)\n\ts_barrier"
                   : "=&s"(keep), "=&s"(tag)
                   : "v"(a0.x), "v"(a0.y), "v"(a0.z), "v"(a0.w), "v"(a4), "s"(srd(p.k, 2 * MB)), "s"(srd(p.v, 2 * MB)), "s"(w == 1 ? srd(p.shk, 4096) : srd(p.k, 2 * MB)),
                     "s"(p.ws), "s"(L_KT + 1024 + 2 * w * 1024), "s"(w == 0 ? L_KT : w == 1 ? L_C0 - 1024 : L_S0 + 1024) : "memory", "scc");
      static_assert(L_VT == L_KT + 1024 + 0x2000, "tile placement");
      tag += 1u;
      c.tag = tag;
    }
    const int oc = lane & 7, pl = 8 * w + (lane >> 3);
    const int upos = B_UT + (x8 * 32 + pl) * 8192 + (8 * hi + oc) * 16, urec = B_UR + b * 1024 + t8 * 4;
    ST(1);
    {
      const int ck = pl >> 2;
      const v4u sk = *(LAS v4u*)(L + L_C0 - 512 + pl * 16);
      LAS unsigned char* ka = L + L_KT + oc * 1024 + ((ck ^ oc) * 16) + (pl & 3) * 4;
      LAS unsigned char* kb = L + L_KT + (oc + 1) * 1024 + ((ck ^ ((oc + 1) & 7)) * 16) + (pl & 3) * 4;
      LAS unsigned char* va = L + L_VT + oc * 1024 + ((ck ^ oc) * 16) + (pl & 3) * 4;
      u32 kk[11], vv[8];
#pragma unroll
      for (int i = 0; i < 3; ++i) kk[i] = *(LAS u32*)(ka + (i + 5) * 128);
#pragma unroll
      for (int i = 0; i < 8; ++i) { kk[3 + i] = *(LAS u32*)(kb + i * 128); vv[i] = *(LAS u32*)(va + i * 128); }
      const f32x2 w0 = {bflo(sk.x), bflo(sk.z)}, w1 = {bfhi(sk.x), bfhi(sk.z)}, w2 = {bflo(sk.y), bflo(sk.w)}, w3 = {bfhi(sk.y), bfhi(sk.w)};
      f32x2 kf[11], u[8];
#pragma unroll
      for (int i = 0; i < 11; ++i) kf[i] = f32x2{bflo(kk[i]), bfhi(kk[i])};
#pragma unroll
      for (int i = 0; i < 8; ++i) u[i] = (kf[i] * w3 + kf[i + 1] * w2 + kf[i + 2] * w1 + kf[i + 3] * w0) * f32x2{bflo(vv[i]), bfhi(vv[i])};
      publish2<4096>(r, upos, urec, v4u{pk2(u[7].x, u[6].x), pk2(u[5].x, u[4].x), pk2(u[3].x, u[2].x), pk2(u[1].x, u[0].x)},
                     v4u{pk2(u[7].y, u[6].y), pk2(u[5].y, u[4].y), pk2(u[3].y, u[2].y), pk2(u[1].y, u[0].y)}, tag);
    }
    if (w == 0 && lane == 0) *(LAS u32*)(L + L_C0 - 528) = tag;
    arrive(L_C0);
    ST(2);
    if (w == 3 && b < 8) { asm volatile("buffer_wbl2 sc0 sc1" ::: "memory"); }
    nap<D_CL>();
    {
      const bool kvr = w < 2, on = lane < 8 && (kvr || (b & 7) == 0);
      if (on) st16(r, (kvr ? B_KVR + w * 32768 + b * 128 : B_ZR + (w - 2) * 4096 + (b >> 3) * 128) + lane * 16, s4);
    }
    ST(3);
    until<1>(L_C0 + TRIG, 4);
    ST(4);
    DRAIN();
    arrive(L_C0 + 4);
    u32 pf = 0, pg = 0;
    WARM(PT, 0, pf);
#define Q_ROWS() _Pragma("unroll") for (int i = 0; i < 8; ++i) \
      dma_run<false, 1, 0>(srd(p.q, 2 * MB), (rb * 64 + hf * 32 + w * 8 + i) * 1024 + ((lane ^ ((w * 8 + i) & 15)) * 16), L_QR + (w * 8 + i) * 1024);
    if (!QL) Q_ROWS();
    stage_kv(p, 0, cg, w, lane);
    if (!QL) { VMW(16); arrive(L_C0 + 28); }
    {
      const rsrc_t rw = __builtin_amdgcn_make_buffer_rsrc((void*)p.wqkv, 0, 512 * 3072, 0x00020000);
      const int src = (256 + (w & 1) * 128 + (lane >> 3)) * 3072 + (1 + (w >> 1)) * 1024 + cg * 128 + (((lane & 7) ^ (2 * ((G & 1) ^ (w & 2)))) * 16);
#pragma unroll
      for (int i = 0; i < 16; ++i) W[16 + i] = __builtin_amdgcn_raw_buffer_load_b128(rw, src, i * 8 * 3072, 0);
      FENCE();
    }
    VMW(16);
    arrive(L_C0 + 12);
    if (QL) { Q_ROWS(); DRAIN(); arrive(L_C0 + 28); }
    WARM(7 & ~(PE | PT), 2, pg);
    WPH_ISSUE();
    FENCE();
    until<1>(L_C0 + 12, 4);
    ST(5);
    if (HOLD == 1) until<2>(L_C0 + 4, 8);
    if (HOLD == 2) until<2>(L_C0 + 32, 4);
    LAS unsigned char* wb = L + R1A + (G & 1) * 32768 + (4 * h + q) * 128 + ((w ^ (q >> 1) ^ (2 * (G & 1))) * 32) + 8 * pp;
#pragma unroll
    for (int i = 0; i < 16; ++i) W[i] = trd2(wb + i * 2048, 1024);
    arrive(L_C0 + 8);
    until<1>(L_C0 + 8, 4);
    ST(6);
#pragma unroll
    for (int i = 0; i < 16; ++i) *(LAS v4u*)(L + R1A + (w * 16 + i) * 1024 + lane * 16) = W[16 + i];
    WPH_FINISH();
    asm volatile("" ::"v"(pf), "v"(pg));
    ST(7);
    until<1>(L_C0 + 32, 4);
    cs_acc<0, 16>(W, cs0, cs1);
    PIN(cs0); PIN(cs1);
    ST(9);
  }
  BAR();
  ST(28);
  if (KV) {
    LAS unsigned char* wb = L + R1A + (G & 1) * 32768 + (4 * h + q) * 128 + ((w ^ (q >> 1) ^ (2 * (G & 1))) * 32) + 8 * pp;
#pragma unroll
    for (int i = 0; i < 16; ++i) W[16 + i] = trd2(wb + i * 2048, 1024);
    cs_acc<16, 32>(W, cs0, cs1);
    csf = cs_pack(cs0 + cs1);
  }
  layer<0, KV>(p, c, W, csf, wph ST_ARG);
  if (KV && b == 0 && w == 0 && lane == 0) __builtin_amdgcn_raw_buffer_store_b32(tag + 1u, r, WS_EPOCH, 0, SC1);
  layer<1, KV>(p, c, W, csf, wph ST_ARG);
}

__global__ void __launch_bounds__(512, 2) fused_kernel(Params p) {
  extern __shared__ __attribute__((aligned(16))) unsigned char smem[];
  LAS unsigned char* L = (LAS unsigned char*)smem;
  const int tid = threadIdx.x, lane = tid & 63, w = __builtin_amdgcn_readfirstlane(tid >> 6), b = blockIdx.x;
  const rsrc_t r = __builtin_amdgcn_make_buffer_rsrc((void*)p.ws, 0, WS_BYTES, 0x00020000);
  v4u a0;
  u32 a4;
  {
    const u32 x8 = b & 7, hi = b >> 3, o0 = (64 * hi + 16 * w + (lane >> 3)) * 1024 + x8 * 128, c0 = lane & 7;
    a0 = v4u{o0 + (c0 ^ (2 * w + 1)) * 16, o0 + (c0 ^ (2 * w)) * 16, o0 + 8192 + (c0 ^ ((2 * w + 2) & 7)) * 16, o0 + 8192 + (c0 ^ (2 * w + 1)) * 16};
    u32 ah = o0 - 8192 + c0 * 16, as = x8 * 512 + (lane - 32) * 16;
    PIN(ah); PIN(as);
    a4 = (lane < 32 || w > 1 || (w == 0 && hi == 0)) ? 0x40000000u : (w == 0 ? ah : as);
    PIN(a0); PIN(a4);
  }
  {
    u64 pc;
    int len;
    asm volatile("s_getpc_b64 %0\n.Lfk_begin:\n\ts_mov_b32 %1, .Lfk_end-.Lfk_begin" : "=s"(pc), "=s"(len));
    if (w == 4 && lane < 16) dma_run<false, 1, 0>(srd((const void*)pc, len), (lane * 32 + (b >> 3)) * 128, L_S0);
  }
  asm volatile("" ::"s"(p.q), "s"(p.k), "s"(p.v), "s"(p.shk), "s"(p.h), "s"(p.ws));
  const Ctx c = {L, r, 0u, b, w, lane};
  if (w >= 4) program<false>(p, c, a0, a4 ST_ARG);
  else program<true>(p, c, a0, a4 ST_ARG);
  ST(30);
  ST_FLUSH();
}

extern "C" void kernel_launch(void* const* d_in, const int* in_sizes, int n_in, void* d_out, int out_size, void* d_ws, size_t ws_size,
                              hipStream_t stream) {
  Params p;
  p.q = (const u16*)d_in[0]; p.k = (const u16*)d_in[1]; p.v = (const u16*)d_in[2]; p.shk = (const u16*)d_in[3];
  p.h = (const u16*)d_in[4]; p.dv = (const u16*)d_in[5]; p.wqkv = (const u16*)d_in[7]; p.wphi = (const u16*)d_in[8];
  p.wo = (const u16*)d_in[9]; p.out = (u16*)d_out; p.ws = (unsigned char*)d_ws + (ws_size - WS_BYTES);
  (void)hipFuncSetAttribute((const void*)fused_kernel, hipFuncAttributeMaxDynamicSharedMemorySize, LDS_BYTES);
  hipLaunchKernelGGL(fused_kernel, dim3(256), dim3(512), LDS_BYTES, stream, p);
}
```

```cpp
#include <hip/hip_runtime.h>
#include <hip/hip_bf16.h>

typedef unsigned short u16;
typedef unsigned u32;
typedef unsigned long long u64;
typedef u32 v4u __attribute__((ext_vector_type(4)));
typedef u32 v2u __attribute__((ext_vector_type(2)));
typedef short v4s __attribute__((ext_vector_type(4)));
typedef float f32x2 __attribute__((ext_vector_type(2)));
typedef float f32x4 __attribute__((ext_vector_type(4)));
typedef float f32x16 __attribute__((ext_vector_type(16)));
typedef __bf16 v8bf __attribute__((ext_vector_type(8)));
typedef __bf16 v2bf __attribute__((ext_vector_type(2)));
#define LAS __attribute__((address_space(3)))
#define GAS __attribute__((address_space(1)))
#define DI __device__ __forceinline__
template <class T, class F> DI T bc(F x) { return __builtin_bit_cast(T, x); }
#define BC(T, x) bc<T>(x)
typedef __amdgpu_buffer_rsrc_t rsrc_t;

constexpr int MB = 1 << 20;
constexpr int WS_EPOCH = 0;
constexpr int B_KVR = 4096;
constexpr int B_ZR = B_KVR + 64 * 1024;
constexpr int B_ZP = 256 * 1024;
constexpr int B_SP = 384 * 1024;
constexpr int B_OR = 640 * 1024;
constexpr int B_UR = 896 * 1024;
constexpr int B_YR = 1152 * 1024;
constexpr int B_XR = 1408 * 1024;
constexpr int B_UT = 2 * MB;
constexpr int B_YI = 4 * MB;
constexpr int B_STAMP = 6 * MB;
constexpr int WS_BYTES = 8 * MB;

constexpr int R1A = 0, R1B = 65536, R2 = 98304, LDS_BYTES = 163840;
constexpr int L_H = R2, L_U = R2 + 16640, L_P = R2 + 25856, L_S0 = R2 + 59136, L_C0 = R2 + 65536 - 64;
constexpr int L_KT = L_P, L_VT = L_P + 9216;
constexpr int L_QR = R1B;
constexpr int L_ST = R1B, L_EX = R1B + 4096, L_C1 = R1B + 20480, L_Y = R1B + 24576;

constexpr u32 SPIN_CAP = 1u << 14;
constexpr int SC1 = 16;
constexpr float LOG2E = 1.4426950408889634f;
constexpr u32 ONES = 0x3f803f80u;

#define D_UT 8
#define D_YI 16
#define D_KVP 18
#define D_OI 18
#define D_KVR 16
#define D_SP 14
#define D_XI 14
#define D_XQ 2
#define OUT_AUX 16
#define CS_EARLY 0
#define D_X0 2
#define ROT 1
#define D_H 3
#define D_CL 8
#define PRIO 1
#define HOLD 1
#define TRIG 24
#define PE 3
#define PT 0
#define QL 0
#define D_RETRY 0

struct Params {
  const u16 *q, *k, *v, *shk, *h, *dv, *wqkv, *wphi, *wo;
  u16* out;
  unsigned char* ws;
};

DI float bflo(u32 x) { return BC(float, x << 16); }
DI float bfhi(u32 x) { return BC(float, x & 0xffff0000u); }
DI u32 pk2(float a, float b) { f32x2 p = {a, b}; return BC(u32, __builtin_convertvector(p, v2bf)); }
DI float op(float r) { asm("" : "+v"(r)); return r; }
DI float mx3(float a, float b, float c) { return __builtin_fmaxf(__builtin_fmaxf(a, b), c); }
DI f32x16 mfma32(v4u a, v4u b, f32x16 c) { return __builtin_amdgcn_mfma_f32_32x32x16_bf16(BC(v8bf, a), BC(v8bf, b), c, 0, 0, 0); }
DI f32x4 mfma16(v4u a, v4u b, f32x4 c) { return __builtin_amdgcn_mfma_f32_16x16x32_bf16(BC(v8bf, a), BC(v8bf, b), c, 0, 0, 0); }
DI float dot2(u32 a, u32 b, float c) { return __builtin_amdgcn_fdot2_f32_bf16(BC(v2bf, a), BC(v2bf, b), c, false); }
DI v2u trd(LAS unsigned char* p) { return BC(v2u, __builtin_amdgcn_ds_read_tr16_b64_v4i16((LAS v4s*)p)); }
DI v4u trd2(LAS unsigned char* p, int second) { v2u a = trd(p), b = trd(p + second); return v4u{a.x, a.y, b.x, b.y}; }
DI v4u pack8(const f32x16& x, int s) { return v4u{pk2(x[8 * s], x[8 * s + 1]), pk2(x[8 * s + 2], x[8 * s + 3]), pk2(x[8 * s + 4], x[8 * s + 5]), pk2(x[8 * s + 6], x[8 * s + 7])}; }
DI void swap32(float& a, float& b) { auto r = __builtin_amdgcn_permlane32_swap(BC(u32, a), BC(u32, b), false, false); a = BC(float, r[0]); b = BC(float, r[1]); }
DI void swap16(float& a, float& b) { auto r = __builtin_amdgcn_permlane16_swap(BC(u32, a), BC(u32, b), false, false); a = BC(float, r[0]); b = BC(float, r[1]); }
DI void swap32u(u32& a, u32& b) { auto r = __builtin_amdgcn_permlane32_swap(a, b, false, false); a = r[0]; b = r[1]; }
template <int CTRL> DI u32 dppu(u32 x) { return (u32)__builtin_amdgcn_update_dpp(0, (int)x, CTRL, 0xf, 0xf, true); }
template <int CTRL> DI float dppf(float x) { return BC(float, dppu<CTRL>(BC(u32, x))); }
DI bool all_lanes(bool ok) { return __builtin_amdgcn_ballot_w64(!ok) == 0ull; }
#define BAR() asm volatile("s_waitcnt lgkmcnt(0)\n\ts_barrier" ::: "memory")
#define DRAIN() asm volatile("s_waitcnt vmcnt(0)" ::: "memory")
#define VMW(n) asm volatile("s_waitcnt vmcnt(%0)" ::"n"(n) : "memory")
#define PIN(x) asm volatile("" : "+v"(x))
#define SB() __builtin_amdgcn_sched_barrier(0)
#define FENCE() asm volatile("" ::: "memory")
template <int I> struct IC { static constexpr int v = I; };
template <int I, int N, class F> DI void sfor(F&& f) { if constexpr (I < N) { f(IC<I>{}); sfor<I + 1, N>(f); } }
template <int N> DI void nap() { if (N > 0) __builtin_amdgcn_s_sleep(N); }

DI v4u srd(const void* base, u32 bytes) { const u64 a = (u64)base; return v4u{(u32)a, (u32)(a >> 32) & 0xffffu, bytes, 0x00020000u}; }
template <bool COH, int N, int SS, bool PAIRS = false> DI void dma_run(v4u d, u32 voff, u32 ldst) {
  u32 keep, so;
  ldst = __builtin_amdgcn_readfirstlane(ldst);
#define DMA_BODY(C) \
  if (PAIRS) asm volatile("s_nop 4\n\ts_mov_b32 %0, m0\n\ts_mov_b32 m0, %4\n\ts_mov_b32 %1, 0\n\t.rept %5\n\tbuffer_load_dwordx4 %2, %3, %1 offen" C " lds\n\t" \
                          "buffer_load_dwordx4 %2, %3, %1 offen offset:1024" C " lds\n\ts_add_u32 m0, m0, 0x800\n\ts_add_u32 %1, %1, %6\n\t.endr\n\ts_mov_b32 m0, %0" \
                          : "=&s"(keep), "=&s"(so) : "v"(voff), "s"(d), "s"(ldst), "n"(N / 2), "n"(SS) : "memory", "scc"); \
  else asm volatile("s_nop 4\n\ts_mov_b32 %0, m0\n\ts_mov_b32 m0, %4\n\ts_mov_b32 %1, 0\n\t.rept %5\n\tbuffer_load_dwordx4 %2, %3, %1 offen" C " lds\n\t" \
                    "s_add_u32 m0, m0, 0x400\n\ts_add_u32 %1, %1, %6\n\t.endr\n\ts_mov_b32 m0, %0" \
                    : "=&s"(keep), "=&s"(so) : "v"(voff), "s"(d), "s"(ldst), "n"(N), "n"(SS) : "memory", "scc");
  if (COH) { DMA_BODY(" sc1") } else { DMA_BODY("") }
}
template <int N, int SS> DI void dma_rot(v4u d, u32 voff, u32 ldst, u32 rot) {
  u32 keep, so, ix;
  ldst = __builtin_amdgcn_readfirstlane(ldst);
  rot = __builtin_amdgcn_readfirstlane(rot);
  asm volatile("s_nop 4\n\ts_mov_b32 %0, m0\n\ts_mov_b32 %2, %6\n\t.rept %7\n\ts_lshl_b32 %1, %2, 10\n\ts_add_u32 m0, %1, %5\n\ts_mul_i32 %1, %2, %8\n\t"
               "buffer_load_dwordx4 %3, %4, %1 offen lds\n\ts_add_u32 %2, %2, 1\n\ts_and_b32 %2, %2, %9\n\t.endr\n\ts_mov_b32 m0, %0"
               : "=&s"(keep), "=&s"(so), "=&s"(ix) : "v"(voff), "s"(d), "s"(ldst), "s"(rot), "n"(N), "n"(SS), "n"(N - 1) : "memory", "scc");
}
DI v4u ld16(rsrc_t r, int off) { return __builtin_amdgcn_raw_buffer_load_b128(r, off, 0, SC1); }
DI u32 ld4(rsrc_t r, int off) { return __builtin_amdgcn_raw_buffer_load_b32(r, off, 0, SC1); }
DI u32 sum4(v4u x, u32 a) { return (a + x.x + x.y) + x.z + x.w; }
DI void st16(rsrc_t r, int off, v4u v) { __builtin_amdgcn_raw_buffer_store_b128(v, r, off, 0, SC1); }

DI void arrive(u32 ctr) {
  asm volatile("s_mov_b64 exec, 1\n\tds_add_u32 %0, %1\n\ts_mov_b64 exec, -1" ::"v"(ctr), "v"(1u) : "memory");
}
template <int NAP = 0> DI void until(u32 ctr, u32 target) {
  u32 t, u;
  if (NAP) asm volatile("s_branch 2f\n1:\n\ts_sleep %4\n2:\n\tds_read_b32 %0, %2\n\ts_waitcnt lgkmcnt(0)\n\tv_readfirstlane_b32 %1, %0\n\ts_cmp_lt_u32 %1, %3\n\ts_cbranch_scc1 1b" : "=&v"(t), "=&s"(u) : "v"(ctr), "s"(target), "n"(NAP) : "memory", "scc");
  else asm volatile("1:\n\tds_read_b32 %0, %2\n\ts_waitcnt lgkmcnt(0)\n\tv_readfirstlane_b32 %1, %0\n\ts_cmp_lt_u32 %1, %3\n\ts_cbranch_scc1 1b" : "=&v"(t), "=&s"(u) : "v"(ctr), "s"(target) : "memory", "scc");
}

DI void vote(u32 ctr, bool ok) {
  asm volatile("s_mov_b64 exec, 1\n\tds_add_u32 %0, %1\n\ts_mov_b64 exec, -1" ::"v"(ctr), "v"(ok ? 1u : 0x10001u) : "memory");
}
DI u32 verdict(u32 ctr, u32 target) {
  u32 t, u, v;
  asm volatile("1:\n\tds_read_b32 %0, %3\n\ts_waitcnt lgkmcnt(0)\n\tv_readfirstlane_b32 %1, %0\n\ts_and_b32 %2, %1, 0xffff\n\ts_cmp_lt_u32 %2, %4\n\ts_cbranch_scc1 1b"
               : "=&v"(t), "=&s"(u), "=&s"(v) : "v"(ctr), "s"(target) : "memory", "scc");
  return u >> 16;
}
DI v4u tag4(u32 t) { return v4u{t & 0xffu, (t + 64u) & 0xffu, (t + 128u) & 0xffu, (t + 192u) & 0xffu}; }
template <int N, int MODE> DI u32 fetch(rsrc_t r, v4u (&v)[N], int off, int stride, u32 tag) {
  u32 spins = 0;
#pragma nounroll
  for (;; ++spins) {
    FENCE();
#pragma unroll
    for (int i = 0; i < N; ++i) v[i] = ld16(r, off + i * stride);
    u32 a = 0u;
#pragma unroll
    for (int i = 0; i < N; ++i) {
      if (MODE == 0) a |= (v[i].y ^ tag) | (v[i].w ^ tag);
      if (MODE == 1) a = max(max(a, max(v[i].x, v[i].y)), max(v[i].z, v[i].w));
      if (MODE == 2) { v[i] ^= tag4(tag); a |= (v[i].x | v[i].y) | (v[i].z | v[i].w); }
    }
    const bool ok = MODE == 0 ? a == 0u : MODE == 1 ? a != ~0u : (a & 0xffu) == 0u;
    if (all_lanes(ok) || spins > SPIN_CAP) break;
    nap<D_RETRY>();
  }
  FENCE();
  return spins;
}

#define STV(i, v) (void)(v)
#define STC(i)
#define ST_PARAM
#define ST_ARG
#define ST_FLUSH()
#define ST(i)

DI void dual_softmax(const f32x16& x, int o, float c, v4u& plus, v4u& minus) {
  float mx = mx3(mx3(x[o], x[o + 1], x[o + 2]), mx3(x[o + 3], x[o + 4], x[o + 5]), mx3(x[o + 6], x[o + 7], x[o]));
  float nm = mx3(mx3(-x[o], -x[o + 1], -x[o + 2]), mx3(-x[o + 3], -x[o + 4], -x[o + 5]), mx3(-x[o + 6], -x[o + 7], -x[o]));
  swap32(mx, nm);
  float t = mx3(mx, nm, nm) * -c, t2 = t;
  swap32(t, t2);
  f32x2 ep[4], em[4];
  const f32x2 c2 = {c, c}, tp = {t, t}, tm = {t2, t2};
#pragma unroll
  for (int i = 0; i < 4; ++i) { const f32x2 v = {x[o + 2 * i], x[o + 2 * i + 1]}; ep[i] = __builtin_elementwise_fma(v, c2, tp); em[i] = __builtin_elementwise_fma(-v, c2, tm); }
#pragma unroll
  for (int i = 0; i < 4; ++i) {
    ep[i] = f32x2{__builtin_amdgcn_exp2f(ep[i].x), __builtin_amdgcn_exp2f(ep[i].y)};
    em[i] = f32x2{__builtin_amdgcn_exp2f(em[i].x), __builtin_amdgcn_exp2f(em[i].y)};
  }
  const f32x2 sp2 = (ep[0] + ep[1]) + (ep[2] + ep[3]), sm2 = (em[0] + em[1]) + (em[2] + em[3]);
  float sp = op(sp2.x + sp2.y), sm = op(sm2.x + sm2.y);
  swap32(sp, sm);
  float ip = __builtin_amdgcn_rcpf(sp + sm), im = ip;
  swap32(ip, im);
#pragma unroll
  for (int i = 0; i < 4; ++i) {
    plus[i] = BC(u32, __builtin_convertvector(ep[i] * ip, v2bf));
    minus[i] = BC(u32, __builtin_convertvector(em[i] * im, v2bf));
  }
}

struct Ctx {
  LAS unsigned char* L;
  rsrc_t r;
  u32 tag;
  int b, w, lane;
};

template <int N> DI void gemm(f32x16& acc, const v4u (&W)[32], LAS unsigned char* xb) {
  constexpr int PD = 4;
  v4u x[PD];
#pragma unroll
  for (int i = 0; i < PD; ++i) x[i] = *(LAS v4u*)(xb + i * 1024);
  SB();
#pragma unroll
  for (int i = 0; i < N; ++i) {
    const v4u cur = x[i % PD];
    acc = mfma32(W[i], cur, acc);
    if (i + PD < N) x[i % PD] = *(LAS v4u*)(xb + (i + PD) * 1024);
    SB();
  }
  PIN(acc);
}
DI void row_moments(LAS unsigned char* st, int lane, float& m, float& rs) {
  LAS f32x4* s = (LAS f32x4*)(st + (lane & 31) * 16);
  const f32x4 a = s[0], b = s[32], c = s[64], d = s[96];
  m = op(op(op(a.x + a.z) + op(b.x + b.z)) + op(op(c.x + c.z) + op(d.x + d.z))) * (-1.f / 512.f);
  const float e2 = op(op(op(a.y + a.w) + op(b.y + b.w)) + op(op(c.y + c.w) + op(d.y + d.w))) * (1.f / 512.f);
  rs = __builtin_amdgcn_rsqf(mx3(__builtin_fmaf(-m, m, op(e2)), 0.f, 0.f) + 1e-5f);
}
DI void row_stats(LAS unsigned char* L, int T, int lane, u32& mf, float& rs) {
  LAS f32x4* s = (LAS f32x4*)(L + L_ST + T * 1024 + (lane & 31) * 16);
  const f32x4 a = s[0], b = s[32];
  const float m = op(op(a.x + a.y) + op(a.z + a.w)) * (-1.f / 512.f);
  const float e2 = op(op(b.x + b.y) + op(b.z + b.w)) * (1.f / 512.f);
  rs = __builtin_amdgcn_rsqf(mx3(__builtin_fmaf(-m, m, op(e2)), 0.f, 0.f) + 1e-5f);
  const float hi = bflo(pk2(m, m));
  mf = pk2(hi, op(m - hi));
}
struct Stat {
  f32x4 a, b;
  float m, e2, rs, hi;
  u32 mf;
  template <int S> DI void step(LAS unsigned char* s) {
    if constexpr (S == 0) { a = *(LAS f32x4*)s; b = *(LAS f32x4*)(s + 512); }
    if constexpr (S == 3) { m = op(a.x + a.y); hi = op(a.z + a.w); e2 = op(b.x + b.y); rs = op(b.z + b.w); }
    if constexpr (S == 4) { m = op(op(m + hi) * (-1.f / 512.f)); e2 = op(op(e2 + rs) * (1.f / 512.f)); }
    if constexpr (S == 5) { e2 = op(mx3(__builtin_fmaf(-m, m, e2), 0.f, 0.f) + 1e-5f); hi = op(bflo(pk2(m, m))); }
    if constexpr (S == 6) { rs = __builtin_amdgcn_rsqf(e2); mf = pk2(hi, op(m - hi)); PIN(rs); PIN(mf); }
  }
};
DI void acc_stats(v4u x, float& s1, float& s2) {
  s1 = dot2(x.x, ONES, s1); s2 = dot2(x.x, x.x, s2); s1 = dot2(x.y, ONES, s1); s2 = dot2(x.y, x.y, s2);
  s1 = dot2(x.z, ONES, s1); s2 = dot2(x.z, x.z, s2); s1 = dot2(x.w, ONES, s1); s2 = dot2(x.w, x.w, s2);
}
template <int N0, int N1> DI void cs_acc(const v4u (&W)[32], float& cs, float& cs2) {
#pragma unroll
  for (int i = N0; i < N1; ++i) { cs = dot2(W[i].x, ONES, cs); cs2 = dot2(W[i].y, ONES, cs2); cs = dot2(W[i].z, ONES, cs); cs2 = dot2(W[i].w, ONES, cs2); }
}
DI v2u cs_pack(float cs) {
  const u32 hh = pk2(cs, cs);
  const float lo = cs - bflo(hh);
  v2u o = {hh, pk2(lo, lo)};
  PIN(o);
  return o;
}
template <int N0, int N1> DI v2u colsum(const v4u (&W)[32]) {
  float cs = 0.f, cs2 = 0.f;
  cs_acc<N0, N1>(W, cs, cs2);
  return cs_pack(cs + cs2);
}
DI u32 opu(u32 r) { asm("" : "+v"(r)); return r; }
DI u32 sum4k(LAS unsigned char* x) {
  const v4u a = *(LAS v4u*)x, b = *(LAS v4u*)(x + 1024), c = *(LAS v4u*)(x + 2048), d = *(LAS v4u*)(x + 3072);
  return opu(opu(a.x + a.y + a.z) + opu(b.x + b.y + b.z) + opu(a.w + b.w + c.w)) + opu(c.x + c.y + c.z) + opu(opu(d.x + d.y + d.z) + d.w);
}
DI u32 load_image(const Params& p, rsrc_t r, LAS unsigned char* L, int rb, int w, int lane, u32 tag) {
  const int f0 = w * 8;
  u32 spins = 0;
  v2u rec[4];
#pragma nounroll
  for (;; ++spins) {
    dma_run<true, 8, 131072, true>(srd(p.ws, WS_BYTES), B_UT + (w & 3) * 524288 + (rb * 2 + (w >> 2)) * 2048 + lane * 16, R2 + f0 * 1024);
#pragma unroll
    for (int i = 0; i < 4; ++i) rec[i] = __builtin_amdgcn_raw_buffer_load_b64(r, B_XR + rb * 16384 + f0 * 256 + i * 512 + lane * 8, 0, SC1);
    LAS unsigned char* x0 = L + R2 + f0 * 1024 + lane * 16;
    VMW(8);
    u32 cs = 4u * tag + sum4k(x0);
    VMW(4);
    cs += sum4k(x0 + 4096);
    FENCE();
    if (all_lanes(cs + (rec[0].x + rec[1].x + rec[2].x + rec[3].x) == rec[0].y + rec[1].y + rec[2].y + rec[3].y) || spins > SPIN_CAP) break;
    nap<D_RETRY>();
  }
  *(LAS float*)(L + L_ST + (w >> 2) * 1024 + lane * 16 + (w & 3) * 4) = op(BC(float, rec[0].x) + BC(float, rec[1].x)) + op(BC(float, rec[2].x) + BC(float, rec[3].x));
  arrive(L_C1 + 20 + (w >> 2) * 4);
  return spins;
}
struct XOut {
  int off, roff;
  DI void prepare(int rb, int hf, int ks0, int lane) {
    off = B_UT + ks0 * 65536 + (rb * 2 + hf) * 2048 + lane * 16;
    roff = B_XR + rb * 16384 + (hf * 32 + ks0) * 256 + lane * 8;
    PIN(off); PIN(roff);
  }
  DI void publish(const f32x2 (&x)[8], rsrc_t r, u32 tag) {
    v4u a, b;
#pragma unroll
    for (int i = 0; i < 4; ++i) a[i] = BC(u32, __builtin_convertvector(x[i], v2bf));
    st16(r, off, a);
#pragma unroll
    for (int i = 0; i < 4; ++i) b[i] = BC(u32, __builtin_convertvector(x[4 + i], v2bf));
    st16(r, off + 1024, b);
    SB();
    float s1 = 0.f, s2 = 0.f, t1 = 0.f, t2 = 0.f;
    acc_stats(a, s1, s2); acc_stats(b, t1, t2);
    const u32 cs = opu(sum4(a, tag)) + opu(sum4(b, 0u));
    s1 += t1; s2 += t2;
    swap32(s1, s2);
    const u32 st = BC(u32, s1 + s2);
    __builtin_amdgcn_raw_buffer_store_b64(v2u{st, cs + st}, r, roff, 0, SC1);
    SB();
  }
};
DI f32x2 unpk(u32 v) { return f32x2{bflo(v), bfhi(v)}; }

struct Post {
  v4u selp, selm, kb, vb, fp, fm, v0, v1;
  u32 ones;
  f32x16 pr, vt, pt;
  float vs[8], ep[8], em[8], mx, nm, t, t2, cc, s0, s1, s2, s3;
  DI void init(int n, int h) {
    const int n16 = n & 15, j = 4 * (n16 >> 3) + (n16 & 3);
    const u32 one = (((n16 >> 2) & 1) == h) ? (0x3f80u << (16 * (j & 1))) : 0u;
    const u32 op_ = n < 16 ? one : 0u, om = n < 16 ? 0u : one;
    selp = v4u{(j >> 1) == 0 ? op_ : 0u, (j >> 1) == 1 ? op_ : 0u, (j >> 1) == 2 ? op_ : 0u, (j >> 1) == 3 ? op_ : 0u};
    selm = v4u{(j >> 1) == 0 ? om : 0u, (j >> 1) == 1 ? om : 0u, (j >> 1) == 2 ? om : 0u, (j >> 1) == 3 ? om : 0u};
    ones = n < 16 ? 0u : ONES;
    PIN(selp); PIN(selm); PIN(ones);
  }
  template <int S> DI void step(const f32x16& a, float rs, v4u wph, f32x16& kv, bool first) {
    f32x16 zero;
#pragma unroll
    for (int i = 0; i < 16; ++i) zero[i] = 0.f;
    if constexpr (S == 0) { kb = pack8(a, 0); cc = rs * LOG2E; }
    if constexpr (S == 1) { pr = mfma32(wph, kb, zero); _Pragma("unroll") for (int i = 0; i < 8; ++i) vs[i] = op(a[8 + i] * rs); }
    if constexpr (S == 2) vb = v4u{pk2(vs[0], vs[1]), pk2(vs[2], vs[3]), pk2(vs[4], vs[5]), pk2(vs[6], vs[7])};
    if constexpr (S == 3) {
      PIN(pr);
      vt = mfma32(vb, selp, zero);
      mx = mx3(mx3(pr[0], pr[1], pr[2]), mx3(pr[3], pr[4], pr[5]), mx3(pr[6], pr[7], pr[0]));
      nm = mx3(mx3(-pr[0], -pr[1], -pr[2]), mx3(-pr[3], -pr[4], -pr[5]), mx3(-pr[6], -pr[7], -pr[0]));
    }
    if constexpr (S == 4) { swap32(mx, nm); t = mx3(mx, nm, nm) * -cc; t2 = t; }
    if constexpr (S == 5) { swap32(t, t2); v0.x = pk2(vt[0], vt[1]) | ones; }
    if constexpr (S == 6) _Pragma("unroll") for (int i = 0; i < 5; ++i) ep[i] = op(__builtin_fmaf(pr[i], cc, t));
    if constexpr (S == 7) { _Pragma("unroll") for (int i = 5; i < 8; ++i) ep[i] = op(__builtin_fmaf(pr[i], cc, t)); _Pragma("unroll") for (int i = 0; i < 2; ++i) em[i] = op(__builtin_fmaf(pr[i], -cc, t2)); }
    if constexpr (S == 8) _Pragma("unroll") for (int i = 2; i < 7; ++i) em[i] = op(__builtin_fmaf(pr[i], -cc, t2));
    if constexpr (S == 9) { em[7] = op(__builtin_fmaf(pr[7], -cc, t2)); ep[0] = __builtin_amdgcn_exp2f(ep[0]); ep[1] = __builtin_amdgcn_exp2f(ep[1]); }
    if constexpr (S == 10) { ep[2] = __builtin_amdgcn_exp2f(ep[2]); ep[3] = __builtin_amdgcn_exp2f(ep[3]); s0 = op(ep[0] + ep[1]); }
    if constexpr (S == 11) { ep[4] = __builtin_amdgcn_exp2f(ep[4]); ep[5] = __builtin_amdgcn_exp2f(ep[5]); s1 = op(ep[2] + ep[3]); }
    if constexpr (S == 12) { ep[6] = __builtin_amdgcn_exp2f(ep[6]); ep[7] = __builtin_amdgcn_exp2f(ep[7]); s0 = op(s0 + s1); }
    if constexpr (S == 13) { em[0] = __builtin_amdgcn_exp2f(em[0]); em[1] = __builtin_amdgcn_exp2f(em[1]); s1 = op(ep[4] + ep[5]); }
    if constexpr (S == 14) { em[2] = __builtin_amdgcn_exp2f(em[2]); em[3] = __builtin_amdgcn_exp2f(em[3]); s2 = op(ep[6] + ep[7]); }
    if constexpr (S == 15) { em[4] = __builtin_amdgcn_exp2f(em[4]); em[5] = __builtin_amdgcn_exp2f(em[5]); s1 = op(s1 + s2); }
    if constexpr (S == 16) { em[6] = __builtin_amdgcn_exp2f(em[6]); em[7] = __builtin_amdgcn_exp2f(em[7]); s0 = op(s0 + s1); }
    if constexpr (S == 17) { s1 = op(em[0] + em[1]); s2 = op(em[2] + em[3]); s3 = op(em[4] + em[5]); t = op(em[6] + em[7]); s1 = op(s1 + s2); }
    if constexpr (S == 18) { s3 = op(s3 + t); v0.y = pk2(vt[2], vt[3]) | ones; s1 = op(s1 + s3); }
    if constexpr (S == 19) { swap32(s0, s1); t = __builtin_amdgcn_rcpf(s0 + s1); }
    if constexpr (S == 20) { t2 = t; v0.z = pk2(vt[4], vt[5]) | ones; swap32(t, t2); }
    if constexpr (S == 21) _Pragma("unroll") for (int i = 0; i < 5; ++i) ep[i] = op(ep[i] * t);
    if constexpr (S == 22) { _Pragma("unroll") for (int i = 5; i < 8; ++i) ep[i] = op(ep[i] * t); fp.x = pk2(ep[0], ep[1]); fp.y = pk2(ep[2], ep[3]); }
    if constexpr (S == 23) { fp.z = pk2(ep[4], ep[5]); fp.w = pk2(ep[6], ep[7]); _Pragma("unroll") for (int i = 0; i < 3; ++i) em[i] = op(em[i] * t2); }
    if constexpr (S == 24) { pt = mfma32(fp, selp, zero); _Pragma("unroll") for (int i = 3; i < 8; ++i) em[i] = op(em[i] * t2); fm = v4u{pk2(em[0], em[1]), pk2(em[2], em[3]), pk2(em[4], em[5]), pk2(em[6], em[7])}; }
    if constexpr (S == 25) { pt = mfma32(fm, selm, pt); v0.w = pk2(vt[6], vt[7]) | ones; v1.x = pk2(vt[8], vt[9]) | ones; v1.y = pk2(vt[10], vt[11]) | ones; v1.z = pk2(vt[12], vt[13]) | ones; v1.w = pk2(vt[14], vt[15]) | ones; }
    if constexpr (S == 27) fp = pack8(pt, 0);
    if constexpr (S == 28) { kv = mfma32(fp, v0, first ? zero : kv); fm = pack8(pt, 1); }
    if constexpr (S == 29) kv = mfma32(fm, v1, kv);
  }
  DI void tail(const f32x16& a, float rs, v4u wph, f32x16& kv) {
    f32x16 zero;
#pragma unroll
    for (int i = 0; i < 16; ++i) zero[i] = 0.f;
    kv = mfma32(fp, v0, zero);
    fm = pack8(pt, 1);
    const float c2 = op(rs * LOG2E);
    SB();
    kb = pack8(a, 0);
    SB();
    pr = mfma32(wph, kb, zero);
    kv = mfma32(fm, v1, kv);
    vb = v4u{pk2(op(a[8] * rs), op(a[9] * rs)), pk2(op(a[10] * rs), op(a[11] * rs)), pk2(op(a[12] * rs), op(a[13] * rs)), pk2(op(a[14] * rs), op(a[15] * rs))};
    SB();
    vt = mfma32(vb, selp, zero);
    SB();
    dual_softmax(pr, 0, c2, fp, fm);
    pt = mfma32(fp, selp, zero);
    pt = mfma32(fm, selm, pt);
    v0 = pack8(vt, 0) | ones; v1 = pack8(vt, 1) | ones;
    kv = mfma32(pack8(pt, 0), v0, kv);
    kv = mfma32(pack8(pt, 1), v1, kv);
  }
};

template <int LY, bool KV>
DI void layer(const Params& p, const Ctx& c, v4u (&W)[32], v2u& csf, const v4u wph ST_PARAM) {
  LAS unsigned char* L = c.L;
  const rsrc_t r = c.r;
  int lane = c.lane;
  PIN(lane);
  const int b = c.b, w = c.w, rb = b & 31, cg = b >> 5, hf = cg & 1, cq = cg >> 1;
  const int G = lane >> 4, q = (lane >> 2) & 3, pp = lane & 3, h = lane >> 5, n = lane & 31;
  constexpr int S0 = LY ? 20 : 10, S1 = LY ? 50 : 40;
  constexpr u32 C4 = LY ? 8 : 4;
  constexpr u32 CX = L_C1 + 20 + LY * 4;
  const u32 tagl = c.tag + LY;
  const v4u z4 = {0u, 0u, 0u, 0u};
  f32x16 zero;
#pragma unroll
  for (int i = 0; i < 16; ++i) zero[i] = 0.f;
  v4u x0, x1;
  if (KV) {
    const v4u cs4 = {csf.x, csf.y, 0u, 0u};
    f32x16 acc0 = zero, acc1 = zero, kv;
    Post ps;
    Stat s0, s1;
    ps.init(n, h);
    const int head = 4 * cg + w;
    int so = B_YI + rb * 65536 + (head * 8 + h * 2) * 256 + (n >> 2) * 128 + (n & 3) * 16, so1 = B_ZP + (head * 32 + rb) * 128 + h * 32;
    PIN(so); PIN(so1);
    so = n < 16 ? so : n == 16 ? so1 : 0x40000000;
    so1 = so + (n < 16 ? 64 : 16);
    int so2 = so + (n < 16 ? 1024 : 64), so3 = so + (n < 16 ? 1088 : 80);
    const v4u tb4 = tag4(tagl);
    u32 tagv = tb4.x | (tb4.y << 8) | (tb4.z << 16) | (tb4.w << 24);
    PIN(so); PIN(so1); PIN(so2); PIN(so3); PIN(tagv);
    nap<LY ? D_XI - 2 : D_X0>();
    STV(LY ? 38 : 57, load_image(p, r, L, rb, w, lane, tagl));
    ST(S0);
    {
      constexpr int PD = 4;
      LAS unsigned char* xb = L + R2 + lane * 16;
      LAS unsigned char* sb = L + L_ST + n * 16;
      v4u x[PD];
      u32 seen;
      until(L_C1 + 20, C4);
#pragma unroll
      for (int i = 0; i < PD; ++i) x[i] = *(LAS v4u*)(xb + i * 1024);
      SB();
      sfor<0, 64>([&](auto I) __attribute__((always_inline)) {
        constexpr int i = decltype(I)::v;
        const v4u cur = x[i % PD];
        if constexpr (i < 32) acc0 = mfma32(W[i & 31], cur, acc0); else acc1 = mfma32(W[i & 31], cur, acc1);
        if constexpr (i == 23) seen = *(volatile LAS u32*)(L + L_C1 + 24);
        if constexpr (i == 27) { if (__builtin_amdgcn_readfirstlane(seen) < C4) until(L_C1 + 24, C4); FENCE(); }
        if constexpr (i + PD < 64) x[i % PD] = *(LAS v4u*)(xb + (i + PD) * 1024);
        if constexpr (i >= 1 && i < 8) s0.template step<i - 1>(sb);
        if constexpr (i == 10) acc0 = mfma32(cs4, v4u{s0.mf, s0.mf, 0u, 0u}, acc0);
        if constexpr (i >= 27 && i < 34) s1.template step<i - 27>(sb + 1024);
        if constexpr (i == 60) acc1 = mfma32(cs4, v4u{s1.mf, s1.mf, 0u, 0u}, acc1);
        if constexpr (i >= 34 && i < 62) ps.template step<i - 34>(acc0, s0.rs, wph, kv, true);
        SB();
      });
    }
    ST(S1 + 2);
    ps.tail(acc1, s1.rs, wph, kv);
#define TG(i) __builtin_amdgcn_perm(BC(u32, kv[i]), tagv, 0x07060500u + ((i) & 3))
    st16(r, so, v4u{TG(0), TG(1), TG(2), TG(3)}); st16(r, so1, v4u{TG(4), TG(5), TG(6), TG(7)});
    st16(r, so2, v4u{TG(8), TG(9), TG(10), TG(11)}); st16(r, so3, v4u{TG(12), TG(13), TG(14), TG(15)});
    ST(S0 + 1);
    arrive(L_C1 + 16);
    if (w < 2 || (w == 2 && (b & 7) == 0)) {
      __builtin_amdgcn_s_setprio(3);
      const int src = w < 2 ? B_YI + (lane >> 3) * 65536 + b * 256 + w * 128 + (lane & 7) * 16 : B_ZP + (b >> 3) * 4096 + lane * 16;
      const int step = w < 2 ? 8 * 65536 : 1024;
      int dst = w < 2 ? (lane < 4 ? B_KVR + LY * 32768 + b * 128 + w * 64 + lane * 16 : 0x40000000)
                      : ((lane < 8 && !(lane & 1)) ? B_ZR + LY * 4096 + (b >> 3) * 128 + lane * 8 : 0x40000000);
      PIN(dst);
      nap<D_KVP>();
      float s[4];
      u32 spins = 0;
#pragma nounroll
      for (;; ++spins) {
        FENCE();
        v4u t[4];
#pragma unroll
        for (int i = 0; i < 4; ++i) t[i] = __builtin_amdgcn_raw_buffer_load_b128(r, src, i * step, SC1) ^ tb4;
        const u32 a = opu(opu(t[0].x | t[0].y | t[0].z) | opu(t[1].x | t[1].y | t[1].z) | opu(t[0].w | t[1].w | t[2].w)) | opu(t[2].x | t[2].y | t[2].z) | opu(opu(t[3].x | t[3].y | t[3].z) | t[3].w);
#pragma unroll
        for (int i = 0; i < 4; ++i) s[i] = op(op(BC(float, t[0][i]) + BC(float, t[1][i])) + op(BC(float, t[2][i]) + BC(float, t[3][i])));
        swap32(s[0], s[2]); swap32(s[1], s[3]);
        s[0] = op(s[0] + s[2]); s[1] = op(s[1] + s[3]);
        swap16(s[0], s[1]);
        s[0] = op(s[0] + s[1]);
        s[0] = op(s[0] + dppf<0x128>(s[0]));
        PIN(s[0]);
        if (all_lanes((a & 0xffu) == 0u) || spins > SPIN_CAP) break;
      }
      STV(58 + LY, spins);
      ST(S0 + 3);
      s[1] = s[0];
      swap16(s[0], s[1]);
      u32 d0 = pk2(s[0], s[1]), d1 = d0;
      swap32u(d0, d1);
      u32 e0, e1;
      if (w < 2) { e0 = dppu<0x104>(d0); e1 = dppu<0x104>(d1); } else { e0 = dppu<0x101>(d0); e1 = dppu<0x101>(d1); }
      st16(r, dst, v4u{d0, d1, e0, e1});
      __builtin_amdgcn_s_setprio(0);
      if (w == 0) {
        nap<D_KVR>();
        arrive(L_C1 + 8);
      }
    }
    if (LY == 0 && w == 3) {
      const int src = q * 1024 + cq * 256 + G * 64 + pp * 16;
      until<1>(L_C1 + 12, 4);
      until(L_C1 + 16, 4);
      dma_run<false, 64, 4096>(srd(p.wo, 512 * 1024), src, R1A);
      until<1>(L_C1, 4);
      dma_run<false, 64, 4096>(srd(p.wo, 512 * 1024), src + 262144, R2);
      DRAIN();
    }
  } else {
    const int hp = (w >> 1) & 1, kh = w & 1;
    nap<D_XI + D_XQ>();
    STV(LY ? 38 : 57, load_image(p, r, L, rb, w, lane, tagl));
    ST(S0);
    if (LY == 0) {
      until(L_C1 + 20, 4);
      dma_rot<16, 8 * 3072>(srd(p.wqkv, 512 * 3072), ((w - 4) * 128 + ((lane >> 2) & 7)) * 3072 + cg * 128 + h * 64 + pp * 16, R1A + (w - 4) * 16384,
                            ROT == 0 ? 0 : (ROT == 1 || ((((u32)(u64)p.wqkv >> 7) + cg) & 7) == 3) ? 4 * (rb >> 3) : 0);
      u32 pf = 0;
      if (lane < 32) pf = *(const GAS u32*)((const GAS unsigned char*)p.wo + ((b >> 3) * 128 + (w - 4) * 32 + lane) * 128);
      VMW(1);
      arrive(L_C1 + 4);
      until<1>(L_C1 + 4, 4);
      ST(S1 + 0);
      LAS unsigned char* wb = L + R1A + kh * 32768 + hp * 512 + (4 * h + q) * 64 + 32 * (G & 1) + 8 * pp;
#pragma unroll
      for (int i = 0; i < 16; ++i) W[i] = trd2(wb + i * 2048, 1024);
      arrive(L_C1 + 12);
      asm volatile("" ::"v"(pf));
      if (CS_EARLY == 1) csf = colsum<0, 16>(W);
    }
    int ko0 = B_KVR + LY * 32768 + (4 * cg + 2 * hp) * 1024 + h * 256 + n * 16, ko1 = B_ZR + LY * 4096 + (4 * cg + 2 * hp) * 128 + h * 16;
    PIN(ko0); PIN(ko1);
    ko0 = n < 16 ? ko0 : ko1;
    ko1 = ko0 + (n < 16 ? 512 : 32);
    int ko2 = ko0 + (n < 16 ? 1024 : 128), ko3 = ko0 + (n < 16 ? 1536 : 160);
    int oo = B_YI + rb * 65536 + (2 * cg + hp) * 4096 + kh * 2048 + lane * 16, oro = B_OR + ((rb * 2 + kh) * 32 + 4 * cg + 2 * hp) * 128 + lane * 4;
    PIN(ko0); PIN(ko1); PIN(ko2); PIN(ko3); PIN(oo); PIN(oro);
    ST(S1 + 1);
    until<1>(L_C1 + 16, C4);
    ST(S1 + 2);
    if (LY == 0 && CS_EARLY == 0) csf = colsum<0, 16>(W);
    const v4u cs4 = {csf.x, csf.y, 0u, 0u};
    u32 mf; float rs;
    row_stats(L, kh ^ 1, lane, mf, rs);
    f32x16 acco = mfma32(cs4, v4u{mf, mf, 0u, 0u}, zero);
    row_stats(L, kh, lane, mf, rs);
    f32x16 acck = mfma32(cs4, v4u{mf, mf, 0u, 0u}, zero);
    gemm<16>(acco, W, L + R2 + (kh ^ 1) * 32768 + kh * 16384 + lane * 16);
    gemm<16>(acck, W, L + R2 + kh * 32768 + kh * 16384 + lane * 16);
    ST(S1 + 3);
#pragma unroll
    for (int i = 0; i < 4; ++i) *(LAS f32x4*)(L + L_EX + ((w - 4) * 4 + i) * 1024 + lane * 16) = f32x4{acco[4 * i], acco[4 * i + 1], acco[4 * i + 2], acco[4 * i + 3]};
    {
      LAS unsigned char* xb = L + R2 + (hf * 32 + 8 * cq + 2 * (w - 4)) * 1024 + lane * 16;
      x0 = *(LAS v4u*)xb; x1 = *(LAS v4u*)(xb + 1024);
    }
    arrive(L_C1);
    until(L_C1, C4);
    ST(S1 + 5);
#pragma unroll
    for (int i = 0; i < 4; ++i) {
      const f32x4 t = *(LAS f32x4*)(L + L_EX + (((w - 4) ^ 1) * 4 + i) * 1024 + lane * 16);
      acck[4 * i] += t.x; acck[4 * i + 1] += t.y; acck[4 * i + 2] += t.z; acck[4 * i + 3] += t.w;
    }
    f32x16 pr = mfma32(n < 16 ? wph : z4, pack8(acck, 0), zero);
    pr = mfma32(n < 16 ? z4 : wph, pack8(acck, 1), pr);
    v4u ph[4];
    dual_softmax(pr, 0, rs * LOG2E, ph[0], ph[1]);
    dual_softmax(pr, 8, rs * LOG2E, ph[2], ph[3]);
    PIN(ph[0]); PIN(ph[1]); PIN(ph[2]); PIN(ph[3]);
    ST(S0 + 1);
    until(L_C1 + 8, LY + 1);
    ST(S0 + 3);
    f32x16 oa, ob2;
    {
      u32 spins = 0;
#pragma nounroll
      for (;; ++spins) {
        FENCE();
        const v4u k0 = ld16(r, ko0), k1 = ld16(r, ko1), k2 = ld16(r, ko2), k3 = ld16(r, ko3);
        SB();
        oa = mfma32(k0, ph[0], zero); oa = mfma32(k1, ph[1], oa);
        ob2 = mfma32(k2, ph[2], zero); ob2 = mfma32(k3, ph[3], ob2);
        SB();
#define MX3(a, b, c) opu(max(max(a, b), c))
        const u32 a = MX3(MX3(MX3(k0.x, k0.y, k0.z), MX3(k1.x, k1.y, k1.z), MX3(k0.w, k1.w, k2.w)), MX3(k2.x, k2.y, k2.z), MX3(MX3(k3.x, k3.y, k3.z), k3.w, k3.w));
        if (all_lanes(a != ~0u) || spins > SPIN_CAP) break;
      }
      STV(62 + LY, spins);
    }
    ST(S0 + 5);
    {
      const float ia = __builtin_amdgcn_rcpf(oa[8] + 1e-6f);
      const v4u fa = v4u{pk2(op(oa[0] * ia), op(oa[1] * ia)), pk2(op(oa[2] * ia), op(oa[3] * ia)), pk2(op(oa[4] * ia), op(oa[5] * ia)), pk2(op(oa[6] * ia), op(oa[7] * ia))};
      st16(r, oo, fa);
      SB();
      const float ib = __builtin_amdgcn_rcpf(ob2[8] + 1e-6f);
      const v4u fb = v4u{pk2(ob2[0] * ib, ob2[1] * ib), pk2(ob2[2] * ib, ob2[3] * ib), pk2(ob2[4] * ib, ob2[5] * ib), pk2(ob2[6] * ib, ob2[7] * ib)};
      st16(r, oo + 1024, fb);
      __builtin_amdgcn_raw_buffer_store_b32(sum4(fb, opu(sum4(fa, tagl))), r, oro, 0, SC1);
      SB();
    }
  }
  ST(S0 + 2);
  BAR();
  ST(S0 + 4);
  LAS unsigned char* ob = L + R2 + (hf ^ 1) * 32768 + lane * 16;
  if (KV) {
    nap<D_OI>();
    if (LY == 0) until(L_C1 + 28, 4);
    u32 tries = 0;
    const int fo = (rb * 2 + hf) * 32 + 4 * w;
#pragma nounroll
    for (;; ++tries) {
      dma_run<true, 4, 4096, true>(srd(p.ws, WS_BYTES), B_YI + rb * 65536 + (4 + w) * 8192 + hf * 2048 + lane * 16, R2 + (hf ^ 1) * 32768 + (16 + 4 * w) * 1024);
      dma_run<true, 4, 4096, true>(srd(p.ws, WS_BYTES), B_YI + rb * 65536 + w * 8192 + hf * 2048 + lane * 16, R2 + (hf ^ 1) * 32768 + 4 * w * 1024);
      v4u rec;
#pragma unroll
      for (int i = 0; i < 4; ++i) rec[i] = ld4(r, B_OR + (fo + (i >> 1) * 16) * 128 + (i & 1) * 256 + lane * 4);
      VMW(8);
      if (tries == 0) BAR();
      u32 a = 4u * tagl + sum4k(ob + (16 + 4 * w) * 1024);
      VMW(4);
      a += sum4k(ob + 4 * w * 1024);
      FENCE();
      if (all_lanes(a == sum4(rec, 0u)) || tries > SPIN_CAP) break;
    }
    vote(CX + 12, tries == 0);
    STV(48 + LY, tries);
  } else if (LY == 0) {
#pragma unroll
    for (int i = 0; i < 16; ++i) W[16 + i] = trd2(L + R2 + i * 4096 + h * 1024 + (w - 4) * 256 + q * 64 + 32 * (G & 1) + 8 * pp, 2048);
    arrive(L_C1 + 28);
  }
  if (!KV) {
    const int ct = w - 4, ks0 = 8 * cq + 2 * ct;
    LAS unsigned char* wb = L + R1A + h * 1024 + ct * 256 + q * 64 + 32 * (G & 1) + 8 * pp;
    f32x16 acc;
    XOut xo;
    int spo = lane < 32 ? B_SP + ((rb * 2 + hf) * 16 + cq * 4 + ct) * 256 + n * 8 : 0x40000000, spi = B_SP + ((rb * 2 + hf) * 16 + 8 * h) * 256 + n * 8;
    LAS unsigned char *ew = L + L_EX + ct * 4096 + n * 64 + h * 8, *er = L + L_EX + ct * 4096 + (lane >> 2) * 64 + (((lane & 3) ^ ((lane >> 3) & 3)) * 16);
    int oo = (rb * 64 + hf * 32 + (lane >> 2)) * 1024 + cq * 256 + ct * 64 + (lane & 3) * 16;
    LAS unsigned char* ew4[4];
    if (LY == 0) { xo.prepare(rb, hf, ks0, lane); PIN(spo); PIN(spi); }
    else {
#pragma unroll
      for (int i = 0; i < 4; ++i) { ew4[i] = ew + ((i ^ ((n >> 1) & 3)) * 16); PIN(ew4[i]); }
      PIN(er); PIN(oo);
    }
    constexpr int PD = 4;
    v4u o[PD], a[PD];
#pragma unroll
    for (int i = 0; i < PD; ++i) a[i] = trd2(wb + i * 4096, 2048);
#pragma nounroll
    for (u32 pass = 0;; ++pass) {
#pragma unroll
      for (int i = 0; i < 4; ++i) { acc[2 * i] = bflo(x0[i]); acc[2 * i + 1] = bfhi(x0[i]); acc[8 + 2 * i] = bflo(x1[i]); acc[9 + 2 * i] = bfhi(x1[i]); }
      PIN(acc);
      if (pass == 0) BAR();
      FENCE();
      ST(S0 + 6);
      u32 seen;
#pragma unroll
      for (int i = 0; i < PD; ++i) o[i] = *(LAS v4u*)(ob + (16 + i) * 1024);
      SB();
#pragma unroll
      for (int i = 0; i < 13; ++i) {
        acc = mfma32(W[16 + i], o[i % PD], acc);
        if (i == 10) seen = *(volatile LAS u32*)(L + CX + 12);
        if (i < 12) o[i % PD] = *(LAS v4u*)(ob + (16 + i + PD) * 1024);
        SB();
      }
      if (pass || __builtin_amdgcn_readfirstlane(seen) == 4u || verdict(CX + 12, 4) == 0u) break;
    }
    FENCE();
    o[0] = *(LAS v4u*)ob;
    SB();
#pragma unroll
    for (int i = 13; i < 32; ++i) {
      acc = mfma32(i < 16 ? W[16 + (i & 15)] : a[i % PD], o[i % PD], acc);
      if (i + PD < 32) o[i % PD] = *(LAS v4u*)(ob + ((16 + i + PD) & 31) * 1024);
      if (i >= 16 && i + PD < 32) a[i % PD] = trd2(wb + (i + PD - 16) * 4096, 2048);
      SB();
    }
    PIN(acc);
    ST(S0 + 7);
    if (LY == 0) {
      f32x2 x[8];
#pragma unroll
      for (int i = 0; i < 8; ++i) x[i] = f32x2{acc[2 * i], acc[2 * i + 1]};
      const f32x2 s1 = ((x[0] + x[1]) + (x[2] + x[3])) + ((x[4] + x[5]) + (x[6] + x[7]));
      const f32x2 s2 = (x[0] * x[0] + x[1] * x[1] + x[2] * x[2] + x[3] * x[3]) + (x[4] * x[4] + x[5] * x[5] + x[6] * x[6] + x[7] * x[7]);
      float u1 = op(s1.x + s1.y), u2 = op(s2.x + s2.y);
      swap32(u1, u2);
      float t = u1 + u2, t2 = t;
      swap32(t, t2);
      const v4u tb = tag4(c.tag);
      __builtin_amdgcn_raw_buffer_store_b64(v2u{__builtin_amdgcn_perm(BC(u32, t), tb.x, 0x07060500u), __builtin_amdgcn_perm(BC(u32, t2), tb.y, 0x07060500u)}, r, spo, 0, SC1);
      SB();
      f32x2 y[8], xy[8];
      {
        const v4u y0 = *(LAS v4u*)(L + L_Y + ct * 2048 + lane * 16), y1 = *(LAS v4u*)(L + L_Y + ct * 2048 + 1024 + lane * 16);
#pragma unroll
        for (int i = 0; i < 8; ++i) { y[i] = unpk(i < 4 ? y0[i & 3] : y1[i & 3]); xy[i] = x[i] * y[i]; PIN(y[i]); PIN(xy[i]); }
      }
      nap<D_SP - 3>();
      u32 spins = 0;
#pragma nounroll
      for (;; ++spins) {
        FENCE();
        v2u sp[8];
#pragma unroll
        for (int i = 0; i < 8; ++i) sp[i] = __builtin_amdgcn_raw_buffer_load_b64(r, spi + i * 256, 0, SC1) ^ v2u{tb.x, tb.y};
        const u32 a = opu(opu(sp[0].x | sp[0].y | sp[1].x) | opu(sp[1].y | sp[2].x | sp[2].y) | opu(sp[3].x | sp[3].y | sp[4].x)) | opu(sp[4].y | sp[5].x | sp[5].y) |
                      opu(opu(sp[6].x | sp[6].y | sp[7].x) | sp[7].y);
        const f32x2 u = ((BC(f32x2, sp[0]) + BC(f32x2, sp[1])) + (BC(f32x2, sp[2]) + BC(f32x2, sp[3]))) + ((BC(f32x2, sp[4]) + BC(f32x2, sp[5])) + (BC(f32x2, sp[6]) + BC(f32x2, sp[7])));
        u1 = u.x; u2 = u.y;
        swap32(u1, u2);
        t = (u1 + u2) * (1.f / 512.f); t2 = t;
        swap32(t, t2);
        PIN(t); PIN(t2);
        if (all_lanes((a & 0xffu) == 0u) || spins > SPIN_CAP) break;
      }
      STV(37, spins);
      ST(S0 + 8);
      const float rs = __builtin_amdgcn_rsqf(mx3(__builtin_fmaf(-t, t, t2), 0.f, 0.f) + 1e-5f);
#pragma unroll
      for (int i = 0; i < 8; ++i) x[i] = __builtin_elementwise_fma(__builtin_elementwise_fma(y[i], f32x2{-t, -t}, xy[i]), f32x2{rs, rs}, x[i]);
      xo.publish(x, r, c.tag + 1u);
    } else {
#pragma unroll
      for (int i = 0; i < 4; ++i) *(LAS v2u*)ew4[i] = v2u{pk2(acc[4 * i], acc[4 * i + 1]), pk2(acc[4 * i + 2], acc[4 * i + 3])};
      FENCE();
      const v4u a = *(LAS v4u*)er, d = *(LAS v4u*)(er + 1024);
      const rsrc_t ro = __builtin_amdgcn_make_buffer_rsrc((void*)p.out, 0, 2 * MB, 0x00020000);
      __builtin_amdgcn_raw_buffer_store_b128(a, ro, oo, 0, OUT_AUX);
      __builtin_amdgcn_raw_buffer_store_b128(d, ro, oo, 16384, OUT_AUX);
    }
  }
  if (LY == 0) {
    BAR();
    ST(S0 + 9);

  }
}

DI void stage_kv(const Params& p, int stage, int cg, int w, int lane, int rot) {
  const int ty = w >> 1, g = ((lane >> 4) & 1) ^ (2 * ty);
  dma_rot<16, 8 * 3072>(srd(p.wqkv, 512 * 3072), (stage * 256 + (w & 1) * 128 + (lane >> 3)) * 3072 + (1 + ty) * 1024 + cg * 128 + (((lane & 7) ^ (2 * g)) * 16), R1A + w * 16384, rot);
}

template <bool KV>
DI void program(const Params& p, Ctx c, v4u a0, u32 a4, u32 a5 ST_PARAM) {
  LAS unsigned char* L = c.L;
  const rsrc_t r = c.r;
  u32 tag;
  const int b = c.b, w = c.w, lane = c.lane, t8 = (w & 3) * 64 + lane;
  const int x8 = b & 7, hi = b >> 3, rb = b & 31, cg = b >> 5, hf = cg & 1, cq = cg >> 1, pair = x8 * 32 + hi;
  const int G = lane >> 4, q = (lane >> 2) & 3, pp = lane & 3, h = lane >> 5, n = lane & 31;
  const v4u z4 = {0u, 0u, 0u, 0u}, s4 = {~0u, ~0u, ~0u, ~0u};
  v4u W[32];
  v2u csf = {0u, 0u};
  float cs0 = 0.f, cs1 = 0.f;
  v4u wph;
#define WPH_ISSUE() \
    u32 we[8]; { \
    const GAS u16* wp = (const GAS u16*)p.wphi + (KV ? 4 * cg + w : 4 * cg + (w & 2) + (n >> 4)) * 256 + (4 * h) * 16 + (n & 15); \
    _Pragma("unroll") for (int j = 0; j < 8; ++j) we[j] = wp[(8 * (j >> 2) + (j & 3)) * 16]; }
#define WPH_FINISH() { wph = v4u{we[0] | (we[1] << 16), we[2] | (we[3] << 16), we[4] | (we[5] << 16), we[6] | (we[7] << 16)}; PIN(wph); }
#define WARM(M, WV, D) { \
    if (((M) & 5) && (w & 3) == (WV) && (lane < 32 ? ((M) & 1) : ((M) & 4))) \
      D = *(const GAS u32*)(lane < 48 ? (const GAS unsigned char*)p.wqkv + (lane < 32 ? (2 * b + (lane >> 4)) * 3072 + 1024 + (lane & 15) * 128 : (2 * b + ((lane >> 3) & 1)) * 3072 + (lane & 7) * 128) \
                                      : (const GAS unsigned char*)p.wo + (b * 16 + lane - 48) * 128); \
    if (((M) & 2) && (w & 3) == ((WV) ^ 1)) D = *(const GAS u32*)((const GAS unsigned char*)p.q + (rb * 64 + hf * 32 + cq * 8 + (lane >> 3)) * 1024 + (lane & 7) * 128); \
    FENCE(); }
  const int yh = 2 * ((t8 >> 5) + 8 * ((t8 >> 3) & 3)) + ((t8 >> 2) & 1);
  const int ypos = B_YI + yh * 32768 + pair * 128 + (lane & 3) * 16;
  if (!KV) {
    const int ct = w - 4, ks0 = 8 * cq + 2 * ct;
    if (w == 4 && lane < 16) *(LAS u32*)(L + L_C0 + lane * 4) = 0u;
    if (w == 6 && hi == 0 && lane < 32) *(LAS v4u*)(L + L_KT + 512 + lane * 16) = z4;
    if (w == 5 && n < 30) *(LAS v4u*)(L + L_U + h * 4608 + 4096 + n * 16) = z4;
    nap<D_H>();
    v4u hv[2]; u32 hn[2];
#pragma unroll
    for (int cc = 0; cc < 2; ++cc) {
      hv[cc] = *(const GAS v4u*)(p.h + pair * 4096 + cc * 2048 + t8 * 8);
      hn[cc] = *(const GAS u32*)(p.h + pair * 4096 + cc * 2048 + min(t8 * 8 + 8, 2040));
    }
    WPH_ISSUE();
    const u32 dd = *(const GAS u32*)(p.dv + 2 * pair);
    u32 pe = 0;
    WARM(PE, 3, pe);
    BAR();
    ST(1);
#pragma unroll
    for (int cc = 0; cc < 2; ++cc) {
      LAS unsigned char* hb = L + L_H + cc * 8320 + t8 * 16 + 32;
      const v4u a = hv[cc];
      *(LAS v4u*)hb = a;
      *(LAS v4u*)(hb + 4160) = v4u{__builtin_amdgcn_alignbit(a.y, a.x, 16), __builtin_amdgcn_alignbit(a.z, a.y, 16), __builtin_amdgcn_alignbit(a.w, a.z, 16), __builtin_amdgcn_alignbit(hn[cc], a.w, 16)};
      if (t8 == 0) {
        *(LAS v4u*)(hb - 32) = z4; *(LAS v4u*)(hb - 16) = z4; *(LAS v4u*)(hb + 4160 - 32) = z4;
        *(LAS v4u*)(hb + 4160 - 16) = v4u{0u, 0u, 0u, a.x << 16};
      }
    }
    arrive(L_C0);
    WPH_FINISH();
    asm volatile("" ::"v"(dd));
    ST(2);
    if (PRIO) __builtin_amdgcn_s_setprio(2);
    until(L_C0, 8);
    tag = __builtin_amdgcn_readfirstlane(*(LAS u32*)(L + L_C0 - 528));
    c.tag = tag;
    const int ch = (w >> 1) & 1, rho = w & 1, i16 = lane & 15;
    LAS unsigned char* ab = L + L_H + ch * 8320 + ((i16 & 1) ? 0 : 4160) + 2 * (((i16 + 1) & ~1) + 8 * G) + 256 * rho;
#pragma unroll
    for (int i = 0; i < 16; ++i) {
      LAS u32* ap = (LAS u32*)(ab + 512 * (i & 7) + 64 * (i >> 3));
      W[i] = v4u{ap[0], ap[1], ap[2], ap[3]};
    }
    FENCE();
    nap<D_UT>();
    ST(3);
    const int usrc = B_UT + pair * 8192 + t8 * 16, urec = B_UR + ((t8 >> 3) * 8 + x8) * 1024 + (hi * 8 + (lane & 7)) * 4;
    v4u u0 = ld16(r, usrc), u1 = ld16(r, usrc + 4096);
    u32 ur_ = ld4(r, urec);
    FENCE();
    arrive(L_C0 + 24);
#pragma unroll
    for (int i = 16; i < 32; ++i) {
      LAS u32* ap = (LAS u32*)(ab + 512 * (i & 7) + 64 * (i >> 3));
      W[i] = v4u{ap[0], ap[1], ap[2], ap[3]};
    }
    const int oct = 4 * yh + (lane & 3);
    LAS unsigned char* uw = L + L_U + 4080 - 16 * t8;
    LAS unsigned char* bb = L + L_U + ch * 4608 + 4064 - 3584 - 32 * i16 + 16 * G + 256 * rho;
    LAS unsigned char* pb = L + L_P + ct * 8192 + G * 256 + ((i16 ^ (8 * (G >> 1))) * 16);
    LAS unsigned char* pb2 = L + L_P + ct * 8192 + G * 256 + ((i16 ^ (8 * (G >> 1)) ^ 4) * 16);
    LAS unsigned char* pr = L + L_P + (oct >> 5) * 1024 + (oct & 1) * 512 + ((((oct >> 1) & 15) ^ (8 * (oct & 1)) ^ ((oct >> 4) & 4)) * 16);
    LAS unsigned char* uo = L + L_U + 4080 - 16 * oct;
    int yp = ypos, yrp = B_YR + pair * 1024 + t8 * 4;
    PIN(uw); PIN(bb); PIN(pb); PIN(pb2); PIN(pr); PIN(uo); PIN(yp); PIN(yrp);
    FENCE();
    *(LAS v4u*)uw = u0;
    *(LAS v4u*)(uw + 4608) = u1;
    if (!all_lanes(opu(sum4(u0, tag)) + opu(sum4(u1, 0u)) == ur_)) {
      u32 spins = 0;
#pragma nounroll
      while (!all_lanes(sum4(u1, sum4(u0, tag)) == ur_) && spins <= SPIN_CAP) {
        ++spins;
        FENCE();
        u0 = ld16(r, usrc); u1 = ld16(r, usrc + 4096); ur_ = ld4(r, urec);
      }
      *(LAS v4u*)uw = u0;
      *(LAS v4u*)(uw + 4608) = u1;
      STV(35, spins);
    }
    if (TRIG == 40) arrive(L_C0 + 40);
    asm volatile("" ::"v"(pe));
    ST(4);
    arrive(L_C0 + 36);
    until(L_C0 + 36, 4);
    ST(5);
    f32x4 acc[8];
#pragma unroll
    for (int i = 0; i < 8; ++i) acc[i] = f32x4{0.f, 0.f, 0.f, 0.f};
    v4u B[8];
#pragma unroll
    for (int i = 0; i < 8; ++i) B[i] = *(LAS v4u*)(bb + 512 * (7 - i));
    SB();
    v4u ur[2];
    float y[2][8];
#pragma unroll
    for (int rr = 0; rr < 4; ++rr) {
#pragma unroll
      for (int m = 0; m < 8; ++m) {
#pragma unroll
        for (int nn = 0; nn + m < 8; ++nn) {
          acc[nn + m] = mfma16(W[8 * rr + nn], B[m], acc[nn + m]);
          if (rr == 3 && m >= 1 && nn == 0) *(LAS f32x4*)((((m - 1) & 2) ? pb2 : pb) + (m - 1) * 1024) = acc[m - 1];
          if (rr == 2 && m == 5 && (nn == 0 || nn == 1)) ur[nn] = *(LAS v4u*)(uo + nn * 4608);
          if (rr == 3 && m < 4 && nn >= 1 && nn <= 4) {
            const int e = 4 * m + nn - 1, i = e >> 3, k = e & 7;
            y[i][k] = ((k & 1) ? bflo(ur[i][3 - (k >> 1)]) : bfhi(ur[i][3 - (k >> 1)])) * (i ? bfhi(dd) : bflo(dd));
            PIN(y[i][k]);
          }
          SB();
        }
        if (rr < 3) B[m] = *(LAS v4u*)(bb + 512 * (7 - m) + 64 * (rr + 1));
        SB();
      }
    }
    *(LAS f32x4*)(pb2 + 7 * 1024) = acc[7];
    ST(6);
    arrive(L_C0 + 4);
    until(L_C0 + 4, 8);
    {
      f32x4 t[8];
#pragma unroll
      for (int i = 0; i < 8; ++i) t[i] = *(LAS f32x4*)(pr + (i >> 1) * 8192 + (i & 1) * 256);
      SB();
      v4u yo[2];
#pragma unroll
      for (int i = 0; i < 2; ++i) {
        const f32x4 a = (f32x4{y[i][0], y[i][1], y[i][2], y[i][3]} + t[4 * i]) + t[4 * i + 2], d = (f32x4{y[i][4], y[i][5], y[i][6], y[i][7]} + t[4 * i + 1]) + t[4 * i + 3];
        yo[i] = v4u{pk2(a.x, a.y), pk2(a.z, a.w), pk2(d.x, d.y), pk2(d.z, d.w)};
        st16(r, yp + 64 * i, yo[i]);
        SB();
      }
      __builtin_amdgcn_raw_buffer_store_b32(sum4(yo[1], opu(sum4(yo[0], tag))), r, yrp, 0, SC1);
    }
    ST(7);
    until(L_C0 + 28, 4);
    f32x2 x[8];
    {
      LAS unsigned char* qb = L + L_QR + n * 1024;
      float s1 = 0.f, s2 = 0.f;
#pragma unroll
      for (int i = 0; i < 8; ++i) acc_stats(*(LAS v4u*)(qb + ((16 * ct + ((8 * h + i) ^ (n & 15))) * 16)), s1, s2);
      *(LAS f32x2*)(L + L_S0 + ct * 512 + n * 16 + h * 8) = f32x2{s1, s2};
#pragma unroll
      for (int i = 0; i < 4; ++i) {
        const v2u t = *(LAS v2u*)(qb + (((2 * ks0 + i) ^ (n & 15)) * 16) + h * 8);
        x[2 * i] = unpk(t.x); x[2 * i + 1] = unpk(t.y);
      }
    }
    arrive(L_C0 + 32);
    ST(29);
    nap<D_YI>();
    ST(8);
    const int ysrc = B_YI + (rb * 2 + hf) * 32768 + ks0 * 1024 + lane * 16;
    const int yrec = B_YR + (8 * ks0 + (lane >> 2)) * 1024 + ((rb & 7) * 32 + (rb >> 3) * 8 + hf * 4 + (lane & 3)) * 4;
    dma_run<true, 2, 1024>(srd(p.ws, WS_BYTES), ysrc, L_H + ct * 2048);
    u32 yr = ld4(r, yrec);
    FENCE();
    until(L_C0 + 32, 4);
    float m, rs;
    row_moments(L + L_S0, lane, m, rs);
    f32x2 nx[8];
#pragma unroll
    for (int i = 0; i < 8; ++i) { nx[i] = (x[i] + m) * rs; PIN(x[i]); PIN(nx[i]); }
    XOut xo;
    xo.prepare(rb, hf, ks0, lane);
    v4u y0, y1;
    u32 spins = 0;
#pragma nounroll
    for (;; ++spins) {
      DRAIN();
      LAS unsigned char* yb = L + L_H + ct * 2048 + (4 * h + q) * 64 + 32 * (G & 1) + 8 * pp;
      LAS unsigned char* yc = L + L_H + ct * 2048 + (lane >> 2) * 128 + (lane & 3) * 16;
      const v4u c0 = *(LAS v4u*)yc, c1 = *(LAS v4u*)(yc + 64);
      y0 = trd2(yb, 512); y1 = trd2(yb + 1024, 512);
      if (all_lanes(opu(sum4(c0, tag)) + opu(sum4(c1, 0u)) == yr) || spins > SPIN_CAP) break;
      asm volatile("s_waitcnt lgkmcnt(0)" ::: "memory");
      nap<D_RETRY>();
      dma_run<true, 2, 1024>(srd(p.ws, WS_BYTES), ysrc, L_H + ct * 2048);
      yr = ld4(r, yrec);
    }
    STV(36, spins);
    ST(9);
#pragma unroll
    for (int i = 0; i < 8; ++i) x[i] = __builtin_elementwise_fma(nx[i], unpk(i < 4 ? y0[i & 3] : y1[i & 3]), x[i]);
    xo.publish(x, r, tag);
    *(LAS v4u*)(L + L_Y + ct * 2048 + lane * 16) = y0; *(LAS v4u*)(L + L_Y + ct * 2048 + 1024 + lane * 16) = y1;
    if (w == 4 && lane < 16) *(LAS u32*)(L + L_C1 + lane * 4) = 0u;
    if (PRIO) __builtin_amdgcn_s_setprio(0);
  } else {
    tag = *(const __attribute__((address_space(4))) u32*)(u64)p.ws;
    SB();
    {
      u32 keep;
      asm volatile("s_mov_b32 %0, m0\n\ts_mov_b32 m0, %7\n\ts_nop 2\n\tglobal_load_lds_dwordx4 %1, %5\n\ts_add_u32 m0, m0, 0x2000\n\ts_nop 0\n\t"
                   "global_load_lds_dwordx4 %2, %6\n\ts_add_u32 m0, m0, 0x400\n\ts_nop 0\n\tglobal_load_lds_dwordx4 %4, %6\n\t"
                   "s_sub_u32 m0, m0, 0x2000\n\ts_nop 0\n\tglobal_load_lds_dwordx4 %3, %5\n\ts_mov_b32 m0, %0"
                   : "=&s"(keep) : "v"(a0.x), "v"(a0.y), "v"(a0.z), "v"(a0.w), "s"(p.k), "s"(p.v), "s"(a5) : "memory", "scc");
      static_assert(L_VT == L_KT + 1024 + 0x2000, "tile placement");
    }
    SB();
    if (w < 2) dma_run<false, 1, 0>(w == 1 ? srd(p.shk, 4096) : srd(p.k, 2 * MB), a4, w == 0 ? L_KT : L_C0 - 1024);
    const int oc = lane & 7, pl = 8 * w + (lane >> 3), ck = pl >> 2;
    int upos = B_UT + (x8 * 32 + pl) * 8192 + (8 * hi + oc) * 16, upos1 = upos + 4096, urec = B_UR + b * 1024 + t8 * 4;
    LAS unsigned char* skp = L + L_C0 - 512 + pl * 16;
    LAS unsigned char* ka = L + L_KT + oc * 1024 + ((ck ^ oc) * 16) + (pl & 3) * 4;
    LAS unsigned char* kb = L + L_KT + (oc + 1) * 1024 + ((ck ^ ((oc + 1) & 7)) * 16) + (pl & 3) * 4;
    LAS unsigned char* va = L + L_VT + oc * 1024 + ((ck ^ oc) * 16) + (pl & 3) * 4;
    PIN(upos); PIN(upos1); PIN(urec); PIN(skp); PIN(ka); PIN(kb); PIN(va);
    asm volatile("s_add_u32 %0, %0, 1\n\ts_waitcnt vmcnt(0)\n\ts_barrier" : "+s"(tag)::"memory", "scc");
    c.tag = tag;
    ST(1);
    {
      const v4u sk = *(LAS v4u*)skp;
      u32 kk[11], vv[8];
#pragma unroll
      for (int i = 0; i < 3; ++i) kk[i] = *(LAS u32*)(ka + (i + 5) * 128);
#pragma unroll
      for (int i = 0; i < 8; ++i) { kk[3 + i] = *(LAS u32*)(kb + i * 128); vv[i] = *(LAS u32*)(va + i * 128); }
      SB();
      const f32x2 w0 = {bflo(sk.x), bflo(sk.z)}, w1 = {bfhi(sk.x), bfhi(sk.z)}, w2 = {bflo(sk.y), bflo(sk.w)}, w3 = {bfhi(sk.y), bfhi(sk.w)};
      f32x2 kf[11], u[8];
#pragma unroll
      for (int i = 0; i < 11; ++i) kf[i] = f32x2{bflo(kk[i]), bfhi(kk[i])};
#pragma unroll
      for (int i = 0; i < 8; ++i) u[i] = (kf[i] * w3 + kf[i + 1] * w2 + kf[i + 2] * w1 + kf[i + 3] * w0) * f32x2{bflo(vv[i]), bfhi(vv[i])};
      const v4u ua = {pk2(u[7].x, u[6].x), pk2(u[5].x, u[4].x), pk2(u[3].x, u[2].x), pk2(u[1].x, u[0].x)};
      const v4u ub = {pk2(u[7].y, u[6].y), pk2(u[5].y, u[4].y), pk2(u[3].y, u[2].y), pk2(u[1].y, u[0].y)};
      st16(r, upos, ua); st16(r, upos1, ub);
      SB();
      __builtin_amdgcn_raw_buffer_store_b32(opu(sum4(ua, tag)) + opu(sum4(ub, 0u)), r, urec, 0, SC1);
      SB();
    }
    if (w == 0 && lane == 0) *(LAS u32*)(L + L_C0 - 528) = tag;
    arrive(L_C0);
    ST(2);
    nap<D_CL>();
    {
      const bool kvr = w < 2, on = lane < 8 && (kvr || (b & 7) == 0);
      if (on) st16(r, (kvr ? B_KVR + w * 32768 + b * 128 : B_ZR + (w - 2) * 4096 + (b >> 3) * 128) + lane * 16, s4);
    }
    ST(3);
    until<1>(L_C0 + TRIG, 4);
    ST(4);
    DRAIN();
    arrive(L_C0 + 4);
    u32 pf = 0, pg = 0;
    WARM(PT, 0, pf);
#define Q_ROWS() _Pragma("unroll") for (int i = 0; i < 8; ++i) \
      dma_run<false, 1, 0>(srd(p.q, 2 * MB), (rb * 64 + hf * 32 + w * 8 + i) * 1024 + ((lane ^ ((w * 8 + i) & 15)) * 16), L_QR + (w * 8 + i) * 1024);
    if (!QL) Q_ROWS();
    const int rot = ROT == 0 ? 0 : (ROT == 1 || ((((u32)(u64)p.wqkv >> 7) + cg) & 7) == 3) ? 4 * (rb >> 3) : 0;
    stage_kv(p, 0, cg, w, lane, rot);
    if (!QL) { VMW(16); arrive(L_C0 + 28); }
    {
      const rsrc_t rw = __builtin_amdgcn_make_buffer_rsrc((void*)p.wqkv, 0, 512 * 3072, 0x00020000);
      const int src = (256 + (w & 1) * 128 + (lane >> 3)) * 3072 + (1 + (w >> 1)) * 1024 + cg * 128 + (((lane & 7) ^ (2 * ((G & 1) ^ (w & 2)))) * 16);
#pragma unroll
      for (int i = 0; i < 16; ++i) W[16 + i] = __builtin_amdgcn_raw_buffer_load_b128(rw, src, ((i + rot) & 15) * 8 * 3072, 0);
      FENCE();
    }
    VMW(16);
    arrive(L_C0 + 12);
    if (QL) { Q_ROWS(); DRAIN(); arrive(L_C0 + 28); }
    WARM(7 & ~(PE | PT), 2, pg);
    WPH_ISSUE();
    FENCE();
    until<1>(L_C0 + 12, 4);
    ST(5);
    if (HOLD == 1) until<2>(L_C0 + 4, 8);
    if (HOLD == 2) until<2>(L_C0 + 32, 4);
    LAS unsigned char* wb = L + R1A + (G & 1) * 32768 + (4 * h + q) * 128 + ((w ^ (q >> 1) ^ (2 * (G & 1))) * 32) + 8 * pp;
#pragma unroll
    for (int i = 0; i < 16; ++i) W[i] = trd2(wb + i * 2048, 1024);
    arrive(L_C0 + 8);
    until<1>(L_C0 + 8, 4);
    ST(6);
#pragma unroll
    for (int i = 0; i < 16; ++i) *(LAS v4u*)(L + R1A + (w * 16 + (((i & 12) + rot) & 15)) * 1024 + lane * 16 + (i & 3) * 1024) = W[16 + i];
    WPH_FINISH();
    asm volatile("" ::"v"(pf), "v"(pg));
    ST(7);
    until<1>(L_C0 + 32, 4);
    cs_acc<0, 16>(W, cs0, cs1);
    PIN(cs0); PIN(cs1);
    ST(9);
  }
  BAR();
  ST(28);
  if (KV) {
    LAS unsigned char* wb = L + R1A + (G & 1) * 32768 + (4 * h + q) * 128 + ((w ^ (q >> 1) ^ (2 * (G & 1))) * 32) + 8 * pp;
#pragma unroll
    for (int i = 0; i < 16; ++i) W[16 + i] = trd2(wb + i * 2048, 1024);
    cs_acc<16, 32>(W, cs0, cs1);
    csf = cs_pack(cs0 + cs1);
  }
  layer<0, KV>(p, c, W, csf, wph ST_ARG);
  if (KV && b == 0 && w == 0 && lane == 0) __builtin_amdgcn_raw_buffer_store_b32(tag + 1u, r, WS_EPOCH, 0, SC1);
  layer<1, KV>(p, c, W, csf, wph ST_ARG);
}

__global__ void __launch_bounds__(512, 2) fused_kernel(Params p) {
  extern __shared__ __attribute__((aligned(16))) unsigned char smem[];
  LAS unsigned char* L = (LAS unsigned char*)smem;
  const int tid = threadIdx.x, lane = tid & 63, w = __builtin_amdgcn_readfirstlane(tid >> 6), b = blockIdx.x;
  const rsrc_t r = __builtin_amdgcn_make_buffer_rsrc((void*)p.ws, 0, WS_BYTES, 0x00020000);
  v4u a0;
  u32 a4, a5 = L_KT + 1024 + 2 * w * 1024;
  asm volatile("" : "+s"(a5));
  {
    const u32 x8 = b & 7, hi = b >> 3, o0 = (64 * hi + 16 * w + (lane >> 3)) * 1024 + x8 * 128, c0 = lane & 7;
    a0 = v4u{o0 + (c0 ^ (2 * w + 1)) * 16, o0 + (c0 ^ (2 * w)) * 16, o0 + 8192 + (c0 ^ ((2 * w + 2) & 7)) * 16, o0 + 8192 + (c0 ^ (2 * w + 1)) * 16};
    u32 ah = o0 - 8192 + c0 * 16, as = x8 * 512 + (lane - 32) * 16;
    PIN(ah); PIN(as);
    a4 = (lane < 32 || (w == 0 && hi == 0)) ? 0x40000000u : (w == 0 ? ah : as);
    PIN(a0); PIN(a4);
  }
  {
    u64 pc;
    int len;
    asm volatile("s_getpc_b64 %0\n.Lfk_begin:\n\ts_mov_b32 %1, fk_end_marker-.Lfk_begin" : "=s"(pc), "=s"(len));
    if (w == 4 && lane < 16) dma_run<false, 1, 0>(srd((const void*)pc, max(len, 0)), (lane * 32 + (b >> 3)) * 128, L_S0);
  }
  asm volatile("" ::"s"(p.q), "s"(p.k), "s"(p.v), "s"(p.shk), "s"(p.h), "s"(p.ws));
  const Ctx c = {L, r, 0u, b, w, lane};
  if (w >= 4) program<false>(p, c, a0, a4, a5 ST_ARG);
  else program<true>(p, c, a0, a4, a5 ST_ARG);
  ST(30);
  ST_FLUSH();
}

extern "C" __device__ __attribute__((used, noinline, visibility("hidden"))) void fk_end_marker() {}

extern "C" void kernel_launch(void* const* d_in, const int* in_sizes, int n_in, void* d_out, int out_size, void* d_ws, size_t ws_size,
                              hipStream_t stream) {
  Params p;
  p.q = (const u16*)d_in[0]; p.k = (const u16*)d_in[1]; p.v = (const u16*)d_in[2]; p.shk = (const u16*)d_in[3];
  p.h = (const u16*)d_in[4]; p.dv = (const u16*)d_in[5]; p.wqkv = (const u16*)d_in[7]; p.wphi = (const u16*)d_in[8];
  p.wo = (const u16*)d_in[9]; p.out = (u16*)d_out; p.ws = (unsigned char*)d_ws + (ws_size - WS_BYTES);
  (void)hipFuncSetAttribute((const void*)fused_kernel, hipFuncAttributeMaxDynamicSharedMemorySize, LDS_BYTES);
  hipLaunchKernelGGL(fused_kernel, dim3(256), dim3(512), LDS_BYTES, stream, p);
}
```
